```python
import torch

def _softmax(x, axis=-1):
    return torch.softmax(x, dim=axis)
def _gelu(x):
    return torch.nn.functional.gelu(x, approximate="tanh")
def _silu(x):
    return torch.nn.functional.silu(x)
def _sigmoid(x):
    return torch.sigmoid(x)
def _layernorm(x, axis=-1, eps=1e-5):
    m = x.mean(dim=axis, keepdim=True)
    v = x.var(dim=axis, keepdim=True, unbiased=False)
    return (x - m) / torch.sqrt(v + eps)
def _scatter_add(tgt, idx, src, axis=0):
    return tgt.clone().index_add_(axis, idx, src)
def _segment_sum(src, seg, n):
    out = torch.zeros((n,) + src.shape[1:], dtype=src.dtype)
    return out.index_add_(0, seg, src)
def _pad(x, pw):
    flat = tuple(v for pair in reversed(pw) for v in pair)
    return torch.nn.functional.pad(x, flat)

kernel_name = 'concat_heads__mha_block_cached__gqa_block_cached__near_ridge'

def setup_inputs():

    _w = (lambda a: (lambda t: t.to(torch.bfloat16) if t.is_floating_point() else t)(torch.as_tensor(a)))
    def _A_setup():
        B, Sq, D = 16, 144, 576
        H, Sk, Dh = 32, 512, 128
        return {
            "x": _w((lambda *s: torch.randn(*s, dtype=torch.bfloat16) * 0.8)(B, Sq, D)),
            "k_cache": _w((lambda *s: torch.randn(*s, dtype=torch.bfloat16) * 0.8)(B, H, Sk, Dh)),
            "v_cache": _w((lambda *s: torch.randn(*s, dtype=torch.bfloat16) * 0.05)(B, H, Sk, Dh)),
            "wq": _w((lambda *s: torch.randn(*s, dtype=torch.bfloat16) * 0.8)(D, H * Dh) * (D ** -0.5)),
            "wo": _w((lambda *s: torch.randn(*s, dtype=torch.bfloat16) * 0.05)(H * Dh, D) * ((H * Dh) ** -0.5)),
        }
    _A_in = _A_setup()

    def _B_setup():
        B, Sq, D = 16, 144, 576
        Hq, Hkv, Sk, Dh = 32, 32, 512, 128
        assert Hq % Hkv == 0, (Hq, Hkv)
        return {
            "x": _w((lambda *s: torch.randn(*s, dtype=torch.bfloat16) * 0.8)(B, Sq, D)),
            "k_cache": _w((lambda *s: torch.randn(*s, dtype=torch.bfloat16) * 0.8)(B, Hkv, Sk, Dh)),
            "v_cache": _w((lambda *s: torch.randn(*s, dtype=torch.bfloat16) * 0.05)(B, Hkv, Sk, Dh)),
            "wq": _w((lambda *s: torch.randn(*s, dtype=torch.bfloat16) * 0.8)(D, Hq * Dh) * (D ** -0.5)),
            "wo": _w((lambda *s: torch.randn(*s, dtype=torch.bfloat16) * 0.05)(Hq * Dh, D) * ((Hq * Dh) ** -0.5)),
        }
    _B_in = _B_setup()

    return {
        "A_x": _A_in["x"],
        "A_k_cache": _A_in["k_cache"],
        "A_v_cache": _A_in["v_cache"],
        "A_wq": _A_in["wq"],
        "A_wo": _A_in["wo"],
        "B_x": _B_in["x"],
        "B_k_cache": _B_in["k_cache"],
        "B_v_cache": _B_in["v_cache"],
        "B_wq": _B_in["wq"],
        "B_wo": _B_in["wo"],
    }

def reference(A_x, A_k_cache, A_v_cache, A_wq, A_wo, B_x, B_k_cache, B_v_cache, B_wq, B_wo):
    def _A_ref(x, k_cache, v_cache, wq, wo):
        B, Sq, D = x.shape
        H, Dh = 32, 128
        q = (lambda x, *d: x.permute(*d))((x @ wq).reshape(B, Sq, H, Dh), 0, 2, 1, 3)
        scores = torch.einsum("bhqd,bhkd->bhqk", q, k_cache) * (Dh ** -0.5)
        p = _softmax(scores, axis=-1)
        o = torch.einsum("bhqk,bhkd->bhqd", p, v_cache)
        return (lambda x, *d: x.permute(*d))(o, 0, 2, 1, 3).reshape(B, Sq, H * Dh) @ wo

    def _B_ref(x, k_cache, v_cache, wq, wo):
        B, Sq, D = x.shape
        Hq, Dh = 32, 128
        Hkv = k_cache.shape[1]
        rep = Hq // Hkv
        q = (lambda x, *d: x.permute(*d))((x @ wq).reshape(B, Sq, Hq, Dh), 0, 2, 1, 3)
        k = (lambda x, n, axis: torch.repeat_interleave(x, n, dim=axis))(k_cache, rep, axis=1)
        v = (lambda x, n, axis: torch.repeat_interleave(x, n, dim=axis))(v_cache, rep, axis=1)
        scores = torch.einsum("bhqd,bhkd->bhqk", q, k) * (Dh ** -0.5)
        p = _softmax(scores, axis=-1)
        o = torch.einsum("bhqk,bhkd->bhqd", p, v)
        return (lambda x, *d: x.permute(*d))(o, 0, 2, 1, 3).reshape(B, Sq, Hq * Dh) @ wo

    a = _A_ref(A_x, A_k_cache, A_v_cache, A_wq, A_wo)
    b = _B_ref(B_x, B_k_cache, B_v_cache, B_wq, B_wo)
    return torch.concatenate([a, b], axis=-1)
```

```cpp
#include <hip/hip_runtime.h>
#include <hip/hip_bf16.h>
typedef __hip_bfloat16 bf16;
constexpr int B=16,SQ=144,D=576,H=32,SK=512,DH=128,HD=H*DH;
__device__ inline float b2f(bf16 x){return __bfloat162float(x);}
__global__ void gemm_naive(const bf16* A,const bf16* W,bf16* C,int M,int N,int K,int ldc,int coff){
  __shared__ float As[16][17], Ws[16][17];
  int tx=threadIdx.x, ty=threadIdx.y;
  int row=blockIdx.y*16+ty, col=blockIdx.x*16+tx;
  float acc=0;
  for(int k0=0;k0<K;k0+=16){
    As[ty][tx]=b2f(A[(size_t)row*K+k0+tx]);
    Ws[ty][tx]=b2f(W[(size_t)(k0+ty)*N+col]);
    __syncthreads();
    for(int k=0;k<16;++k) acc+=As[ty][k]*Ws[k][tx];
    __syncthreads();
  }
  C[(size_t)row*ldc+coff+col]=__float2bfloat16(acc);
}
__global__ void attn_naive(const bf16* Q,const bf16* K,const bf16* V,bf16* O){
  int q=blockIdx.x, h=blockIdx.y, b=blockIdx.z, t=threadIdx.x;
  __shared__ float qs[DH], p[SK], red[128];
  qs[t]=b2f(Q[((size_t)b*SQ+q)*HD+h*DH+t]);
  __syncthreads();
  const bf16* Kb=K+((size_t)b*H+h)*SK*DH; const bf16* Vb=V+((size_t)b*H+h)*SK*DH;
  float m=-1e30f;
  for(int k=t;k<SK;k+=128){ float s=0; for(int d=0;d<DH;++d) s+=qs[d]*b2f(Kb[(size_t)k*DH+d]); s*=0.08838834764831845f; p[k]=s; m=fmaxf(m,s);}
  red[t]=m; __syncthreads();
  for(int s=64;s>0;s>>=1){ if(t<s) red[t]=fmaxf(red[t],red[t+s]); __syncthreads(); }
  m=red[0]; __syncthreads();
  float l=0; for(int k=t;k<SK;k+=128){ float e=__expf(p[k]-m); p[k]=e; l+=e; }
  red[t]=l; __syncthreads();
  for(int s=64;s>0;s>>=1){ if(t<s) red[t]+=red[t+s]; __syncthreads(); }
  l=red[0];
  float o=0; for(int k=0;k<SK;++k) o+=p[k]*b2f(Vb[(size_t)k*DH+t]);
  O[((size_t)b*SQ+q)*HD+h*DH+t]=__float2bfloat16(o/l);
}
extern "C" void kernel_launch(void* const* d_in, const int* in_sizes, int n_in, void* d_out, int out_size, void* d_ws, size_t ws_size, hipStream_t stream) {
  bf16* Qw=(bf16*)d_ws; bf16* Ow=Qw+(size_t)B*SQ*HD;
  for(int br=0;br<2;++br){
    const bf16* x=(const bf16*)d_in[br*5+0]; const bf16* k=(const bf16*)d_in[br*5+1]; const bf16* v=(const bf16*)d_in[br*5+2];
    const bf16* wq=(const bf16*)d_in[br*5+3]; const bf16* wo=(const bf16*)d_in[br*5+4];
    gemm_naive<<<dim3(HD/16,B*SQ/16),dim3(16,16),0,stream>>>(x,wq,Qw,B*SQ,HD,D,HD,0);
    attn_naive<<<dim3(SQ,H,B),128,0,stream>>>(Qw,k,v,Ow);
    gemm_naive<<<dim3(D/16,B*SQ/16),dim3(16,16),0,stream>>>(Ow,wo,(bf16*)d_out,B*SQ,D,HD,2*D,br*D);
  }
}
```

```cpp
#define WSEND
#include <hip/hip_runtime.h>
#include <stdint.h>

typedef short    bf16x8 __attribute__((ext_vector_type(8)));
typedef short    s16x4  __attribute__((ext_vector_type(4)));
typedef float    f32x4  __attribute__((ext_vector_type(4)));
typedef float    f32x2  __attribute__((ext_vector_type(2)));
typedef __bf16   bf16x2 __attribute__((ext_vector_type(2)));
typedef unsigned u32x4  __attribute__((ext_vector_type(4)));
typedef unsigned u32x2  __attribute__((ext_vector_type(2)));
typedef unsigned short u16;
#define WARMPOL ""
#define NOWARM
#define OST_POL "sc1"
#define DI __device__ __forceinline__
#define LAS __attribute__((address_space(3)))
#define MFMA16(a, b, c) __builtin_amdgcn_mfma_f32_16x16x32_bf16(a, b, c, 0, 0, 0)
#define SBAR() __builtin_amdgcn_sched_barrier(0)
#define PIN(x) asm volatile("" : "+v"(x))
#define MFMA16_V(acc, a, b) asm volatile("v_mfma_f32_16x16x32_bf16 %0, %1, %2, %0" : "+v"(acc) : "v"(a), "v"(b))
#define MFMA16_C(d, a, b, cc) asm volatile("v_mfma_f32_16x16x32_bf16 %0, %1, %2, %3" : "=&v"(d) : "v"(a), "v"(b), "v"(cc))

constexpr int NB = 16, SQ = 144, DM = 576, NH = 32, SK = 512, DH = 128, HD = NH * DH;
constexpr int ITEM_B = 9 * 4 * 1024;
constexpr size_t WS_O = 0, WS_WT = (size_t)1024 * ITEM_B;
constexpr float QSCALE = 0.08838834764831845f * 1.4426950408889634f;

DI unsigned rfl(unsigned x) { return __builtin_amdgcn_readfirstlane(x); }
DI unsigned lane_id() { unsigned l; asm volatile("v_mbcnt_lo_u32_b32 %0, -1, 0\n\tv_mbcnt_hi_u32_b32 %0, -1, %0" : "=v"(l)); return l; }
DI unsigned cvtpk(float lo, float hi) { f32x2 v = {lo, hi}; bf16x2 b = __builtin_convertvector(v, bf16x2); return __builtin_bit_cast(unsigned, b); }
DI float fadd_s(float a, float b) { float r; asm("v_add_f32_e32 %0, %1, %2" : "=v"(r) : "v"(a), "v"(b)); return r; }
DI bf16x8 lds128(unsigned a) { return *(const LAS bf16x8*)a; }
DI s16x4 ldstr(unsigned a) { return __builtin_bit_cast(s16x4, __builtin_amdgcn_ds_read_tr16_b64_v4i16((LAS s16x4*)a)); }
DI bf16x8 cat8(s16x4 lo, s16x4 hi) { return __builtin_shufflevector(lo, hi, 0, 1, 2, 3, 4, 5, 6, 7); }
DI void dma16(unsigned m0v, unsigned voff, __amdgpu_buffer_rsrc_t srd, unsigned soff) {
  asm volatile("s_mov_b32 m0, %0\n\ts_nop 0\n\tbuffer_load_dwordx4 %1, %2, %3 offen lds" :: "s"(m0v), "v"(voff), "s"(srd), "s"(soff) : "memory");
}
DI void dma16nt(unsigned m0v, unsigned voff, __amdgpu_buffer_rsrc_t srd, unsigned soff) {
  asm volatile("s_mov_b32 m0, %0\n\ts_nop 0\n\tbuffer_load_dwordx4 %1, %2, %3 offen nt lds" :: "s"(m0v), "v"(voff), "s"(srd), "s"(soff) : "memory");
}
DI void dma4(unsigned m0v, unsigned voff, __amdgpu_buffer_rsrc_t srd) {
  asm volatile("s_mov_b32 m0, %0\n\ts_nop 0\n\tbuffer_load_dword %1, %2, 0 offen " WARMPOL " lds" :: "s"(m0v), "v"(voff), "s"(srd) : "memory");
}
#define WAITV(n) asm volatile("s_waitcnt vmcnt(" #n ")" ::: "memory")
#define BAR() asm volatile("s_waitcnt lgkmcnt(0)\n\ts_barrier" ::: "memory")

constexpr unsigned KRING = 0, VRING = 32768;
constexpr unsigned WRING = 65536, XRING = WRING + 3 * 8192;
constexpr unsigned XSLOT = 18432, QX = XRING + 2 * XSLOT, JUNK = QX, LDS_BYTES = QX + ITEM_B;
static_assert(LDS_BYTES <= 163840, "LDS");

#define STAMP(k)
#define STAMPW(k)

DI float red_g_max(float m) {
  auto r = __builtin_amdgcn_permlane32_swap(__float_as_uint(m), __float_as_uint(m), false, false);
  m = fmaxf(__uint_as_float(r[0]), __uint_as_float(r[1]));
  auto s = __builtin_amdgcn_permlane16_swap(__float_as_uint(m), __float_as_uint(m), false, false);
  return fmaxf(__uint_as_float(s[0]), __uint_as_float(s[1]));
}
DI float red_g_sum(float m) {
  auto r = __builtin_amdgcn_permlane32_swap(__float_as_uint(m), __float_as_uint(m), false, false);
  m = __uint_as_float(r[0]) + __uint_as_float(r[1]);
  auto s = __builtin_amdgcn_permlane16_swap(__float_as_uint(m), __float_as_uint(m), false, false);
  return __uint_as_float(s[0]) + __uint_as_float(s[1]);
}

template <int NQ> struct AttC {
  f32x4 O[NQ][8]; bf16x8 Q[NQ][4]; f32x4 S[2][NQ][2]; f32x4 negm[NQ]; float l[NQ];
  unsigned kb, vb0, vb1;
};
template <int NQ, int X, int F> DI void sm_fill(AttC<NQ>& c, u32x4 (&Pw)[NQ]) {
  constexpr int qt = F / 12, r = F % 12, kt = r / 6, u = r % 6;
  if constexpr (u < 4) { c.S[X][qt][kt][u] = __builtin_amdgcn_exp2f(c.S[X][qt][kt][u]); }
  else { Pw[qt][2 * kt + (u - 4)] = cvtpk(c.S[X][qt][kt][2 * (u - 4)], c.S[X][qt][kt][2 * (u - 4) + 1]); }
}
template <int NQ, int X, int LO, int HI> DI void sm_fills(AttC<NQ>& c, u32x4 (&Pw)[NQ]) {
  if constexpr (LO < HI) {
    sm_fill<NQ, X, LO>(c, Pw);
    if constexpr (LO + 1 == HI || (LO + 1) % 6 == 0) { PIN(c.S[X][LO / 12][(LO % 12) / 6]); if constexpr (LO % 6 >= 4) PIN(Pw[LO / 12]); }
    sm_fills<NQ, X, LO + 1, HI>(c, Pw);
  }
}
template <int NQ, int P, bool QK> struct Step {
  static constexpr int X = P & 1;
  static constexpr unsigned ks_ = 8192 * ((P + 1) & 3), vs_ = 8192 * P;
  static constexpr int NF = 12 * NQ, PRE = QK ? 4 : NF;
  static constexpr int fl(int k) { return PRE + ((NF - PRE) * k) / 8; }
  bf16x8 fr[4]; u32x4 Pw[NQ];
  template <int R> DI void rd(AttC<NQ>& c) {
    if constexpr (R < 8) fr[R & 3] = lds128(c.kb + ks_ + 4096 * (R & 1) + 512 * (R >> 1));
    else if constexpr (R < 16) {
      constexpr int dt = R - 8;
      const unsigned vb = ((dt & 1) ? c.vb1 : c.vb0) + vs_ + 512 * (dt >> 1);
      fr[R & 3] = cat8(ldstr(vb), ldstr(vb + 4096));
    }
  }
  template <int K> DI void gx(AttC<NQ>& c) {
    constexpr int s = K >> 1, kt = K & 1, lo = fl(K), hi = fl(K + 1), mid = NQ == 2 ? (lo + hi + 1) / 2 : hi;
    if constexpr (s == 0) MFMA16_C(c.S[X ^ 1][0][kt], fr[K & 3], c.Q[0][s], c.negm[0]); else MFMA16_V(c.S[X ^ 1][0][kt], fr[K & 3], c.Q[0][s]);
    sm_fills<NQ, X, lo, mid>(c, Pw);
    SBAR();
    if constexpr (NQ == 2) {
      if constexpr (s == 0) MFMA16_C(c.S[X ^ 1][1][kt], fr[K & 3], c.Q[1][s], c.negm[1]); else MFMA16_V(c.S[X ^ 1][1][kt], fr[K & 3], c.Q[1][s]);
      sm_fills<NQ, X, mid, hi>(c, Pw);
      SBAR();
    }
    rd<K + 4>(c);
    SBAR();
  }
  template <int Dt> DI void gy(AttC<NQ>& c) {
    MFMA16_V(c.O[0][Dt], fr[Dt & 3], Pw[0]);
#pragma unroll
    for (int qt = 0; qt < NQ; ++qt) { c.l[qt] = fadd_s(c.l[qt], c.S[X][qt][Dt >> 2][Dt & 3]); PIN(c.l[qt]); }
    SBAR();
    if constexpr (NQ == 2) { MFMA16_V(c.O[1][Dt], fr[Dt & 3], Pw[1]); SBAR(); }
    rd<Dt + 12>(c);
    SBAR();
  }
  DI void run(AttC<NQ>& c) {
    if constexpr (QK) {
      rd<0>(c); rd<1>(c); rd<2>(c); rd<3>(c); SBAR();
      sm_fills<NQ, X, 0, PRE>(c, Pw); SBAR();
      gx<0>(c); gx<1>(c); gx<2>(c); gx<3>(c); gx<4>(c); gx<5>(c); gx<6>(c); gx<7>(c);
    } else {
      rd<8>(c); rd<9>(c); rd<10>(c); rd<11>(c); SBAR();
      sm_fills<NQ, X, 0, PRE>(c, Pw); SBAR();
    }
    asm volatile("s_nop 1");
    gy<0>(c); gy<1>(c); gy<2>(c); gy<3>(c); gy<4>(c); gy<5>(c); gy<6>(c); gy<7>(c);
  }
};

template <int NQ, int P, bool QK> DI void att_step(AttC<NQ>& c) { BAR(); SBAR(); Step<NQ, P, QK> s; s.run(c); SBAR(); }

template <int NQ> DI void att_prologue(AttC<NQ>& c, unsigned qa) {
#pragma unroll
  for (int qt = 0; qt < NQ; ++qt)
#pragma unroll
    for (int s = 0; s < 4; ++s) c.Q[qt][s] = lds128(qa + qt * 4096 + s * 1024);
#pragma unroll
  for (int s = 0; s < 4; ++s)
#pragma unroll
    for (int kt = 0; kt < 2; ++kt) {
      const bf16x8 kf = lds128(c.kb + 4096 * kt + 512 * s);
#pragma unroll
      for (int qt = 0; qt < NQ; ++qt) c.S[0][qt][kt] = MFMA16(kf, c.Q[qt][s], s == 0 ? (f32x4{0, 0, 0, 0}) : c.S[0][qt][kt]);
    }
#pragma unroll
  for (int qt = 0; qt < NQ; ++qt) {
    float mx = c.S[0][qt][0][0];
#pragma unroll
    for (int j = 1; j < 8; ++j) mx = fmaxf(mx, c.S[0][qt][j >> 2][j & 3]);
    mx = red_g_max(mx);
    c.negm[qt] = f32x4{-mx, -mx, -mx, -mx};
    c.S[0][qt][0] += c.negm[qt]; c.S[0][qt][1] += c.negm[qt];
    c.l[qt] = 0.f;
#pragma unroll
    for (int dt = 0; dt < 8; ++dt) c.O[qt][dt] = f32x4{0, 0, 0, 0};
  }
}
template <int NQ> DI void att_epilogue(AttC<NQ>& c, char* ob) {
#pragma unroll
  for (int qt = 0; qt < NQ; ++qt) {
    const float inv = __builtin_amdgcn_rcpf(red_g_sum(c.l[qt]));
#pragma unroll
    for (int s = 0; s < 4; ++s) {
      const f32x4 a = c.O[qt][2 * s] * inv, bb = c.O[qt][2 * s + 1] * inv;
      u32x4 w = {cvtpk(a[0], a[1]), cvtpk(a[2], a[3]), cvtpk(bb[0], bb[1]), cvtpk(bb[2], bb[3])};
      asm volatile("global_store_dwordx4 %0, %1, off " OST_POL "\n\ts_nop 1" :: "v"(ob + (qt * 4 + s) * 1024), "v"(w) : "memory");
    }
  }
}

template <int NQ> DI void att_wave(char* ows_pair, unsigned qt0, unsigned m, unsigned rot) {
  asm volatile("; attention wave NQ=%0" :: "i"(NQ) : "memory");
  AttC<NQ> c;
  const unsigned lane = lane_id(), li = lane & 15, g = lane >> 4, pos = lane & 3, tq = li >> 2;
  c.kb = KRING + 2048 * (li >> 3) + 64 * (li & 7) + 16 * (g ^ (2 * (li >> 3)));
  c.vb0 = VRING + 2048 * (g >> 1) + 64 * (4 * (g & 1) + tq) + 32 * (g & 1) + 8 * pos; c.vb1 = c.vb0 ^ 32u;
  PIN(c.kb); PIN(c.vb0); PIN(c.vb1);
#pragma unroll 1
  for (int j = 0; j < 20; ++j) { if (j == 12) WAITV(0); BAR();
    if (j == 0) STAMP(21); if (j == 1) STAMP(22); if (j == 2) STAMP(23); if (j == 9) STAMP(24); if (j == 17) STAMP(25); }
#pragma unroll 1
  for (int r = 0; r < 4; ++r) {
    STAMP(4 * r);
    { unsigned q2 = QX + qt0 * 4096 + lane_id() * 16; PIN(q2); att_prologue<NQ>(c, q2); }
    SBAR();
    STAMP(4 * r + 1);
#pragma unroll 1
    for (int ii = 0; ii < 3; ++ii) { att_step<NQ, 0, true>(c); att_step<NQ, 1, true>(c); att_step<NQ, 2, true>(c); att_step<NQ, 3, true>(c); }
    att_step<NQ, 0, true>(c); att_step<NQ, 1, true>(c); att_step<NQ, 2, true>(c); att_step<NQ, 3, false>(c);
    STAMP(4 * r + 2);
    if (r < 3) BAR();
    asm volatile("s_nop 7\n\ts_nop 1" ::: "memory"); SBAR();
    att_epilogue<NQ>(c, ows_pair + (size_t)(8 * ((r + rot) & 3) + m) * ITEM_B + qt0 * 4096 + lane_id() * 16);
    STAMP(4 * r + 3);
    if (r < 3) BAR();
  }
}

DI void q_unit(f32x4 (&qa)[3][8], bf16x8 (&w)[8], const bf16x8 (&xc)[3], bf16x8 (&xn)[3], unsigned w0, unsigned w1, unsigned xa) {
#pragma unroll
  for (int dt = 0; dt < 8; ++dt) {
    MFMA16_V(qa[0][dt], w[dt], xc[0]);
    if (dt < 3) xn[dt] = lds128(xa + dt * 2048);
    SBAR();
    MFMA16_V(qa[1][dt], w[dt], xc[1]);
    MFMA16_V(qa[2][dt], w[dt], xc[2]);
    SBAR();
    const unsigned a = ((dt & 1) ? w1 : w0) + 512 * (dt >> 1); w[dt] = cat8(ldstr(a), ldstr(a + 256));
    SBAR();
  }
}
DI void proj_wave(unsigned v) {
  asm volatile("; projection wave" ::: "memory");
  const unsigned lane = lane_id(), li = lane & 15, g = lane >> 4, pos = lane & 3, tq = li >> 2;
  unsigned w0 = WRING + 2048 * g + 64 * tq + 32 * (g & 1) + 8 * pos, w1 = w0 ^ 32u;
  unsigned xa = XRING + v * 6144 + 1024 * (li >> 3) + 64 * (li & 7) + 16 * (g ^ (2 * (li >> 3)));
  unsigned qw = QX + v * 3 * 4096 + ((g >> 1) * 16 + li) * 16 + 8 * (g & 1);
  PIN(qw);
  int ws = 0, xd = XSLOT;
#define NEXTW() do { const int d_ = (ws == 2) ? -16384 : 8192; ws = (ws == 2) ? 0 : ws + 1; w0 += d_; w1 += d_; PIN(w0); PIN(w1); } while (0)
#define NEXTX() do { xa += xd; xd = -xd; PIN(xa); } while (0)
  bf16x8 w[8], xA[3], xB[3];
  PIN(w0); PIN(w1); PIN(xa);
  BAR();
#pragma unroll
  for (int q = 0; q < 3; ++q) xA[q] = lds128(xa + q * 2048);
#pragma unroll
  for (int dt = 0; dt < 8; ++dt) { const unsigned a = ((dt & 1) ? w1 : w0) + 512 * (dt >> 1); w[dt] = cat8(ldstr(a), ldstr(a + 256)); }
  NEXTW();
#pragma unroll 1
  for (int r = 0; r < 4; ++r) {
    f32x4 qa[3][8];
#pragma unroll
    for (int q = 0; q < 3; ++q)
#pragma unroll
      for (int dt = 0; dt < 8; ++dt) { qa[q][dt] = f32x4{0, 0, 0, 0}; PIN(qa[q][dt]); }
    asm volatile("s_nop 1");
#pragma unroll 1
    for (int n = 0; n < 18; n += 2) {
      BAR(); q_unit(qa, w, xA, xB, w0, w1, xa + 512); NEXTW(); NEXTX();
      BAR(); q_unit(qa, w, xB, xA, w0, w1, xa); NEXTW();
    }
    asm volatile("s_nop 7\n\ts_nop 1" ::: "memory"); SBAR();
#pragma unroll
    for (int q = 0; q < 3; ++q)
#pragma unroll
      for (int dt = 0; dt < 8; ++dt) {
        const f32x4 t = qa[q][dt] * QSCALE;
        u32x2 pk = {cvtpk(t[0], t[1]), cvtpk(t[2], t[3])};
        *(LAS u32x2*)(qw + q * 4096 + (dt >> 1) * 1024 + (dt & 1) * 512) = pk;
      }
  }
  BAR();
}

#define KVSTART 15
#define DMAW dma16
#define DMAX dma16
#define RBAR() asm volatile("s_barrier" ::: "memory")
DI void kv_loader(const u16* kbase, const u16* vbase, unsigned m, unsigned rot) {
  const unsigned lane = lane_id(), row7 = (lane >> 2) & 7, sh = lane >> 5, pos = lane & 3;
  const __amdgpu_buffer_rsrc_t srdK = __builtin_amdgcn_make_buffer_rsrc((void*)kbase, (short)0, NH * SK * DH * 2, 0x00020000);
  const __amdgpu_buffer_rsrc_t srdV = __builtin_amdgcn_make_buffer_rsrc((void*)vbase, (short)0, NH * SK * DH * 2, 0x00020000);
  const unsigned vk0 = row7 * 256 + sh * 64 + pos * 16, vk1 = vk0 ^ 32u, vv = row7 * 256 + sh * 64 + ((pos ^ (2 * (row7 >> 2))) * 16);
  auto issue = [&](int t, bool isv) {
    const unsigned so = (8 * (((t >> 4) + rot) & 3) + m) * (SK * DH * 2) + (t & 15) * 8192, lo = (isv ? VRING : KRING) + (t & 3) * 8192;
#pragma unroll
    for (int p = 0; p < 8; ++p)
      dma16nt(lo + 1024 * p, isv ? vv : ((p & 2) ? vk1 : vk0), isv ? srdV : srdK, so + 2048 * (p >> 1) + 128 * (p & 1));
  };
#pragma unroll 1
  for (int j = 0; j < 19; ++j) {
    RBAR();
    if (j == KVSTART) { issue(0, false); issue(1, false); issue(0, true); issue(2, false); issue(1, true); issue(3, false); issue(2, true); }
  }
  WAITV(48);
#pragma unroll 1
  for (int r = 0; r < 4; ++r) {
    RBAR();
#pragma unroll 1
    for (int i = 0; i < 16; ++i) {
      const int t = 16 * r + i;
      if (t <= 60) WAITV(32); else WAITV(0);
      RBAR();
      if (t + 4 < 64) issue(t + 4, false);
      if (t + 3 < 64) issue(t + 3, true);
    }
    if (r < 3) RBAR();
  }
}
DI void wx_loader(const u16* wq, const u16* x, unsigned m, unsigned rot) {
  const unsigned lane = lane_id(), row7 = (lane >> 2) & 7, sh = lane >> 5, pos = lane & 3;
  const __amdgpu_buffer_rsrc_t srdW = __builtin_amdgcn_make_buffer_rsrc((void*)wq, (short)0, DM * HD * 2, 0x00020000);
  const __amdgpu_buffer_rsrc_t srdX = __builtin_amdgcn_make_buffer_rsrc((void*)x, (short)0, SQ * DM * 2, 0x00020000);
  const unsigned vw0 = row7 * (HD * 2) + sh * 64 + pos * 16, vw1 = vw0 ^ 32u;
  const unsigned vx0 = row7 * (DM * 2) + sh * 64 + pos * 16, vx1 = vx0 ^ 32u;
  unsigned wl = 0, wso = (8 * rot + m) * (DH * 2), wr = rot; int wj = 0;
  auto issue_w = [&]() {
#pragma unroll
    for (int p = 0; p < 8; ++p) DMAW(WRING + wl + 1024 * p, (p & 2) ? vw1 : vw0, srdW, wso + 8 * (p >> 1) * (HD * 2) + 128 * (p & 1));
    wl = (wl == 16384) ? 0 : wl + 8192;
    wso += 32 * (HD * 2);
    if (++wj == 18) { wj = 0; wr = (wr + 1) & 3; wso = (8 * wr + m) * (DH * 2); }
  };
  unsigned xl = 0, xso = 0;
  auto issue_x = [&]() {
#pragma unroll
    for (int p = 0; p < 18; ++p) DMAX(XRING + xl + 1024 * p, (p & 1) ? vx1 : vx0, srdX, xso + p * 8 * (DM * 2));
    xl = XSLOT - xl;
    xso = (xso == 1024) ? 0 : xso + 128;
  };
  STAMPW(26);
  issue_w(); issue_x(); issue_w();
  STAMPW(27);
  WAITV(8);
  STAMPW(28);
  RBAR();
  issue_x(); issue_w();
#pragma unroll 1
  for (int G = 1; G < 72; G += 2) {
    if (G < 70) WAITV(26); else WAITV(0);
    RBAR();
    if (G + 2 < 72) issue_w();
    if (G == 71) break;
    if (G + 1 < 70) WAITV(8); else WAITV(0);
    RBAR();
    if (G + 3 < 72) { issue_x(); issue_w(); }
  }
}
DI void warm_up(const u16* wq, const u16* x, unsigned wid, unsigned m, unsigned bshare) {
  const unsigned lane = lane_id();
  const __amdgpu_buffer_rsrc_t srdW = __builtin_amdgcn_make_buffer_rsrc((void*)wq, (short)0, DM * HD * 2, 0x00020000);
  const __amdgpu_buffer_rsrc_t srdX = __builtin_amdgcn_make_buffer_rsrc((void*)x, (short)0, SQ * DM * 2, 0x00020000);
  __builtin_amdgcn_s_sleep(8);
  { const unsigned n = lane + 64 * wid, rw = (n >> 1) * 4 + bshare; dma4(JUNK, rw * (HD * 2) + (n & 1) * 128 + m * (DH * 2), srdW); }
  if (wid < 3) { const unsigned n = lane + 64 * wid, ch = n / 18, rw = (n % 18) * 8 + (m & 7); dma4(JUNK, n < 162 ? rw * (DM * 2) + ch * 128 : 0, srdX); }
  else { const unsigned n = 256 + (lane & 31), rw = (n >> 1) * 4 + bshare; dma4(JUNK, rw * (HD * 2) + (n & 1) * 128 + m * (DH * 2), srdW); }
}

__global__ __launch_bounds__(640, 3) void k_attn(const u16* __restrict__ xA, const u16* __restrict__ kA, const u16* __restrict__ vA, const u16* __restrict__ wqA,
                                                 const u16* __restrict__ xB, const u16* __restrict__ kB, const u16* __restrict__ vB, const u16* __restrict__ wqB,
                                                 char* __restrict__ ows) {
  extern __shared__ __attribute__((aligned(16))) char smem[];
  const unsigned wid = rfl(threadIdx.x >> 6);
  const unsigned blk = blockIdx.x, idx = blk >> 3, pair = (blk & 7) * 4 + (idx >> 3), m = idx & 7, br = pair >> 4, b = pair & 15;
  char* op = ows + (size_t)pair * NH * ITEM_B;
  const u16* wq = br ? wqB : wqA; const u16* x = (br ? xB : xA) + (size_t)b * SQ * DM;
  const unsigned rot = blk & 3;
  STAMP(20);
  if (wid < 4) {
    att_wave<2>(op, 2 * wid, m, rot); }
  else if (wid < 7) proj_wave(wid - 4);
  else if (wid == 7) att_wave<1>(op, 8, m, rot);
  else if (wid == 8) wx_loader(wq, x, m, rot);
  else kv_loader((br ? kB : kA) + (size_t)b * NH * SK * DH, (br ? vB : vA) + (size_t)b * NH * SK * DH, m, rot);
}

__global__ __launch_bounds__(256) void k_wtile(const u16* __restrict__ woA, const u16* __restrict__ woB, char* __restrict__ wt) {
  const unsigned f = blockIdx.x * 4 + (threadIdx.x >> 6), lane = threadIdx.x & 63, li = lane & 15, g = lane >> 4;
  const unsigned br = f / (128 * 36), r = f % (128 * 36), ks = r / 36, nt = r % 36;
  const u16* wo = br ? woB : woA;
  u16 e[8];
#pragma unroll
  for (int j = 0; j < 8; ++j) e[j] = wo[(size_t)(32 * ks + 16 * (j >> 2) + 4 * g + (j & 3)) * DM + 16 * nt + li];
  u32x4 w = {e[0] | (unsigned)e[1] << 16, e[2] | (unsigned)e[3] << 16, e[4] | (unsigned)e[5] << 16, e[6] | (unsigned)e[7] << 16};
  *(u32x4*)(wt + (size_t)f * 1024 + lane * 16) = w;
}

#define MFMA16_ACC(acc, a, b) asm volatile("v_mfma_f32_16x16x32_bf16 %0, %1, %2, %0" : "+a"(acc) : "v"(a), "v"(b))
template <int NT> DI void oproj_body(__amdgpu_buffer_rsrc_t srdO, __amdgpu_buffer_rsrc_t srdW, u16* __restrict__ outp, unsigned wid, unsigned lane) {
  f32x4 acc[NT][9];
#pragma unroll
  for (int n = 0; n < NT; ++n)
#pragma unroll
    for (int q = 0; q < 9; ++q) { acc[n][q] = f32x4{0, 0, 0, 0}; asm volatile("" : "+a"(acc[n][q])); }
  u32x4 of[4][9], wf[4][NT];
  const unsigned vo = lane * 16;
#define LOADSET(S, H) do { const unsigned so_ = (unsigned)(H) * ITEM_B; \
    _Pragma("unroll") for (int n = 0; n < NT; ++n) wf[S][n] = __builtin_amdgcn_raw_buffer_load_b128(srdW, vo + (n & 3) * 1024, 4 * so_ + (n >> 2) * 4096, 0); \
    _Pragma("unroll") for (int q = 0; q < 9; ++q) of[S][q] = __builtin_amdgcn_raw_buffer_load_b128(srdO, vo, so_ + q * 4096, 0); SBAR(); } while (0)
#define MMASET(S) do { _Pragma("unroll") for (int q = 0; q < 9; ++q) _Pragma("unroll") for (int n = 0; n < NT; ++n) MFMA16_ACC(acc[n][q], wf[S][n], of[S][q]); SBAR(); } while (0)
#define STEPSET(S, L, H) do { const unsigned so_ = (unsigned)(H) * ITEM_B; \
    _Pragma("unroll") for (int q = 0; q < 9; ++q) { \
      if (q < NT) wf[L][q] = __builtin_amdgcn_raw_buffer_load_b128(srdW, vo + (q & 3) * 1024, 4 * so_ + (q >> 2) * 4096, 0); \
      of[L][q] = __builtin_amdgcn_raw_buffer_load_b128(srdO, vo, so_ + q * 4096, 0); SBAR(); \
      _Pragma("unroll") for (int n = 0; n < NT; ++n) MFMA16_ACC(acc[n][q], wf[S][n], of[S][q]); SBAR(); } } while (0)
  LOADSET(0, 0); LOADSET(1, 1); LOADSET(2, 2);
#pragma unroll 1
  for (int h = 0; h < 28; h += 4) {
    STEPSET(0, 3, h + 3); STEPSET(1, 0, h + 4); STEPSET(2, 1, h + 5); STEPSET(3, 2, h + 6);
  }
  STEPSET(0, 3, 31); MMASET(1); MMASET(2); MMASET(3);
  asm volatile("s_nop 7\n\ts_nop 7" ::: "memory"); SBAR();
#pragma unroll
  for (int q = 0; q < 9; ++q)
    if ((unsigned)(q & 3) != wid) {
      const unsigned slot = (wid - (q & 3) - 1) & 3;
#pragma unroll
      for (int n = 0; n < NT; ++n) *(LAS f32x4*)(((q * 3 + slot) * NT + n) * 1024 + lane * 16) = acc[n][q];
    }
  __syncthreads();
  const unsigned li = lane & 15, g = lane >> 4;
#pragma unroll
  for (int q = 0; q < 9; ++q)
    if ((unsigned)(q & 3) == wid) {
#pragma unroll
      for (int n = 0; n < NT; ++n) {
        f32x4 a = acc[n][q];
#pragma unroll
        for (int s = 0; s < 3; ++s) a += *(const LAS f32x4*)(((q * 3 + s) * NT + n) * 1024 + lane * 16);
        u32x2 w = {cvtpk(a[0], a[1]), cvtpk(a[2], a[3])};
        *(u32x2*)(outp + (size_t)(16 * q + li) * (2 * DM) + 16 * n + 4 * g) = w;
      }
    }
}
__global__ __launch_bounds__(256, 1) void k_oproj(const char* __restrict__ ows, const char* __restrict__ wt, u16* __restrict__ out) {
  extern __shared__ __attribute__((aligned(16))) char smem[];
  const unsigned lane = threadIdx.x & 63u, wid = rfl(threadIdx.x >> 6);
  const unsigned blk = blockIdx.x, idx = blk >> 3, pair = (blk & 7) * 4 + (idx >> 3), ns = idx & 7, br = pair >> 4, b = pair & 15;
  const unsigned nt0 = ns < 4 ? 5 * ns : 20 + 4 * (ns - 4);
  const unsigned oo = wid * 1024, wo_ = (wid * 36 + nt0) * 1024;
  const __amdgpu_buffer_rsrc_t srdO = __builtin_amdgcn_make_buffer_rsrc((void*)(ows + (size_t)pair * NH * ITEM_B + oo), (short)0, (int)(NH * ITEM_B - oo), 0x00020000);
  const __amdgpu_buffer_rsrc_t srdW = __builtin_amdgcn_make_buffer_rsrc((void*)(wt + (size_t)br * 128 * 36 * 1024 + wo_), (short)0, (int)(128 * 36 * 1024 - wo_), 0x00020000);
  u16* outp = out + (size_t)b * SQ * (2 * DM) + br * DM + nt0 * 16;
  if (ns < 4) { asm volatile("; nt5" ::: "memory"); oproj_body<5>(srdO, srdW, outp, wid, lane); } else { asm volatile("; nt4" ::: "memory"); oproj_body<4>(srdO, srdW, outp, wid, lane); }
}

extern "C" void kernel_launch(void* const* d_in, const int* in_sizes, int n_in, void* d_out, int out_size, void* d_ws, size_t ws_size, hipStream_t stream) {
  (void)hipFuncSetAttribute((const void*)k_attn, hipFuncAttributeMaxDynamicSharedMemorySize, LDS_BYTES);
  (void)hipFuncSetAttribute((const void*)k_oproj, hipFuncAttributeMaxDynamicSharedMemorySize, 27 * 5 * 1024);
  char* ws = (char*)d_ws + ((ws_size - (WS_WT + 2 * 128 * 36 * 1024)) & ~(size_t)4095);
  k_wtile<<<2 * 128 * 36 / 4, 256, 0, stream>>>((const u16*)d_in[4], (const u16*)d_in[9], ws + WS_WT);
  k_attn<<<256, 640, LDS_BYTES, stream>>>((const u16*)d_in[0], (const u16*)d_in[1], (const u16*)d_in[2], (const u16*)d_in[3],
                                           (const u16*)d_in[5], (const u16*)d_in[6], (const u16*)d_in[7], (const u16*)d_in[8], ws + WS_O);
  k_oproj<<<256, 256, 27 * 5 * 1024, stream>>>(ws + WS_O, ws + WS_WT, (u16*)d_out);
}
```

```cpp
#include <hip/hip_runtime.h>
#include <stdint.h>

typedef short    bf16x8 __attribute__((ext_vector_type(8)));
typedef short    s16x4  __attribute__((ext_vector_type(4)));
typedef float    f32x4  __attribute__((ext_vector_type(4)));
typedef float    f32x2  __attribute__((ext_vector_type(2)));
typedef __bf16   bf16x2 __attribute__((ext_vector_type(2)));
typedef unsigned u32x4  __attribute__((ext_vector_type(4)));
typedef unsigned u32x2  __attribute__((ext_vector_type(2)));
typedef unsigned short u16;
#define WARMPOL ""
#define NOKROT
#define OST_POL "sc1"
#define WNT false
#define XPOL ""
#define KVSTART 14
#define DI __device__ __forceinline__
#define LAS __attribute__((address_space(3)))
#define MFMA16(a, b, c) __builtin_amdgcn_mfma_f32_16x16x32_bf16(a, b, c, 0, 0, 0)
#define SBAR() __builtin_amdgcn_sched_barrier(0)
#define PIN(x) asm volatile("" : "+v"(x))
#define MFMA16_V(acc, a, b) asm volatile("v_mfma_f32_16x16x32_bf16 %0, %1, %2, %0" : "+v"(acc) : "v"(a), "v"(b))
#define MFMA16_Z(d, a, b) asm volatile("v_mfma_f32_16x16x32_bf16 %0, %1, %2, 0" : "=&v"(d) : "v"(a), "v"(b))
#define MFMA16_C(d, a, b, cc) asm volatile("v_mfma_f32_16x16x32_bf16 %0, %1, %2, %3" : "=&v"(d) : "v"(a), "v"(b), "v"(cc))

constexpr int NB = 16, SQ = 144, DM = 576, NH = 32, SK = 512, DH = 128, HD = NH * DH;
constexpr int ITEM_B = 9 * 4 * 1024;
constexpr size_t WS_O = 0;
constexpr size_t WS_X = (size_t)1024 * ITEM_B, WS_F = WS_X + (size_t)256 * 2 * 25 * 1024;
constexpr float QSCALE = 0.08838834764831845f * 1.4426950408889634f;

DI unsigned rfl(unsigned x) { return __builtin_amdgcn_readfirstlane(x); }
DI unsigned lane_id() { unsigned l; asm volatile("v_mbcnt_lo_u32_b32 %0, -1, 0\n\tv_mbcnt_hi_u32_b32 %0, -1, %0" : "=v"(l)); return l; }
DI unsigned cvtpk(float lo, float hi) { f32x2 v = {lo, hi}; bf16x2 b = __builtin_convertvector(v, bf16x2); return __builtin_bit_cast(unsigned, b); }
DI float fadd_s(float a, float b) { float r; asm("v_add_f32_e32 %0, %1, %2" : "=v"(r) : "v"(a), "v"(b)); return r; }
DI bf16x8 lds128(unsigned a) { return *(const LAS bf16x8*)a; }
DI s16x4 ldstr(unsigned a) { return __builtin_bit_cast(s16x4, __builtin_amdgcn_ds_read_tr16_b64_v4i16((LAS s16x4*)a)); }
DI bf16x8 cat8(s16x4 lo, s16x4 hi) { return __builtin_shufflevector(lo, hi, 0, 1, 2, 3, 4, 5, 6, 7); }
DI void dma16(unsigned m0v, unsigned voff, __amdgpu_buffer_rsrc_t srd, unsigned soff) {
  asm volatile("s_mov_b32 m0, %0\n\ts_nop 0\n\tbuffer_load_dwordx4 %1, %2, %3 offen lds" :: "s"(m0v), "v"(voff), "s"(srd), "s"(soff) : "memory");
}
DI void dma16nt(unsigned m0v, unsigned voff, __amdgpu_buffer_rsrc_t srd, unsigned soff) {
  asm volatile("s_mov_b32 m0, %0\n\ts_nop 0\n\tbuffer_load_dwordx4 %1, %2, %3 offen nt lds" :: "s"(m0v), "v"(voff), "s"(srd), "s"(soff) : "memory");
}
DI void dma4(unsigned m0v, unsigned voff, __amdgpu_buffer_rsrc_t srd) {
  asm volatile("s_mov_b32 m0, %0\n\ts_nop 0\n\tbuffer_load_dword %1, %2, 0 offen " WARMPOL " lds" :: "s"(m0v), "v"(voff), "s"(srd) : "memory");
}
#define TILE8_BODY(POL) \
  "s_mov_b32 m0, %1\n\ts_mov_b32 %0, %2\n\t" \
  "buffer_load_dwordx4 %3, %5, %0 offen " POL " lds\n\ts_add_u32 m0, m0, 896\n\ts_nop 0\n\tbuffer_load_dwordx4 %3, %5, %0 offen offset:128 " POL " lds\n\ts_add_u32 m0, m0, 1152\n\ts_add_u32 %0, %0, %6\n\t" \
  "buffer_load_dwordx4 %4, %5, %0 offen " POL " lds\n\ts_add_u32 m0, m0, 896\n\ts_nop 0\n\tbuffer_load_dwordx4 %4, %5, %0 offen offset:128 " POL " lds\n\ts_add_u32 m0, m0, 1152\n\ts_add_u32 %0, %0, %6\n\t" \
  "buffer_load_dwordx4 %3, %5, %0 offen " POL " lds\n\ts_add_u32 m0, m0, 896\n\ts_nop 0\n\tbuffer_load_dwordx4 %3, %5, %0 offen offset:128 " POL " lds\n\ts_add_u32 m0, m0, 1152\n\ts_add_u32 %0, %0, %6\n\t" \
  "buffer_load_dwordx4 %4, %5, %0 offen " POL " lds\n\ts_add_u32 m0, m0, 896\n\ts_nop 0\n\tbuffer_load_dwordx4 %4, %5, %0 offen offset:128 " POL " lds"
template <bool NT, int D2> DI void dma_tile8(unsigned lo, unsigned so, unsigned va, unsigned vb, __amdgpu_buffer_rsrc_t srd) {
  unsigned t;
  if constexpr (NT) asm volatile(TILE8_BODY("nt") : "=&s"(t) : "s"(lo), "s"(so), "v"(va), "v"(vb), "s"(srd), "i"(D2) : "memory", "scc");
  else              asm volatile(TILE8_BODY("")   : "=&s"(t) : "s"(lo), "s"(so), "v"(va), "v"(vb), "s"(srd), "i"(D2) : "memory", "scc");
}
template <int D2> DI void dma_tile9(unsigned lo, unsigned so, unsigned va, __amdgpu_buffer_rsrc_t srd) {
  unsigned t;
  asm volatile("s_mov_b32 m0, %1\n\ts_mov_b32 %0, %2\n\t.rept 9\n\t"
               "buffer_load_dwordx4 %3, %4, %0 offen " XPOL " lds\n\ts_add_u32 m0, m0, 2048\n\ts_add_u32 %0, %0, %5\n\t.endr"
               : "=&s"(t) : "s"(lo), "s"(so), "v"(va), "s"(srd), "i"(D2) : "memory", "scc");
}
#define WAITV(n) asm volatile("s_waitcnt vmcnt(" #n ")" ::: "memory")
#define BAR() do { asm volatile("" ::: "memory"); __builtin_amdgcn_s_waitcnt(0xC07F); asm volatile("s_barrier" ::: "memory"); } while (0)
#define RBAR() asm volatile("s_barrier" ::: "memory")
constexpr unsigned GRP = 65536, WRING = 3 * 8192, XSLOT = 18432, XB = 126976, XA = XB + XSLOT, LDS_BYTES = 163840;
constexpr unsigned kslot(int i) { return 8192 * ((i & 3) == 0 ? 0 : (i & 3) == 1 ? 1 : (i & 3) == 2 ? 5 : 3); }
constexpr unsigned vslot(int i) { return 8192 * ((i & 3) == 0 ? 2 : (i & 3) == 1 ? 6 : (i & 3) == 2 ? 4 : 7); }
static_assert(XA + XSLOT == LDS_BYTES, "LDS map");

#define STAMP2(k)
#define STAMP(k)
#define STAMPW(k)

DI float red_g_max(float m) {
  auto r = __builtin_amdgcn_permlane32_swap(__float_as_uint(m), __float_as_uint(m), false, false);
  m = fmaxf(__uint_as_float(r[0]), __uint_as_float(r[1]));
  auto s = __builtin_amdgcn_permlane16_swap(__float_as_uint(m), __float_as_uint(m), false, false);
  return fmaxf(__uint_as_float(s[0]), __uint_as_float(s[1]));
}
DI float red_g_sum(float m) {
  auto r = __builtin_amdgcn_permlane32_swap(__float_as_uint(m), __float_as_uint(m), false, false);
  m = __uint_as_float(r[0]) + __uint_as_float(r[1]);
  auto s = __builtin_amdgcn_permlane16_swap(__float_as_uint(m), __float_as_uint(m), false, false);
  return __uint_as_float(s[0]) + __uint_as_float(s[1]);
}

template <int NQ> struct AttC {
  f32x4 O[NQ][8]; bf16x8 Q[NQ][4]; f32x4 S[2][NQ][2]; f32x4 negm[NQ]; float l[NQ];
  unsigned kb, vb0, vb1;
};
template <int NQ> struct Tab;
template <> struct Tab<2> {
  static constexpr int NPRE = 6;
  static constexpr int pre[6] = {100, 101, 102, 103, 104, 105};
  static constexpr int gx[16][2] = {{106, 0}, {107, 0}, {200, 201}, {110, 0}, {202, 203}, {111, 0}, {112, 0}, {113, 0}, {114, 0}, {210, 211}, {115, 0}, {116, 0}, {117, 0}, {300, 310}, {212, 213}, {301, 311}};
  static constexpr int gy[16] = {302, 312, 303, 313, 304, 314, 305, 315, 306, 316, 307, 317, 0, 0, 0, 0};
  static constexpr int all[24] = {100, 101, 102, 103, 104, 105, 106, 107, 110, 200, 201, 202, 203, 111, 112, 113, 114, 115, 116, 117, 210, 211, 212, 213};
  static constexpr int ady[16] = {300, 310, 301, 311, 302, 312, 303, 313, 304, 314, 305, 315, 306, 316, 307, 317};
};
template <> struct Tab<1> {
  static constexpr int NPRE = 4;
  static constexpr int pre[4] = {100, 101, 102, 103};
  static constexpr int gx[8][2] = {{104, 0}, {105, 0}, {200, 201}, {106, 0}, {107, 0}, {300, 301}, {202, 203}, {302, 303}};
  static constexpr int gy[8] = {304, 305, 306, 307, 0, 0, 0, 0};
  static constexpr int all[12] = {100, 101, 102, 103, 104, 105, 106, 107, 200, 201, 202, 203};
  static constexpr int ady[8] = {300, 301, 302, 303, 304, 305, 306, 307};
};
template <int NQ, int P, bool QK> struct Step {
  static constexpr int X = P & 1;
  static constexpr unsigned ks_ = kslot(P + 1), vs_ = vslot(P);
  bf16x8 fr[4]; float e[NQ][8]; unsigned pw[NQ][4]; u32x4 Pw[NQ];
  template <int OP> DI void op(AttC<NQ>& c) {
    constexpr int ty = OP / 100, qt = (OP / 10) % 10, j = OP % 10;
    if constexpr (ty == 1) { e[qt][j] = c.S[X][qt][j >> 2][j & 3]; asm volatile("v_exp_f32 %0, %0" : "+v"(e[qt][j])); }
    if constexpr (ty == 2) asm volatile("v_cvt_pk_bf16_f32 %0, %1, %2" : "=v"(pw[qt][j]) : "v"(e[qt][2 * j]), "v"(e[qt][2 * j + 1]));
    if constexpr (ty == 3) asm volatile("v_add_f32 %0, %0, %1" : "+v"(c.l[qt]) : "v"(e[qt][j]));
  }
  template <int R> DI void rd(AttC<NQ>& c) {
    if constexpr (R < 8) fr[R & 3] = lds128(c.kb + ks_ + 4096 * (R & 1) + 512 * (R >> 1));
    else if constexpr (R < 16) {
      constexpr int dt = R - 8;
      const unsigned vb = ((dt & 1) ? c.vb1 : c.vb0) + vs_ + 512 * (dt >> 1);
      fr[R & 3] = cat8(ldstr(vb), ldstr(vb + 4096));
    }
    SBAR();
  }
  template <int I> DI void gx(AttC<NQ>& c) {
    constexpr int K = I / NQ, qt = I % NQ, s = K >> 1, kt = K & 1;
    if constexpr (s == 0) MFMA16_C(c.S[X ^ 1][qt][kt], fr[K & 3], c.Q[qt][s], c.negm[qt]); else MFMA16_V(c.S[X ^ 1][qt][kt], fr[K & 3], c.Q[qt][s]);
    if constexpr (qt == NQ - 1) { SBAR(); rd<K + 4>(c); }
    op<Tab<NQ>::gx[I][0]>(c); op<Tab<NQ>::gx[I][1]>(c);
    if constexpr (I + 1 < 8 * NQ) gx<I + 1>(c);
  }
  template <int I> DI void gy(AttC<NQ>& c) {
    constexpr int Dt = I / NQ, qt = I % NQ;
    MFMA16_V(c.O[qt][Dt], fr[Dt & 3], Pw[qt]);
    if constexpr (qt == NQ - 1) { SBAR(); rd<Dt + 12>(c); }
    op<(QK ? Tab<NQ>::gy[I] : Tab<NQ>::ady[I])>(c);
    if constexpr (I + 1 < 8 * NQ) gy<I + 1>(c);
  }
  template <int I> DI void pre(AttC<NQ>& c) {
    op<(QK ? Tab<NQ>::pre[I < Tab<NQ>::NPRE ? I : 0] : Tab<NQ>::all[I])>(c);
    if constexpr (I + 1 < (QK ? Tab<NQ>::NPRE : 12 * NQ)) pre<I + 1>(c);
  }
  DI void run(AttC<NQ>& c) {
    if constexpr (QK) {
      rd<0>(c); rd<1>(c); rd<2>(c); rd<3>(c);
      pre<0>(c);
      gx<0>(c);
    } else {
      rd<8>(c); rd<9>(c); rd<10>(c); rd<11>(c);
      pre<0>(c);
      asm volatile("s_nop 1");
    }
#pragma unroll
    for (int qt = 0; qt < NQ; ++qt) Pw[qt] = u32x4{pw[qt][0], pw[qt][1], pw[qt][2], pw[qt][3]};
    gy<0>(c);
  }
};

template <int NQ, int P, bool QK> DI void att_step(AttC<NQ>& c) { BAR(); SBAR(); Step<NQ, P, QK> s; s.run(c); SBAR(); }

template <int NQ> DI void att_prologue(AttC<NQ>& c) {
#pragma unroll
  for (int s = 0; s < 4; ++s)
#pragma unroll
    for (int kt = 0; kt < 2; ++kt) {
      const bf16x8 kf = lds128(c.kb + 4096 * kt + 512 * s);
#pragma unroll
      for (int qt = 0; qt < NQ; ++qt) c.S[0][qt][kt] = MFMA16(kf, c.Q[qt][s], s == 0 ? (f32x4{0, 0, 0, 0}) : c.S[0][qt][kt]);
    }
#pragma unroll
  for (int qt = 0; qt < NQ; ++qt) {
    float mx = c.S[0][qt][0][0];
#pragma unroll
    for (int j = 1; j < 8; ++j) mx = fmaxf(mx, c.S[0][qt][j >> 2][j & 3]);
    mx = red_g_max(mx);
    c.negm[qt] = f32x4{-mx, -mx, -mx, -mx};
    c.S[0][qt][0] += c.negm[qt]; c.S[0][qt][1] += c.negm[qt];
    c.l[qt] = 0.f;
#pragma unroll
    for (int dt = 0; dt < 8; ++dt) c.O[qt][dt] = f32x4{0, 0, 0, 0};
  }
}
template <int NQ> DI void att_redo_true_max(AttC<NQ>& c, const u16* __restrict__ kh, const u16* __restrict__ vh) {
  const unsigned lane = lane_id(), li = lane & 15, g = lane >> 4;
  const u16* kp = kh + li * DH + g * 8;
  float mx[NQ];
#pragma unroll
  for (int qt = 0; qt < NQ; ++qt) mx[qt] = -__builtin_inff();
#pragma unroll 1
  for (int kt = 0; kt < SK / 16; ++kt) {
    f32x4 s4[NQ];
#pragma unroll
    for (int qt = 0; qt < NQ; ++qt) s4[qt] = f32x4{0, 0, 0, 0};
#pragma unroll
    for (int s = 0; s < 4; ++s) {
      const bf16x8 kf = *(const bf16x8*)(kp + kt * 16 * DH + s * 32);
#pragma unroll
      for (int qt = 0; qt < NQ; ++qt) s4[qt] = MFMA16(kf, c.Q[qt][s], s4[qt]);
    }
#pragma unroll
    for (int qt = 0; qt < NQ; ++qt) mx[qt] = fmaxf(fmaxf(fmaxf(mx[qt], s4[qt][0]), fmaxf(s4[qt][1], s4[qt][2])), s4[qt][3]);
  }
#pragma unroll
  for (int qt = 0; qt < NQ; ++qt) {
    mx[qt] = red_g_max(mx[qt]); c.l[qt] = 0.f;
#pragma unroll
    for (int dt = 0; dt < 8; ++dt) c.O[qt][dt] = f32x4{0, 0, 0, 0};
  }
#pragma unroll 1
  for (int st = 0; st < SK / 32; ++st) {
    u32x4 pw[NQ];
#pragma unroll
    for (int kt = 0; kt < 2; ++kt) {
      f32x4 s4[NQ];
#pragma unroll
      for (int qt = 0; qt < NQ; ++qt) s4[qt] = f32x4{-mx[qt], -mx[qt], -mx[qt], -mx[qt]};
#pragma unroll
      for (int s = 0; s < 4; ++s) {
        const bf16x8 kf = *(const bf16x8*)(kp + (2 * st + kt) * 16 * DH + s * 32);
#pragma unroll
        for (int qt = 0; qt < NQ; ++qt) s4[qt] = MFMA16(kf, c.Q[qt][s], s4[qt]);
      }
#pragma unroll
      for (int qt = 0; qt < NQ; ++qt) {
        float e[4];
#pragma unroll
        for (int j = 0; j < 4; ++j) { e[j] = __builtin_amdgcn_exp2f(s4[qt][j]); c.l[qt] += e[j]; }
        pw[qt][2 * kt] = cvtpk(e[0], e[1]); pw[qt][2 * kt + 1] = cvtpk(e[2], e[3]);
      }
    }
    const u16* vp = vh + (32 * st + 4 * g) * DH + li;
#pragma unroll
    for (int dt = 0; dt < 8; ++dt) {
      bf16x8 vf;
#pragma unroll
      for (int j = 0; j < 8; ++j) vf[j] = (short)vp[(16 * (j >> 2) + (j & 3)) * DH + 16 * dt];
#pragma unroll
      for (int qt = 0; qt < NQ; ++qt) c.O[qt][dt] = MFMA16(vf, __builtin_bit_cast(bf16x8, pw[qt]), c.O[qt][dt]);
      SBAR();
    }
  }
#pragma unroll
  for (int qt = 0; qt < NQ; ++qt) c.l[qt] = red_g_sum(c.l[qt]);
}
template <int NQ> DI void att_epilogue(AttC<NQ>& c, char* ob, const u16* kh, const u16* vh) {
  bool bad = false;
#pragma unroll
  for (int qt = 0; qt < NQ; ++qt) { c.l[qt] = red_g_sum(c.l[qt]); bad |= !(c.l[qt] < 16777216.0f); }
  if (__builtin_expect(__any(bad), 0)) att_redo_true_max<NQ>(c, kh, vh);
#pragma unroll
  for (int qt = 0; qt < NQ; ++qt) {
    const float inv = __builtin_amdgcn_rcpf(c.l[qt]);
#pragma unroll
    for (int s = 0; s < 4; ++s) {
      const f32x4 a = c.O[qt][2 * s] * inv, bb = c.O[qt][2 * s + 1] * inv;
      u32x4 w = {cvtpk(a[0], a[1]), cvtpk(a[2], a[3]), cvtpk(bb[0], bb[1]), cvtpk(bb[2], bb[3])};
      asm volatile("global_store_dwordx4 %0, %1, off " OST_POL "\n\ts_nop 1" :: "v"(ob + (qt * 4 + s) * 1024), "v"(w) : "memory");
    }
  }
}

DI bf16x8 q_frag(const f32x4& t0, const f32x4& t1) {
  const f32x4 u0 = t0 * QSCALE, u1 = t1 * QSCALE;
  unsigned a0 = cvtpk(u0[0], u0[1]), a1 = cvtpk(u0[2], u0[3]), b0 = cvtpk(u1[0], u1[1]), b1 = cvtpk(u1[2], u1[3]);
  auto r0 = __builtin_amdgcn_permlane32_swap(a0, b0, false, false);
  auto r1 = __builtin_amdgcn_permlane32_swap(a1, b1, false, false);
  auto s0 = __builtin_amdgcn_permlane16_swap(r0[0], r0[1], false, false);
  auto s1 = __builtin_amdgcn_permlane16_swap(r1[0], r1[1], false, false);
  u32x4 w = {s0[0], s1[0], s0[1], s1[1]};
  return __builtin_bit_cast(bf16x8, w);
}
template <int NQ, int WS, bool RX, bool Z = false> DI void q_unit(f32x4 (&qa)[NQ][8], const bf16x8 (&wc)[8], bf16x8 (&wn)[8], const bf16x8 (&xc)[NQ], bf16x8 (&xn)[2][NQ], unsigned w0, unsigned w1, unsigned xa) {
#pragma unroll
  for (int i = 0; i < 8 * NQ; ++i) {
    const int dt = i / NQ, q = i % NQ;
    if constexpr (Z) MFMA16_Z(qa[q][dt], wc[dt], xc[q]); else MFMA16_V(qa[q][dt], wc[dt], xc[q]);
    SBAR();
#pragma unroll
    for (int u = i * (3 - NQ); u < (i + 1) * (3 - NQ); ++u) {
      if (WS >= 0 && u < 8) { const unsigned a = ((u & 1) ? w1 : w0) + 8192 * WS + 512 * (u >> 1); wn[u] = cat8(ldstr(a), ldstr(a + 256)); }
      if (RX && u >= 8 && u < 8 + 2 * NQ) xn[(u - 8) / NQ][(u - 8) % NQ] = lds128(xa + ((u - 8) % NQ) * 2048 + ((u - 8) / NQ) * 512);
    }
    SBAR();
  }
}

template <int NQ> DI void att_wave(char* ows_pair, const u16* kbase, const u16* vbase, unsigned qt0, unsigned m, unsigned hg0, unsigned lb) {
  asm volatile("; attention wave NQ=%0" :: "i"(NQ) : "memory");
  STAMP(20);
#pragma unroll 1
  for (int r = 0; r < 2; ++r) {
    AttC<NQ> c;
    STAMP(4 * r);
    {
      const unsigned lane = lane_id(), li = lane & 15, g = lane >> 4, pos = lane & 3, tq = li >> 2;
      unsigned w0 = lb + WRING + 2048 * g + 64 * tq + 32 * (g & 1) + 8 * pos, w1 = w0 ^ 32u;
      unsigned xb = XB + qt0 * 2048 + 1024 * (li >> 3) + 64 * (li & 7) + 16 * (g ^ (2 * (li >> 3)));
      bf16x8 w[8], v[8], x0[2][NQ], x1[2][NQ];
      f32x4 qa[NQ][8];
      PIN(w0); PIN(w1); PIN(xb);
      BAR();
      if (r == 0) STAMP(21);
#pragma unroll
      for (int q = 0; q < 2 * NQ; ++q) x0[q / NQ][q % NQ] = lds128(xb + XSLOT + (q % NQ) * 2048 + (q / NQ) * 512);
#pragma unroll
      for (int dt = 0; dt < 8; ++dt) { const unsigned a = ((dt & 1) ? w1 : w0) + 512 * (dt >> 1); w[dt] = cat8(ldstr(a), ldstr(a + 256)); }
      BAR(); q_unit<NQ, 1, false, true>(qa, w, v, x0[0], x1, w0, w1, 0);
#pragma unroll 1
      for (int n = 0; n < 4; ++n) {
        BAR(); if (r == 0 && n == 1) STAMP(22); if (r == 0 && n == 3) STAMP(23);
               q_unit<NQ, 2, true>(qa, v, w, x0[1], x1, w0, w1, xb);
        BAR(); q_unit<NQ, 3, false>(qa, w, v, x1[0], x0, w0, w1, 0);
        BAR(); q_unit<NQ, 0, true>(qa, v, w, x1[1], x0, w0, w1, xb + XSLOT);
        BAR(); q_unit<NQ, 1, false>(qa, w, v, x0[0], x1, w0, w1, 0);
      }
      BAR(); q_unit<NQ, -1, false>(qa, v, w, x0[1], x1, 0, 0, 0);
      c.kb = lb + 2048 * (li >> 3) + 64 * (li & 7) + 16 * (g ^ (2 * (li >> 3)));
      c.vb0 = lb + 2048 * (g >> 1) + 64 * (4 * (g & 1) + tq) + 32 * (g & 1) + 8 * pos; c.vb1 = c.vb0 ^ 32u;
      PIN(c.kb); PIN(c.vb0); PIN(c.vb1);
      asm volatile("s_nop 7" ::: "memory"); SBAR();
#pragma unroll
      for (int q = 0; q < NQ; ++q)
#pragma unroll
        for (int s = 0; s < 4; ++s) c.Q[q][s] = q_frag(qa[q][2 * s], qa[q][2 * s + 1]);
    }
    STAMP(4 * r + 1);
    att_prologue<NQ>(c);
    SBAR();
#pragma unroll 1
    for (int ii = 0; ii < 3; ++ii) { att_step<NQ, 0, true>(c); att_step<NQ, 1, true>(c); att_step<NQ, 2, true>(c); att_step<NQ, 3, true>(c); }
    att_step<NQ, 0, true>(c); att_step<NQ, 1, true>(c); att_step<NQ, 2, true>(c); att_step<NQ, 3, false>(c);
    STAMP(4 * r + 2);
    asm volatile("s_nop 7\n\ts_nop 1" ::: "memory"); SBAR();
    const unsigned h = 8 * ((hg0 + 2 * r) & 3) + m;
    att_epilogue<NQ>(c, ows_pair + (size_t)h * ITEM_B + qt0 * 4096 + lane_id() * 16, kbase + (size_t)h * SK * DH, vbase + (size_t)h * SK * DH);
    STAMP(4 * r + 3);
  }
}

DI void wait_newer(int n) {
  if (n >= 32) { if (n >= 40) WAITV(40); else WAITV(32); }
  else if (n >= 16) { if (n >= 24) WAITV(24); else WAITV(16); }
  else if (n >= 8) WAITV(8); else WAITV(0);
}
DI void loader(const u16* wq, const u16* x, const u16* kbase, const u16* vbase, unsigned m, unsigned hg0, unsigned e0, unsigned half) {
  const unsigned lane = lane_id(), row7 = (lane >> 2) & 7, sh = lane >> 5, pos = lane & 3, lb = half * GRP;
  const __amdgpu_buffer_rsrc_t srdW = __builtin_amdgcn_make_buffer_rsrc((void*)wq, (short)0, DM * HD * 2, 0x00020000);
  const __amdgpu_buffer_rsrc_t srdX = __builtin_amdgcn_make_buffer_rsrc((void*)x, (short)0, SQ * DM * 2, 0x00020000);
  const __amdgpu_buffer_rsrc_t srdK = __builtin_amdgcn_make_buffer_rsrc((void*)kbase, (short)0, NH * SK * DH * 2, 0x00020000);
  const __amdgpu_buffer_rsrc_t srdV = __builtin_amdgcn_make_buffer_rsrc((void*)vbase, (short)0, NH * SK * DH * 2, 0x00020000);
  const unsigned vw0 = row7 * (HD * 2) + sh * 64 + pos * 16, vw1 = vw0 ^ 32u;
  const unsigned vx = (row7 * (DM * 2) + sh * 64 + pos * 16) ^ (32u * half);
  const unsigned vk0 = row7 * 256 + sh * 64 + pos * 16, vk1 = vk0 ^ 32u, vv = row7 * 256 + sh * 64 + ((pos ^ (2 * (row7 >> 2))) * 16);
  int cnt = 0;
  auto issue_w = [&](unsigned h, int s) {
    int ks = s + 2 * e0; if (ks >= 18) ks -= 18;
    dma_tile8<WNT, 8 * HD * 2>(lb + WRING + (s & 3) * 8192, ks * 32 * (HD * 2) + h * (DH * 2), vw0, vw1, srdW);
    return cnt += 8;
  };
  auto issue_x = [&](int e) {
    int ee = e + e0; if (ee >= 9) ee -= 9;
    dma_tile9<16 * DM * 2>(((e & 1) ? XB : XA) + half * 1024, ee * 128 + half * 8 * (DM * 2), vx, srdX);
    return cnt += 9;
  };
  auto issue_k = [&](unsigned h, int t) {
    const unsigned q = t & 3, lo = q == 0 ? kslot(0) : q == 1 ? kslot(1) : q == 2 ? kslot(2) : kslot(3);
    dma_tile8<true, 2048>(lb + lo, h * (SK * DH * 2) + t * 8192, vk0, vk1, srdK);
    return cnt += 8;
  };
  auto issue_v = [&](unsigned h, int t) {
    const unsigned q = t & 3, lo = q == 0 ? vslot(0) : q == 1 ? vslot(1) : q == 2 ? vslot(2) : vslot(3);
    dma_tile8<true, 2048>(lb + lo, h * (SK * DH * 2) + t * 8192, vv, vv, srdV);
    return cnt += 8;
  };
  unsigned h = 8 * hg0 + m;
  int n0, n1, n2, n3;
  issue_w(h, 0); n0 = issue_x(0); n1 = issue_w(h, 1); issue_w(h, 2);
#pragma unroll 1
  for (int r = 0; r < 2; ++r) {
    const unsigned hn = 8 * ((hg0 + 2) & 3) + m;
    wait_newer(cnt - n0); RBAR();
    n2 = issue_x(1);
    if (r == 0) issue_w(h, 3);
    n3 = cnt;
#pragma unroll 1
    for (int G = 1; G < 15; ++G) {
      wait_newer(cnt - n1); RBAR();
      n1 = n2; n2 = n3;
      if (G & 1) issue_x((G + 3) >> 1);
      n3 = issue_w(h, G + 3);
      if (r == 0 && G == KVSTART) { issue_k(h, 0); issue_k(h, 1); issue_v(h, 0); }
    }
    int a0 = cnt, a1, a2;
    wait_newer(cnt - n1); RBAR();
    issue_k(h, 2);
    wait_newer(cnt - n2); RBAR();
    a1 = issue_v(h, 1);
    wait_newer(cnt - n3); RBAR();
    issue_k(h, 3);
    wait_newer(cnt - a0); RBAR();
    a2 = issue_v(h, 2); issue_v(h, 3);
#pragma unroll 1
    for (int i = 0; i < 16; ++i) {
      wait_newer(cnt - a0); RBAR();
      a0 = a1; a1 = a2;
      if (i < 12) issue_k(h, i + 4);
      if (i >= 1 && i < 13) issue_v(h, i + 3);
      a2 = cnt;
      if (r == 0) {
        if (i == 12) issue_k(hn, 0);
        if (i == 13) { issue_k(hn, 1); issue_v(hn, 0); }
        if (i == 14) { issue_x(0); issue_w(hn, 2); issue_w(hn, 3); }
        if (i == 15) { n0 = issue_w(hn, 0); n1 = issue_w(hn, 1); }
      }
    }
    h = hn;
  }
}

__global__ __launch_bounds__(768, 3) void k_attn(const u16* __restrict__ xA, const u16* __restrict__ kA, const u16* __restrict__ vA, const u16* __restrict__ wqA,
                                                 const u16* __restrict__ xB, const u16* __restrict__ kB, const u16* __restrict__ vB, const u16* __restrict__ wqB,
                                                 char* __restrict__ ows) {
  extern __shared__ __attribute__((aligned(16))) char smem[];
  const unsigned wid12 = rfl(threadIdx.x >> 6), half = wid12 >= 6, wid = wid12 - 6 * half, lb = half * GRP;
  const unsigned blk = blockIdx.x, idx = blk >> 3, pair = (blk & 7) * 4 + (idx >> 3), m = idx & 7, br = pair >> 4, b = pair & 15;
  const unsigned hg0 = (blk + half) & 3;
  char* op = ows + (size_t)pair * NH * ITEM_B;
  const u16* kb = (br ? kB : kA) + (size_t)b * NH * SK * DH; const u16* vb = (br ? vB : vA) + (size_t)b * NH * SK * DH;
  if (wid < 4) att_wave<2>(op, kb, vb, 2 * wid, m, hg0, lb);
  else if (wid == 4) {
    if (lane_id() == 0) ((unsigned*)(ows + WS_F))[(blk * 2 + half) * 16] = 0u;
    att_wave<1>(op, kb, vb, 8, m, hg0, lb);
  }
  else {
    const unsigned e0 = 0;
    loader(br ? wqB : wqA, (br ? xB : xA) + (size_t)b * SQ * DM, kb, vb, m, hg0, e0, half);

  }
}

constexpr unsigned OP_SLOT = 36864, OP_LDS = 4 * OP_SLOT;
template <int NQT, int NNT> struct OSet { bf16x8 w[NNT], ol; };
template <int NQT, int NNT, bool RD, bool Z> DI void o_unit(f32x4 (&acc)[NQT][NNT], bf16x8 (&of)[NQT - 1], const OSet<NQT, NNT>& cs, OSet<NQT, NNT>& ns, unsigned oa, unsigned wa) {
#pragma unroll
  for (int q = 0; q < NQT; ++q) {
#pragma unroll
    for (int n = 0; n < NNT; ++n) {
      if constexpr (Z) MFMA16_Z(acc[q][n], cs.w[n], q < NQT - 1 ? of[q < NQT - 1 ? q : 0] : cs.ol); else MFMA16_V(acc[q][n], cs.w[n], q < NQT - 1 ? of[q < NQT - 1 ? q : 0] : cs.ol);
      SBAR();
      if (RD && q == 0) ns.w[n] = cat8(ldstr(wa + 32 * n), ldstr(wa + 32 * n + 16 * 288));
      if (RD && q == 1 && n == 1) ns.ol = lds128(oa + (NQT - 1) * 1024);
      SBAR();
    }
    if (RD && q < NQT - 1) { of[q] = lds128(oa + q * 1024); SBAR(); }
  }
}
template <int NQT, int NNT> DI void oproj_consumer(u16* __restrict__ outp, char* __restrict__ wsb, unsigned kh, unsigned kh2, unsigned blk) {
  asm volatile("; oproj consumer %0 x %1" :: "i"(NQT), "i"(NNT) : "memory");
  constexpr unsigned qh = NQT == 4, nh = NNT == 4, q0 = 5 * qh, n0 = 5 * nh;
  f32x4 acc[NQT][NNT];
  bf16x8 of[NQT - 1]; OSet<NQT, NNT> sa, sb;
  unsigned oa, wa;
  { const unsigned lane = lane_id(), li = lane & 15, g = lane >> 4;
    oa = kh * 18432 + q0 * 1024 + lane * 16; wa = kh * 18432 + 9216 + 288 * (4 * g + (li >> 2)) + 8 * (lane & 3) + 32 * n0; }
  int sd = 2 * OP_SLOT;
#define FLIP() do { oa += sd; wa += sd; sd = -sd; PIN(oa); PIN(wa); } while (0)
  PIN(oa); PIN(wa);
  STAMP2(24);
  BAR();
  STAMP2(25);
#pragma unroll
  for (int n = 0; n < NNT; ++n) sa.w[n] = cat8(ldstr(wa + 32 * n), ldstr(wa + 32 * n + 16 * 288));
#pragma unroll
  for (int q = 0; q < NQT - 1; ++q) of[q] = lds128(oa + q * 1024);
  sa.ol = lds128(oa + (NQT - 1) * 1024);
  BAR(); o_unit<NQT, NNT, true, true>(acc, of, sa, sb, oa + OP_SLOT, wa + OP_SLOT); FLIP();
#pragma unroll 1
  for (int j = 2; j < 32; j += 2) {
    BAR(); o_unit<NQT, NNT, true, false>(acc, of, sb, sa, oa, wa);
    BAR(); o_unit<NQT, NNT, true, false>(acc, of, sa, sb, oa + OP_SLOT, wa + OP_SLOT); FLIP();
  }
  BAR();
  o_unit<NQT, NNT, false, false>(acc, of, sb, sa, 0, 0);
  asm volatile("s_nop 7\n\ts_nop 1" ::: "memory"); SBAR();
  STAMP2(26);
  const unsigned lane = lane_id(), li = lane & 15, g = lane >> 4;
  unsigned ra = (2 * qh + nh) * 25600 + lane * 16; PIN(ra);
  if (kh) {
#pragma unroll
    for (int q = 0; q < NQT; ++q)
#pragma unroll
      for (int n = 0; n < NNT; ++n) *(LAS f32x4*)(ra + (q * NNT + n) * 1024) = acc[q][n];
  }
  BAR();
  if (kh) return;
#pragma unroll
  for (int q = 0; q < NQT; ++q)
#pragma unroll
    for (int n = 0; n < NNT; ++n) acc[q][n] += *(const LAS f32x4*)(ra + (q * NNT + n) * 1024);
  if (qh != kh2) {
    char* xp = wsb + WS_X + (size_t)(blk * 2 + nh) * 25600 + lane * 16;
#pragma unroll
    for (int q = 0; q < NQT; ++q)
#pragma unroll
      for (int n = 0; n < NNT; ++n) asm volatile("global_store_dwordx4 %0, %1, off sc1\n\ts_nop 1" :: "v"(xp + (q * NNT + n) * 1024), "v"(acc[q][n]) : "memory");
    asm volatile("s_waitcnt vmcnt(0)" ::: "memory");
  }
  RBAR();
  if (qh != kh2) {
    if (nh == 0 && lane == 0) __hip_atomic_store((unsigned*)(wsb + WS_F) + blk * 32, 1u, __ATOMIC_RELAXED, __HIP_MEMORY_SCOPE_AGENT);
    return;
  }
  STAMP2(27);
  const unsigned pb = blk ^ 1u;
  { unsigned* fl = (unsigned*)(wsb + WS_F) + pb * 32;
    for (int spin = 0; spin < (1 << 22) && __hip_atomic_load(fl, __ATOMIC_RELAXED, __HIP_MEMORY_SCOPE_AGENT) != 1u; ++spin) __builtin_amdgcn_s_sleep(1);
    __builtin_amdgcn_fence(__ATOMIC_ACQUIRE, "wavefront"); }
  STAMP2(28);
  const __amdgpu_buffer_rsrc_t srdP = __builtin_amdgcn_make_buffer_rsrc((void*)(wsb + WS_X + (size_t)(pb * 2 + nh) * 25600), (short)0, 25600, 0x00020000);
  constexpr unsigned RB = NNT * 32 + 16;
  unsigned ta = (2 * qh + nh) * 25600 + li * RB + g * 8; PIN(ta);
  f32x4 px[NQT * NNT > 16 ? 13 : 16];
#pragma unroll
  for (int q = 0; q < NQT; ++q)
#pragma unroll
    for (int n = 0; n < NNT; ++n) {
      constexpr int NB_ = NQT * NNT > 16 ? 13 : 16;
      const int t = q * NNT + n;
      if (t % NB_ == 0) {
#pragma unroll
        for (int u = 0; u < NB_; ++u) if (t + u < NQT * NNT) px[u] = __builtin_bit_cast(f32x4, __builtin_amdgcn_raw_buffer_load_b128(srdP, lane * 16, (t + u) * 1024, 16));
        SBAR();
      }
      const f32x4 a = acc[q][n] + px[t % NB_];
      *(LAS u32x2*)(ta + q * 16 * RB + n * 32) = u32x2{cvtpk(a[0], a[1]), cvtpk(a[2], a[3])};
    }
  constexpr unsigned CPR = NNT * 2, NCH = NQT * 16 * CPR;
#pragma unroll
  for (unsigned i = 0; i < (NCH + 63) / 64; ++i) {
    const unsigned ch = i * 64 + lane, row = ch / CPR, cc = ch - row * CPR;
    if (ch < NCH) *(u32x4*)(outp + (size_t)(16 * q0 + row) * (2 * DM) + 16 * n0 + cc * 8) = *(const LAS u32x4*)((2 * qh + nh) * 25600 + row * RB + cc * 16);
  }
  STAMP2(29);
}
DI void oproj_loader(const char* op, const u16* wo, unsigned nq, unsigned kh2, unsigned l, unsigned rot) {
  const unsigned lane = lane_id(), kh = l >> 1, isw = l & 1;
  const __amdgpu_buffer_rsrc_t srd = isw ? __builtin_amdgcn_make_buffer_rsrc((void*)wo, (short)0, HD * DM * 2, 0x00020000)
                                         : __builtin_amdgcn_make_buffer_rsrc((void*)op, (short)0, NH * ITEM_B, 0x00020000);
  unsigned vo[9];
#pragma unroll
  for (int p = 0; p < 9; ++p) { const unsigned n = 64 * p + lane, row = n / 18; vo[p] = isw ? row * (DM * 2) + (n - 18 * row) * 16 + nq * 288 : p * 4096 + lane * 16; }
  auto issue = [&](int j) {
    const unsigned h = 16 * kh2 + (((j >> 1) + rot) & 15), s = 2 * (j & 1) + kh;
    const unsigned so = isw ? (h * 4 + s) * 32 * (DM * 2) : h * ITEM_B + s * 1024, lo = (j & 3) * OP_SLOT + l * 9216;
    {
#pragma unroll
      for (int p = 0; p < 9; ++p) dma16(lo + 1024 * p, vo[p], srd, so);
    }
  };
  issue(0); issue(1); issue(2);
#pragma unroll 1
  for (int j = 0; j < 32; ++j) {
    if (j < 30) WAITV(18); else if (j == 30) WAITV(9); else WAITV(0);
    RBAR();
    if (j + 3 < 32) issue(j + 3);
  }
}
#define OROT (pair & 15)
__global__ __launch_bounds__(768, 3) void k_oproj(char* __restrict__ wsb, const u16* __restrict__ woA, const u16* __restrict__ woB, u16* __restrict__ out) {
  extern __shared__ __attribute__((aligned(16))) char smem[];
  const unsigned wid = rfl(threadIdx.x >> 6);
  const unsigned blk = blockIdx.x, idx = blk >> 3, pair = (blk & 6) * 4 + (idx >> 2), nq = idx & 3, kh2 = blk & 1, br = pair >> 4, b = pair & 15;
  u16* outp = out + (size_t)b * SQ * (2 * DM) + br * DM + nq * 144;
  if (wid >= 8) { oproj_loader(wsb + WS_O + (size_t)pair * NH * ITEM_B, br ? woB : woA, nq, kh2, wid - 8, OROT); return; }
  const unsigned kh = wid >> 2, t = kh ? 7 - wid : wid;
  if (t == 0) oproj_consumer<5, 5>(outp, wsb, kh, kh2, blk);
  else if (t == 1) oproj_consumer<5, 4>(outp, wsb, kh, kh2, blk);
  else if (t == 2) oproj_consumer<4, 5>(outp, wsb, kh, kh2, blk);
  else oproj_consumer<4, 4>(outp, wsb, kh, kh2, blk);
}

extern "C" void kernel_launch(void* const* d_in, const int* in_sizes, int n_in, void* d_out, int out_size, void* d_ws, size_t ws_size, hipStream_t stream) {
  (void)hipFuncSetAttribute((const void*)k_attn, hipFuncAttributeMaxDynamicSharedMemorySize, LDS_BYTES);
  (void)hipFuncSetAttribute((const void*)k_oproj, hipFuncAttributeMaxDynamicSharedMemorySize, OP_LDS);
  char* ws = (char*)d_ws;
  k_attn<<<256, 768, LDS_BYTES, stream>>>((const u16*)d_in[0], (const u16*)d_in[1], (const u16*)d_in[2], (const u16*)d_in[3],
                                          (const u16*)d_in[5], (const u16*)d_in[6], (const u16*)d_in[7], (const u16*)d_in[8], ws);
  k_oproj<<<256, 768, OP_LDS, stream>>>(ws, (const u16*)d_in[4], (const u16*)d_in[9], (u16*)d_out);
}
```

```cpp
#define OST_POL ""
#include <hip/hip_runtime.h>
#include <stdint.h>

typedef short    bf16x8 __attribute__((ext_vector_type(8)));
typedef short    s16x4  __attribute__((ext_vector_type(4)));
typedef float    f32x4  __attribute__((ext_vector_type(4)));
typedef float    f32x2  __attribute__((ext_vector_type(2)));
typedef __bf16   bf16x2 __attribute__((ext_vector_type(2)));
typedef unsigned u32x4  __attribute__((ext_vector_type(4)));
typedef unsigned u32x2  __attribute__((ext_vector_type(2)));
typedef unsigned short u16;
#define WARMPOL ""
#define NOKROT
#define WNT false
#define XPOL ""
#define KVSTART 14
#define KVNT_ true
#define DI __device__ __forceinline__
#define LAS __attribute__((address_space(3)))
#define MFMA16(a, b, c) __builtin_amdgcn_mfma_f32_16x16x32_bf16(a, b, c, 0, 0, 0)
#define SBAR() __builtin_amdgcn_sched_barrier(0)
#define PIN(x) asm volatile("" : "+v"(x))
#define MFMA16_V(acc, a, b) asm volatile("v_mfma_f32_16x16x32_bf16 %0, %1, %2, %0" : "+v"(acc) : "v"(a), "v"(b))
#define MFMA16_Z(d, a, b) asm volatile("v_mfma_f32_16x16x32_bf16 %0, %1, %2, 0" : "=&v"(d) : "v"(a), "v"(b))
#define MFMA16_C(d, a, b, cc) asm volatile("v_mfma_f32_16x16x32_bf16 %0, %1, %2, %3" : "=&v"(d) : "v"(a), "v"(b), "v"(cc))

constexpr int NB = 16, SQ = 144, DM = 576, NH = 32, SK = 512, DH = 128, HD = NH * DH;
constexpr int ITEM_B = 9 * 4 * 1024;
constexpr size_t WS_O = 0;
constexpr size_t WS_X = (size_t)1024 * ITEM_B, WS_F = WS_X + (size_t)256 * 2 * 25 * 1024;
constexpr float QSCALE = 0.08838834764831845f * 1.4426950408889634f;

DI unsigned rfl(unsigned x) { return __builtin_amdgcn_readfirstlane(x); }
DI unsigned lane_id() { unsigned l; asm volatile("v_mbcnt_lo_u32_b32 %0, -1, 0\n\tv_mbcnt_hi_u32_b32 %0, -1, %0" : "=v"(l)); return l; }
DI unsigned cvtpk(float lo, float hi) { f32x2 v = {lo, hi}; bf16x2 b = __builtin_convertvector(v, bf16x2); return __builtin_bit_cast(unsigned, b); }
DI float fadd_s(float a, float b) { float r; asm("v_add_f32_e32 %0, %1, %2" : "=v"(r) : "v"(a), "v"(b)); return r; }
DI bf16x8 lds128(unsigned a) { return *(const LAS bf16x8*)a; }
DI s16x4 ldstr(unsigned a) { return __builtin_bit_cast(s16x4, __builtin_amdgcn_ds_read_tr16_b64_v4i16((LAS s16x4*)a)); }
DI bf16x8 cat8(s16x4 lo, s16x4 hi) { return __builtin_shufflevector(lo, hi, 0, 1, 2, 3, 4, 5, 6, 7); }
DI void dma16(unsigned m0v, unsigned voff, __amdgpu_buffer_rsrc_t srd, unsigned soff) {
  asm volatile("s_mov_b32 m0, %0\n\ts_nop 0\n\tbuffer_load_dwordx4 %1, %2, %3 offen lds" :: "s"(m0v), "v"(voff), "s"(srd), "s"(soff) : "memory");
}
DI void dma16nt(unsigned m0v, unsigned voff, __amdgpu_buffer_rsrc_t srd, unsigned soff) {
  asm volatile("s_mov_b32 m0, %0\n\ts_nop 0\n\tbuffer_load_dwordx4 %1, %2, %3 offen nt lds" :: "s"(m0v), "v"(voff), "s"(srd), "s"(soff) : "memory");
}
DI void dma4(unsigned m0v, unsigned voff, __amdgpu_buffer_rsrc_t srd) {
  asm volatile("s_mov_b32 m0, %0\n\ts_nop 0\n\tbuffer_load_dword %1, %2, 0 offen " WARMPOL " lds" :: "s"(m0v), "v"(voff), "s"(srd) : "memory");
}
#define TILE8_BODY(POL) \
  "s_mov_b32 m0, %1\n\ts_mov_b32 %0, %2\n\t" \
  "buffer_load_dwordx4 %3, %5, %0 offen " POL " lds\n\ts_add_u32 m0, m0, 896\n\ts_nop 0\n\tbuffer_load_dwordx4 %3, %5, %0 offen offset:128 " POL " lds\n\ts_add_u32 m0, m0, 1152\n\ts_add_u32 %0, %0, %6\n\t" \
  "buffer_load_dwordx4 %4, %5, %0 offen " POL " lds\n\ts_add_u32 m0, m0, 896\n\ts_nop 0\n\tbuffer_load_dwordx4 %4, %5, %0 offen offset:128 " POL " lds\n\ts_add_u32 m0, m0, 1152\n\ts_add_u32 %0, %0, %6\n\t" \
  "buffer_load_dwordx4 %3, %5, %0 offen " POL " lds\n\ts_add_u32 m0, m0, 896\n\ts_nop 0\n\tbuffer_load_dwordx4 %3, %5, %0 offen offset:128 " POL " lds\n\ts_add_u32 m0, m0, 1152\n\ts_add_u32 %0, %0, %6\n\t" \
  "buffer_load_dwordx4 %4, %5, %0 offen " POL " lds\n\ts_add_u32 m0, m0, 896\n\ts_nop 0\n\tbuffer_load_dwordx4 %4, %5, %0 offen offset:128 " POL " lds"
template <bool NT, int D2> DI void dma_tile8(unsigned lo, unsigned so, unsigned va, unsigned vb, __amdgpu_buffer_rsrc_t srd) {
  unsigned t;
  if constexpr (NT) asm volatile(TILE8_BODY("nt") : "=&s"(t) : "s"(lo), "s"(so), "v"(va), "v"(vb), "s"(srd), "i"(D2) : "memory", "scc");
  else              asm volatile(TILE8_BODY("")   : "=&s"(t) : "s"(lo), "s"(so), "v"(va), "v"(vb), "s"(srd), "i"(D2) : "memory", "scc");
}
template <int D2> DI void dma_tile9(unsigned lo, unsigned so, unsigned va, __amdgpu_buffer_rsrc_t srd) {
  unsigned t;
  asm volatile("s_mov_b32 m0, %1\n\ts_mov_b32 %0, %2\n\t.rept 9\n\t"
               "buffer_load_dwordx4 %3, %4, %0 offen " XPOL " lds\n\ts_add_u32 m0, m0, 2048\n\ts_add_u32 %0, %0, %5\n\t.endr"
               : "=&s"(t) : "s"(lo), "s"(so), "v"(va), "s"(srd), "i"(D2) : "memory", "scc");
}
#define WAITV(n) asm volatile("s_waitcnt vmcnt(" #n ")" ::: "memory")
#define BAR() do { asm volatile("" ::: "memory"); __builtin_amdgcn_s_waitcnt(0xC07F); asm volatile("s_barrier" ::: "memory"); } while (0)
#define RBAR() asm volatile("s_barrier" ::: "memory")
constexpr unsigned GRP = 65536, WRING = 3 * 8192, XSLOT = 18432, XB = 126976, XA = XB + XSLOT, LDS_BYTES = 163840;
constexpr unsigned kslot(int i) { return 8192 * ((i & 3) == 0 ? 0 : (i & 3) == 1 ? 1 : (i & 3) == 2 ? 5 : 3); }
constexpr unsigned vslot(int i) { return 8192 * ((i & 3) == 0 ? 2 : (i & 3) == 1 ? 6 : (i & 3) == 2 ? 4 : 7); }
static_assert(XA + XSLOT == LDS_BYTES, "LDS map");

#define STAMP2(k)
#define STAMP(k)
#define STAMPW(k)

DI float red_g_max(float m) {
  auto r = __builtin_amdgcn_permlane32_swap(__float_as_uint(m), __float_as_uint(m), false, false);
  m = fmaxf(__uint_as_float(r[0]), __uint_as_float(r[1]));
  auto s = __builtin_amdgcn_permlane16_swap(__float_as_uint(m), __float_as_uint(m), false, false);
  return fmaxf(__uint_as_float(s[0]), __uint_as_float(s[1]));
}
DI float red_g_sum(float m) {
  auto r = __builtin_amdgcn_permlane32_swap(__float_as_uint(m), __float_as_uint(m), false, false);
  m = __uint_as_float(r[0]) + __uint_as_float(r[1]);
  auto s = __builtin_amdgcn_permlane16_swap(__float_as_uint(m), __float_as_uint(m), false, false);
  return __uint_as_float(s[0]) + __uint_as_float(s[1]);
}

template <int NQ> struct AttC {
  f32x4 O[NQ][8]; bf16x8 Q[NQ][4]; f32x4 S[2][NQ][2]; f32x4 negm[NQ]; float l[NQ];
  unsigned kb, vb0, vb1;
};
template <int NQ> struct Tab;
template <> struct Tab<2> {
  static constexpr int NPRE = 6;
  static constexpr int pre[6] = {100, 101, 102, 103, 104, 105};
  static constexpr int gx[16][2] = {{106, 0}, {107, 0}, {200, 201}, {110, 0}, {202, 203}, {111, 0}, {112, 0}, {113, 0}, {114, 0}, {210, 211}, {115, 0}, {116, 0}, {117, 0}, {300, 310}, {212, 213}, {301, 311}};
  static constexpr int gy[16] = {302, 312, 303, 313, 304, 314, 305, 315, 306, 316, 307, 317, 0, 0, 0, 0};
  static constexpr int all[24] = {100, 101, 102, 103, 104, 105, 106, 107, 110, 200, 201, 202, 203, 111, 112, 113, 114, 115, 116, 117, 210, 211, 212, 213};
  static constexpr int ady[16] = {300, 310, 301, 311, 302, 312, 303, 313, 304, 314, 305, 315, 306, 316, 307, 317};
};
template <> struct Tab<1> {
  static constexpr int NPRE = 4;
  static constexpr int pre[4] = {100, 101, 102, 103};
  static constexpr int gx[8][2] = {{104, 0}, {105, 0}, {200, 201}, {106, 0}, {107, 0}, {300, 301}, {202, 203}, {302, 303}};
  static constexpr int gy[8] = {304, 305, 306, 307, 0, 0, 0, 0};
  static constexpr int all[12] = {100, 101, 102, 103, 104, 105, 106, 107, 200, 201, 202, 203};
  static constexpr int ady[8] = {300, 301, 302, 303, 304, 305, 306, 307};
};
template <int NQ, int P, bool QK> struct Step {
  static constexpr int X = P & 1;
  static constexpr unsigned ks_ = kslot(P + 1), vs_ = vslot(P);
  bf16x8 fr[4]; float e[NQ][8]; unsigned pw[NQ][4]; u32x4 Pw[NQ];
  template <int OP> DI void op(AttC<NQ>& c) {
    constexpr int ty = OP / 100, qt = (OP / 10) % 10, j = OP % 10;
    if constexpr (ty == 1) { e[qt][j] = c.S[X][qt][j >> 2][j & 3]; asm volatile("v_exp_f32 %0, %0" : "+v"(e[qt][j])); }
    if constexpr (ty == 2) asm volatile("v_cvt_pk_bf16_f32 %0, %1, %2" : "=v"(pw[qt][j]) : "v"(e[qt][2 * j]), "v"(e[qt][2 * j + 1]));
    if constexpr (ty == 3) asm volatile("v_add_f32 %0, %0, %1" : "+v"(c.l[qt]) : "v"(e[qt][j]));
  }
  template <int R> DI void rd(AttC<NQ>& c) {
    if constexpr (R < 8) fr[R & 3] = lds128(c.kb + ks_ + 4096 * (R & 1) + 512 * (R >> 1));
    else if constexpr (R < 16) {
      constexpr int dt = R - 8;
      const unsigned vb = ((dt & 1) ? c.vb1 : c.vb0) + vs_ + 512 * (dt >> 1);
      fr[R & 3] = cat8(ldstr(vb), ldstr(vb + 4096));
    }
    SBAR();
  }
  template <int I> DI void gx(AttC<NQ>& c) {
    constexpr int K = I / NQ, qt = I % NQ, s = K >> 1, kt = K & 1;
    if constexpr (s == 0) MFMA16_C(c.S[X ^ 1][qt][kt], fr[K & 3], c.Q[qt][s], c.negm[qt]); else MFMA16_V(c.S[X ^ 1][qt][kt], fr[K & 3], c.Q[qt][s]);
    if constexpr (qt == NQ - 1) { SBAR(); rd<K + 4>(c); }
    op<Tab<NQ>::gx[I][0]>(c); op<Tab<NQ>::gx[I][1]>(c);
    if constexpr (I + 1 < 8 * NQ) gx<I + 1>(c);
  }
  template <int I> DI void gy(AttC<NQ>& c) {
    constexpr int Dt = I / NQ, qt = I % NQ;
    MFMA16_V(c.O[qt][Dt], fr[Dt & 3], Pw[qt]);
    if constexpr (qt == NQ - 1) { SBAR(); rd<Dt + 12>(c); }
    op<(QK ? Tab<NQ>::gy[I] : Tab<NQ>::ady[I])>(c);
    if constexpr (I + 1 < 8 * NQ) gy<I + 1>(c);
  }
  template <int I> DI void pre(AttC<NQ>& c) {
    op<(QK ? Tab<NQ>::pre[I < Tab<NQ>::NPRE ? I : 0] : Tab<NQ>::all[I])>(c);
    if constexpr (I + 1 < (QK ? Tab<NQ>::NPRE : 12 * NQ)) pre<I + 1>(c);
  }
  DI void run(AttC<NQ>& c) {
    if constexpr (QK) {
      rd<0>(c); rd<1>(c); rd<2>(c); rd<3>(c);
      pre<0>(c);
      gx<0>(c);
    } else {
      rd<8>(c); rd<9>(c); rd<10>(c); rd<11>(c);
      pre<0>(c);
      asm volatile("s_nop 1");
    }
#pragma unroll
    for (int qt = 0; qt < NQ; ++qt) Pw[qt] = u32x4{pw[qt][0], pw[qt][1], pw[qt][2], pw[qt][3]};
    gy<0>(c);
  }
};

template <int NQ, int P, bool QK> DI void att_step(AttC<NQ>& c) { BAR(); SBAR(); Step<NQ, P, QK> s; s.run(c); SBAR(); }

template <int NQ> DI void att_prologue(AttC<NQ>& c) {
#pragma unroll
  for (int s = 0; s < 4; ++s)
#pragma unroll
    for (int kt = 0; kt < 2; ++kt) {
      const bf16x8 kf = lds128(c.kb + 4096 * kt + 512 * s);
#pragma unroll
      for (int qt = 0; qt < NQ; ++qt) c.S[0][qt][kt] = MFMA16(kf, c.Q[qt][s], s == 0 ? (f32x4{0, 0, 0, 0}) : c.S[0][qt][kt]);
    }
#pragma unroll
  for (int qt = 0; qt < NQ; ++qt) {
    float mx = c.S[0][qt][0][0];
#pragma unroll
    for (int j = 1; j < 8; ++j) mx = fmaxf(mx, c.S[0][qt][j >> 2][j & 3]);
    mx = red_g_max(mx);
    c.negm[qt] = f32x4{-mx, -mx, -mx, -mx};
    c.S[0][qt][0] += c.negm[qt]; c.S[0][qt][1] += c.negm[qt];
    c.l[qt] = 0.f;
#pragma unroll
    for (int dt = 0; dt < 8; ++dt) c.O[qt][dt] = f32x4{0, 0, 0, 0};
  }
}
template <int NQ> DI void att_redo_true_max(AttC<NQ>& c, const u16* __restrict__ kh, const u16* __restrict__ vh) {
  const unsigned lane = lane_id(), li = lane & 15, g = lane >> 4;
  const u16* kp = kh + li * DH + g * 8;
  float mx[NQ];
#pragma unroll
  for (int qt = 0; qt < NQ; ++qt) mx[qt] = -__builtin_inff();
#pragma unroll 1
  for (int kt = 0; kt < SK / 16; ++kt) {
    f32x4 s4[NQ];
#pragma unroll
    for (int qt = 0; qt < NQ; ++qt) s4[qt] = f32x4{0, 0, 0, 0};
#pragma unroll
    for (int s = 0; s < 4; ++s) {
      const bf16x8 kf = *(const bf16x8*)(kp + kt * 16 * DH + s * 32);
#pragma unroll
      for (int qt = 0; qt < NQ; ++qt) s4[qt] = MFMA16(kf, c.Q[qt][s], s4[qt]);
    }
#pragma unroll
    for (int qt = 0; qt < NQ; ++qt) mx[qt] = fmaxf(fmaxf(fmaxf(mx[qt], s4[qt][0]), fmaxf(s4[qt][1], s4[qt][2])), s4[qt][3]);
  }
#pragma unroll
  for (int qt = 0; qt < NQ; ++qt) {
    mx[qt] = red_g_max(mx[qt]); c.l[qt] = 0.f;
#pragma unroll
    for (int dt = 0; dt < 8; ++dt) c.O[qt][dt] = f32x4{0, 0, 0, 0};
  }
#pragma unroll 1
  for (int st = 0; st < SK / 32; ++st) {
    u32x4 pw[NQ];
#pragma unroll
    for (int kt = 0; kt < 2; ++kt) {
      f32x4 s4[NQ];
#pragma unroll
      for (int qt = 0; qt < NQ; ++qt) s4[qt] = f32x4{-mx[qt], -mx[qt], -mx[qt], -mx[qt]};
#pragma unroll
      for (int s = 0; s < 4; ++s) {
        const bf16x8 kf = *(const bf16x8*)(kp + (2 * st + kt) * 16 * DH + s * 32);
#pragma unroll
        for (int qt = 0; qt < NQ; ++qt) s4[qt] = MFMA16(kf, c.Q[qt][s], s4[qt]);
      }
#pragma unroll
      for (int qt = 0; qt < NQ; ++qt) {
        float e[4];
#pragma unroll
        for (int j = 0; j < 4; ++j) { e[j] = __builtin_amdgcn_exp2f(s4[qt][j]); c.l[qt] += e[j]; }
        pw[qt][2 * kt] = cvtpk(e[0], e[1]); pw[qt][2 * kt + 1] = cvtpk(e[2], e[3]);
      }
    }
    const u16* vp = vh + (32 * st + 4 * g) * DH + li;
#pragma unroll
    for (int dt = 0; dt < 8; ++dt) {
      bf16x8 vf;
#pragma unroll
      for (int j = 0; j < 8; ++j) vf[j] = (short)vp[(16 * (j >> 2) + (j & 3)) * DH + 16 * dt];
#pragma unroll
      for (int qt = 0; qt < NQ; ++qt) c.O[qt][dt] = MFMA16(vf, __builtin_bit_cast(bf16x8, pw[qt]), c.O[qt][dt]);
      SBAR();
    }
  }
#pragma unroll
  for (int qt = 0; qt < NQ; ++qt) c.l[qt] = red_g_sum(c.l[qt]);
}
template <int NQ> DI void att_epilogue(AttC<NQ>& c, char* ob, const u16* kh, const u16* vh) {
  bool bad = false;
#pragma unroll
  for (int qt = 0; qt < NQ; ++qt) { c.l[qt] = red_g_sum(c.l[qt]); bad |= !(c.l[qt] < 16777216.0f); }
  if (__builtin_expect(__any(bad), 0)) att_redo_true_max<NQ>(c, kh, vh);
#pragma unroll
  for (int qt = 0; qt < NQ; ++qt) {
    const float inv = __builtin_amdgcn_rcpf(c.l[qt]);
#pragma unroll
    for (int s = 0; s < 4; ++s) {
      const f32x4 a = c.O[qt][2 * s] * inv, bb = c.O[qt][2 * s + 1] * inv;
      u32x4 w = {cvtpk(a[0], a[1]), cvtpk(a[2], a[3]), cvtpk(bb[0], bb[1]), cvtpk(bb[2], bb[3])};
      asm volatile("global_store_dwordx4 %0, %1, off " OST_POL "\n\ts_nop 1" :: "v"(ob + (qt * 4 + s) * 1024), "v"(w) : "memory");
    }
  }
}

DI bf16x8 q_frag(const f32x4& t0, const f32x4& t1) {
  const f32x4 u0 = t0 * QSCALE, u1 = t1 * QSCALE;
  unsigned a0 = cvtpk(u0[0], u0[1]), a1 = cvtpk(u0[2], u0[3]), b0 = cvtpk(u1[0], u1[1]), b1 = cvtpk(u1[2], u1[3]);
  auto r0 = __builtin_amdgcn_permlane32_swap(a0, b0, false, false);
  auto r1 = __builtin_amdgcn_permlane32_swap(a1, b1, false, false);
  auto s0 = __builtin_amdgcn_permlane16_swap(r0[0], r0[1], false, false);
  auto s1 = __builtin_amdgcn_permlane16_swap(r1[0], r1[1], false, false);
  u32x4 w = {s0[0], s1[0], s0[1], s1[1]};
  return __builtin_bit_cast(bf16x8, w);
}
template <int NQ, int WS, bool RX, bool Z = false> DI void q_unit(f32x4 (&qa)[NQ][8], const bf16x8 (&wc)[8], bf16x8 (&wn)[8], const bf16x8 (&xc)[NQ], bf16x8 (&xn)[2][NQ], unsigned w0, unsigned w1, unsigned xa) {
#pragma unroll
  for (int i = 0; i < 8 * NQ; ++i) {
    const int dt = i / NQ, q = i % NQ;
    if constexpr (Z) MFMA16_Z(qa[q][dt], wc[dt], xc[q]); else MFMA16_V(qa[q][dt], wc[dt], xc[q]);
    SBAR();
#pragma unroll
    for (int u = i * (3 - NQ); u < (i + 1) * (3 - NQ); ++u) {
      if (WS >= 0 && u < 8) { const unsigned a = ((u & 1) ? w1 : w0) + 8192 * WS + 512 * (u >> 1); wn[u] = cat8(ldstr(a), ldstr(a + 256)); }
      if (RX && u >= 8 && u < 8 + 2 * NQ) xn[(u - 8) / NQ][(u - 8) % NQ] = lds128(xa + ((u - 8) % NQ) * 2048 + ((u - 8) / NQ) * 512);
    }
    SBAR();
  }
}

template <int NQ> DI void att_wave(char* ows_pair, const u16* kbase, const u16* vbase, unsigned qt0, unsigned m, unsigned hg0, unsigned lb) {
  asm volatile("; attention wave NQ=%0" :: "i"(NQ) : "memory");
  STAMP(20);
#pragma unroll 1
  for (int r = 0; r < 2; ++r) {
    AttC<NQ> c;
    STAMP(4 * r);
    {
      const unsigned lane = lane_id(), li = lane & 15, g = lane >> 4, pos = lane & 3, tq = li >> 2;
      unsigned w0 = lb + WRING + 2048 * g + 64 * tq + 32 * (g & 1) + 8 * pos, w1 = w0 ^ 32u;
      unsigned xb = XB + qt0 * 2048 + 1024 * (li >> 3) + 64 * (li & 7) + 16 * (g ^ (2 * (li >> 3)));
      bf16x8 w[8], v[8], x0[2][NQ], x1[2][NQ];
      f32x4 qa[NQ][8];
      PIN(w0); PIN(w1); PIN(xb);
      BAR();
      if (r == 0) STAMP(21);
#pragma unroll
      for (int q = 0; q < 2 * NQ; ++q) x0[q / NQ][q % NQ] = lds128(xb + XSLOT + (q % NQ) * 2048 + (q / NQ) * 512);
#pragma unroll
      for (int dt = 0; dt < 8; ++dt) { const unsigned a = ((dt & 1) ? w1 : w0) + 512 * (dt >> 1); w[dt] = cat8(ldstr(a), ldstr(a + 256)); }
      BAR(); q_unit<NQ, 1, false, true>(qa, w, v, x0[0], x1, w0, w1, 0);
#pragma unroll 1
      for (int n = 0; n < 4; ++n) {
        BAR(); if (r == 0 && n == 1) STAMP(22); if (r == 0 && n == 3) STAMP(23);
               q_unit<NQ, 2, true>(qa, v, w, x0[1], x1, w0, w1, xb);
        BAR(); q_unit<NQ, 3, false>(qa, w, v, x1[0], x0, w0, w1, 0);
        BAR(); q_unit<NQ, 0, true>(qa, v, w, x1[1], x0, w0, w1, xb + XSLOT);
        BAR(); q_unit<NQ, 1, false>(qa, w, v, x0[0], x1, w0, w1, 0);
      }
      BAR(); q_unit<NQ, -1, false>(qa, v, w, x0[1], x1, 0, 0, 0);
      c.kb = lb + 2048 * (li >> 3) + 64 * (li & 7) + 16 * (g ^ (2 * (li >> 3)));
      c.vb0 = lb + 2048 * (g >> 1) + 64 * (4 * (g & 1) + tq) + 32 * (g & 1) + 8 * pos; c.vb1 = c.vb0 ^ 32u;
      PIN(c.kb); PIN(c.vb0); PIN(c.vb1);
      asm volatile("s_nop 7" ::: "memory"); SBAR();
#pragma unroll
      for (int q = 0; q < NQ; ++q)
#pragma unroll
        for (int s = 0; s < 4; ++s) c.Q[q][s] = q_frag(qa[q][2 * s], qa[q][2 * s + 1]);
    }
    STAMP(4 * r + 1);
    att_prologue<NQ>(c);
    SBAR();
#pragma unroll 1
    for (int ii = 0; ii < 3; ++ii) { att_step<NQ, 0, true>(c); att_step<NQ, 1, true>(c); att_step<NQ, 2, true>(c); att_step<NQ, 3, true>(c); }
    att_step<NQ, 0, true>(c); att_step<NQ, 1, true>(c); att_step<NQ, 2, true>(c); att_step<NQ, 3, false>(c);
    STAMP(4 * r + 2);
    asm volatile("s_nop 7\n\ts_nop 1" ::: "memory"); SBAR();
    const unsigned h = 8 * ((hg0 + 2 * r) & 3) + m;
    att_epilogue<NQ>(c, ows_pair + (size_t)h * ITEM_B + qt0 * 4096 + lane_id() * 16, kbase + (size_t)h * SK * DH, vbase + (size_t)h * SK * DH);
    STAMP(4 * r + 3);
  }
}

DI void wait_newer(int n) {
  if (n >= 32) { if (n >= 40) WAITV(40); else WAITV(32); }
  else if (n >= 16) { if (n >= 24) WAITV(24); else WAITV(16); }
  else if (n >= 8) WAITV(8); else WAITV(0);
}
DI void loader(const u16* wq, const u16* x, const u16* kbase, const u16* vbase, unsigned m, unsigned hg0, unsigned e0, unsigned half) {
  const unsigned lane = lane_id(), row7 = (lane >> 2) & 7, sh = lane >> 5, pos = lane & 3, lb = half * GRP;
  const __amdgpu_buffer_rsrc_t srdW = __builtin_amdgcn_make_buffer_rsrc((void*)wq, (short)0, DM * HD * 2, 0x00020000);
  const __amdgpu_buffer_rsrc_t srdX = __builtin_amdgcn_make_buffer_rsrc((void*)x, (short)0, SQ * DM * 2, 0x00020000);
  const __amdgpu_buffer_rsrc_t srdK = __builtin_amdgcn_make_buffer_rsrc((void*)kbase, (short)0, NH * SK * DH * 2, 0x00020000);
  const __amdgpu_buffer_rsrc_t srdV = __builtin_amdgcn_make_buffer_rsrc((void*)vbase, (short)0, NH * SK * DH * 2, 0x00020000);
  const unsigned vw0 = row7 * (HD * 2) + sh * 64 + pos * 16, vw1 = vw0 ^ 32u;
  const unsigned vx = (row7 * (DM * 2) + sh * 64 + pos * 16) ^ (32u * half);
  const unsigned vk0 = row7 * 256 + sh * 64 + pos * 16, vk1 = vk0 ^ 32u, vv = row7 * 256 + sh * 64 + ((pos ^ (2 * (row7 >> 2))) * 16);
  int cnt = 0;
  auto issue_w = [&](unsigned h, int s) {
    int ks = s + 2 * e0; if (ks >= 18) ks -= 18;
    dma_tile8<WNT, 8 * HD * 2>(lb + WRING + (s & 3) * 8192, ks * 32 * (HD * 2) + h * (DH * 2), vw0, vw1, srdW);
    return cnt += 8;
  };
  auto issue_x = [&](int e) {
    int ee = e + e0; if (ee >= 9) ee -= 9;
    dma_tile9<16 * DM * 2>(((e & 1) ? XB : XA) + half * 1024, ee * 128 + half * 8 * (DM * 2), vx, srdX);
    return cnt += 9;
  };
  auto issue_k = [&](unsigned h, int t) {
    const unsigned q = t & 3, lo = q == 0 ? kslot(0) : q == 1 ? kslot(1) : q == 2 ? kslot(2) : kslot(3);
    dma_tile8<KVNT_, 2048>(lb + lo, h * (SK * DH * 2) + t * 8192, vk0, vk1, srdK);
    return cnt += 8;
  };
  auto issue_v = [&](unsigned h, int t) {
    const unsigned q = t & 3, lo = q == 0 ? vslot(0) : q == 1 ? vslot(1) : q == 2 ? vslot(2) : vslot(3);
    dma_tile8<KVNT_, 2048>(lb + lo, h * (SK * DH * 2) + t * 8192, vv, vv, srdV);
    return cnt += 8;
  };
  unsigned h = 8 * hg0 + m;
  int n0, n1, n2, n3;
  issue_w(h, 0); n0 = issue_x(0); n1 = issue_w(h, 1); issue_w(h, 2);
#pragma unroll 1
  for (int r = 0; r < 2; ++r) {
    const unsigned hn = 8 * ((hg0 + 2) & 3) + m;
    wait_newer(cnt - n0); RBAR();
    n2 = issue_x(1);
    if (r == 0) issue_w(h, 3);
    n3 = cnt;
#pragma unroll 1
    for (int G = 1; G < 15; ++G) {
      wait_newer(cnt - n1); RBAR();
      n1 = n2; n2 = n3;
      if (G & 1) issue_x((G + 3) >> 1);
      n3 = issue_w(h, G + 3);
      if (r == 0 && G == KVSTART) { issue_k(h, 0); issue_k(h, 1); issue_v(h, 0); }
    }
    int a0 = cnt, a1, a2;
    wait_newer(cnt - n1); RBAR();
    issue_k(h, 2);
    wait_newer(cnt - n2); RBAR();
    a1 = issue_v(h, 1);
    wait_newer(cnt - n3); RBAR();
    issue_k(h, 3);
    wait_newer(cnt - a0); RBAR();
    a2 = issue_v(h, 2); issue_v(h, 3);
#pragma unroll 1
    for (int i = 0; i < 16; ++i) {
      wait_newer(cnt - a0); RBAR();
      a0 = a1; a1 = a2;
      if (i < 12) issue_k(h, i + 4);
      if (i >= 1 && i < 13) issue_v(h, i + 3);
      a2 = cnt;
      if (r == 0) {
        if (i == 12) issue_k(hn, 0);
        if (i == 13) { issue_k(hn, 1); issue_v(hn, 0); }
        if (i == 14) { issue_x(0); issue_w(hn, 2); issue_w(hn, 3); }
        if (i == 15) { n0 = issue_w(hn, 0); n1 = issue_w(hn, 1); }
      }
    }
    h = hn;
  }
}

__global__ __launch_bounds__(768, 3) void k_attn(const u16* __restrict__ xA, const u16* __restrict__ kA, const u16* __restrict__ vA, const u16* __restrict__ wqA,
                                                 const u16* __restrict__ xB, const u16* __restrict__ kB, const u16* __restrict__ vB, const u16* __restrict__ wqB,
                                                 char* __restrict__ ows) {
  extern __shared__ __attribute__((aligned(16))) char smem[];
  const unsigned wid12 = rfl(threadIdx.x >> 6), half = wid12 >= 6, wid = wid12 - 6 * half, lb = half * GRP;
  const unsigned blk = blockIdx.x, idx = blk >> 3, pair = (blk & 7) * 4 + (idx >> 3), m = idx & 7, br = pair >> 4, b = pair & 15;
  const unsigned hg0 = (blk + half) & 3;
  char* op = ows + (size_t)pair * NH * ITEM_B;
  const u16* kb = (br ? kB : kA) + (size_t)b * NH * SK * DH; const u16* vb = (br ? vB : vA) + (size_t)b * NH * SK * DH;
  if (wid < 4) att_wave<2>(op, kb, vb, 2 * wid, m, hg0, lb);
  else if (wid == 4) {
    if (lane_id() == 0) ((unsigned*)(ows + WS_F))[(blk * 2 + half) * 16] = 0u;
    att_wave<1>(op, kb, vb, 8, m, hg0, lb);
  }
  else {
    const unsigned e0 = 0;
    loader(br ? wqB : wqA, (br ? xB : xA) + (size_t)b * SQ * DM, kb, vb, m, hg0, e0, half);

  }
}

constexpr unsigned OP_SLOT = 36864, OP_LDS = 4 * OP_SLOT;
template <int NQT, int NNT> struct OSet { bf16x8 w[NNT], ol; };
template <int NQT, int NNT, bool RD, bool Z> DI void o_unit(f32x4 (&acc)[NQT][NNT], bf16x8 (&of)[NQT - 1], const OSet<NQT, NNT>& cs, OSet<NQT, NNT>& ns, unsigned oa, unsigned wa) {
#pragma unroll
  for (int q = 0; q < NQT; ++q) {
#pragma unroll
    for (int n = 0; n < NNT; ++n) {
      if constexpr (Z) MFMA16_Z(acc[q][n], cs.w[n], q < NQT - 1 ? of[q < NQT - 1 ? q : 0] : cs.ol); else MFMA16_V(acc[q][n], cs.w[n], q < NQT - 1 ? of[q < NQT - 1 ? q : 0] : cs.ol);
      SBAR();
      if (RD && q == 0) ns.w[n] = cat8(ldstr(wa + 32 * n), ldstr(wa + 32 * n + 16 * 288));
      if (RD && q == 1 && n == 1) ns.ol = lds128(oa + (NQT - 1) * 1024);
      SBAR();
    }
    if (RD && q < NQT - 1) { of[q] = lds128(oa + q * 1024); SBAR(); }
  }
}
template <int NQT, int NNT> DI void oproj_consumer(u16* __restrict__ outp, char* __restrict__ wsb, unsigned kh, unsigned kh2, unsigned blk) {
  asm volatile("; oproj consumer %0 x %1" :: "i"(NQT), "i"(NNT) : "memory");
  constexpr unsigned qh = NQT == 4, nh = NNT == 4, q0 = 5 * qh, n0 = 5 * nh;
  f32x4 acc[NQT][NNT];
  bf16x8 of[NQT - 1]; OSet<NQT, NNT> sa, sb;
  unsigned oa, wa;
  { const unsigned lane = lane_id(), li = lane & 15, g = lane >> 4;
    oa = kh * 18432 + q0 * 1024 + lane * 16; wa = kh * 18432 + 9216 + 288 * (4 * g + (li >> 2)) + 8 * (lane & 3) + 32 * n0; }
  int sd = 2 * OP_SLOT;
#define FLIP() do { oa += sd; wa += sd; sd = -sd; PIN(oa); PIN(wa); } while (0)
  PIN(oa); PIN(wa);
  STAMP2(24);
  BAR();
  STAMP2(25);
#pragma unroll
  for (int n = 0; n < NNT; ++n) sa.w[n] = cat8(ldstr(wa + 32 * n), ldstr(wa + 32 * n + 16 * 288));
#pragma unroll
  for (int q = 0; q < NQT - 1; ++q) of[q] = lds128(oa + q * 1024);
  sa.ol = lds128(oa + (NQT - 1) * 1024);
  BAR(); o_unit<NQT, NNT, true, true>(acc, of, sa, sb, oa + OP_SLOT, wa + OP_SLOT); FLIP();
#pragma unroll 1
  for (int j = 2; j < 32; j += 2) {
    BAR(); o_unit<NQT, NNT, true, false>(acc, of, sb, sa, oa, wa);
    BAR(); o_unit<NQT, NNT, true, false>(acc, of, sa, sb, oa + OP_SLOT, wa + OP_SLOT); FLIP();
  }
  BAR();
  o_unit<NQT, NNT, false, false>(acc, of, sb, sa, 0, 0);
  asm volatile("s_nop 7\n\ts_nop 1" ::: "memory"); SBAR();
  STAMP2(26);
  const unsigned lane = lane_id(), li = lane & 15, g = lane >> 4;
  unsigned ra = (2 * qh + nh) * 25600 + lane * 16; PIN(ra);
  if (kh) {
#pragma unroll
    for (int q = 0; q < NQT; ++q)
#pragma unroll
      for (int n = 0; n < NNT; ++n) *(LAS f32x4*)(ra + (q * NNT + n) * 1024) = acc[q][n];
  }
  BAR();
  if (kh) return;
#pragma unroll
  for (int q = 0; q < NQT; ++q)
#pragma unroll
    for (int n = 0; n < NNT; ++n) acc[q][n] += *(const LAS f32x4*)(ra + (q * NNT + n) * 1024);
  if (qh != kh2) {
    char* xp = wsb + WS_X + (size_t)(blk * 2 + nh) * 25600 + lane * 16;
#pragma unroll
    for (int q = 0; q < NQT; ++q)
#pragma unroll
      for (int n = 0; n < NNT; ++n) asm volatile("global_store_dwordx4 %0, %1, off sc1\n\ts_nop 1" :: "v"(xp + (q * NNT + n) * 1024), "v"(acc[q][n]) : "memory");
    asm volatile("s_waitcnt vmcnt(0)" ::: "memory");
  }
  RBAR();
  if (qh != kh2) {
    if (nh == 0 && lane == 0) __hip_atomic_store((unsigned*)(wsb + WS_F) + blk * 32, 1u, __ATOMIC_RELAXED, __HIP_MEMORY_SCOPE_AGENT);
    return;
  }
  STAMP2(27);
  const unsigned pb = blk ^ 1u;
  { unsigned* fl = (unsigned*)(wsb + WS_F) + pb * 32;
    for (int spin = 0; spin < (1 << 22) && __hip_atomic_load(fl, __ATOMIC_RELAXED, __HIP_MEMORY_SCOPE_AGENT) != 1u; ++spin) __builtin_amdgcn_s_sleep(1);
    __builtin_amdgcn_fence(__ATOMIC_ACQUIRE, "wavefront"); }
  STAMP2(28);
  const __amdgpu_buffer_rsrc_t srdP = __builtin_amdgcn_make_buffer_rsrc((void*)(wsb + WS_X + (size_t)(pb * 2 + nh) * 25600), (short)0, 25600, 0x00020000);
  constexpr unsigned RB = NNT * 32 + 16;
  unsigned ta = (2 * qh + nh) * 25600 + li * RB + g * 8; PIN(ta);
  f32x4 px[NQT * NNT > 16 ? 13 : 16];
#pragma unroll
  for (int q = 0; q < NQT; ++q)
#pragma unroll
    for (int n = 0; n < NNT; ++n) {
      constexpr int NB_ = NQT * NNT > 16 ? 13 : 16;
      const int t = q * NNT + n;
      if (t % NB_ == 0) {
#pragma unroll
        for (int u = 0; u < NB_; ++u) if (t + u < NQT * NNT) px[u] = __builtin_bit_cast(f32x4, __builtin_amdgcn_raw_buffer_load_b128(srdP, lane * 16, (t + u) * 1024, 16));
        SBAR();
      }
      const f32x4 a = acc[q][n] + px[t % NB_];
      *(LAS u32x2*)(ta + q * 16 * RB + n * 32) = u32x2{cvtpk(a[0], a[1]), cvtpk(a[2], a[3])};
    }
  constexpr unsigned CPR = NNT * 2, NCH = NQT * 16 * CPR;
#pragma unroll
  for (unsigned i = 0; i < (NCH + 63) / 64; ++i) {
    const unsigned ch = i * 64 + lane, row = ch / CPR, cc = ch - row * CPR;
    if (ch < NCH) *(u32x4*)(outp + (size_t)(16 * q0 + row) * (2 * DM) + 16 * n0 + cc * 8) = *(const LAS u32x4*)((2 * qh + nh) * 25600 + row * RB + cc * 16);
  }
  STAMP2(29);
}
DI void oproj_loader(const char* op, const u16* wo, unsigned nq, unsigned kh2, unsigned l, unsigned rot) {
  const unsigned lane = lane_id(), kh = l >> 1, isw = l & 1;
  const __amdgpu_buffer_rsrc_t srd = isw ? __builtin_amdgcn_make_buffer_rsrc((void*)wo, (short)0, HD * DM * 2, 0x00020000)
                                         : __builtin_amdgcn_make_buffer_rsrc((void*)op, (short)0, NH * ITEM_B, 0x00020000);
  unsigned vo[9];
#pragma unroll
  for (int p = 0; p < 9; ++p) { const unsigned n = 64 * p + lane, row = n / 18; vo[p] = isw ? row * (DM * 2) + (n - 18 * row) * 16 + nq * 288 : p * 4096 + lane * 16; }
  auto issue = [&](int j) {
    const unsigned h = 16 * kh2 + (((j >> 1) + rot) & 15), s = 2 * (j & 1) + kh;
    const unsigned so = isw ? (h * 4 + s) * 32 * (DM * 2) : h * ITEM_B + s * 1024, lo = (j & 3) * OP_SLOT + l * 9216;
    {
#pragma unroll
      for (int p = 0; p < 9; ++p) dma16(lo + 1024 * p, vo[p], srd, so);
    }
  };
  issue(0); issue(1); issue(2);
#pragma unroll 1
  for (int j = 0; j < 32; ++j) {
    if (j < 30) WAITV(18); else if (j == 30) WAITV(9); else WAITV(0);
    RBAR();
    if (j + 3 < 32) issue(j + 3);
  }
}
#define OROT (pair & 15)
__global__ __launch_bounds__(768, 3) void k_oproj(char* __restrict__ wsb, const u16* __restrict__ woA, const u16* __restrict__ woB, u16* __restrict__ out) {
  extern __shared__ __attribute__((aligned(16))) char smem[];
  const unsigned wid = rfl(threadIdx.x >> 6);
  const unsigned blk = blockIdx.x, idx = blk >> 3, pair = (blk & 6) * 4 + (idx >> 2), nq = idx & 3, kh2 = blk & 1, br = pair >> 4, b = pair & 15;
  u16* outp = out + (size_t)b * SQ * (2 * DM) + br * DM + nq * 144;
  if (wid >= 8) { oproj_loader(wsb + WS_O + (size_t)pair * NH * ITEM_B, br ? woB : woA, nq, kh2, wid - 8, OROT); return; }
  const unsigned kh = wid >> 2, t = kh ? 7 - wid : wid;
  if (t == 0) oproj_consumer<5, 5>(outp, wsb, kh, kh2, blk);
  else if (t == 1) oproj_consumer<5, 4>(outp, wsb, kh, kh2, blk);
  else if (t == 2) oproj_consumer<4, 5>(outp, wsb, kh, kh2, blk);
  else oproj_consumer<4, 4>(outp, wsb, kh, kh2, blk);
}

extern "C" void kernel_launch(void* const* d_in, const int* in_sizes, int n_in, void* d_out, int out_size, void* d_ws, size_t ws_size, hipStream_t stream) {
  (void)hipFuncSetAttribute((const void*)k_attn, hipFuncAttributeMaxDynamicSharedMemorySize, LDS_BYTES);
  (void)hipFuncSetAttribute((const void*)k_oproj, hipFuncAttributeMaxDynamicSharedMemorySize, OP_LDS);
  char* ws = (char*)d_ws;
  k_attn<<<256, 768, LDS_BYTES, stream>>>((const u16*)d_in[0], (const u16*)d_in[1], (const u16*)d_in[2], (const u16*)d_in[3],
                                          (const u16*)d_in[5], (const u16*)d_in[6], (const u16*)d_in[7], (const u16*)d_in[8], ws);
  k_oproj<<<256, 768, OP_LDS, stream>>>(ws, (const u16*)d_in[4], (const u16*)d_in[9], (u16*)d_out);
}
```

```cpp
#include <hip/hip_runtime.h>
#include <stdint.h>

typedef short    bf16x8 __attribute__((ext_vector_type(8)));
typedef short    s16x4  __attribute__((ext_vector_type(4)));
typedef float    f32x4  __attribute__((ext_vector_type(4)));
typedef float    f32x2  __attribute__((ext_vector_type(2)));
typedef __bf16   bf16x2 __attribute__((ext_vector_type(2)));
typedef unsigned u32x4  __attribute__((ext_vector_type(4)));
typedef unsigned u32x2  __attribute__((ext_vector_type(2)));
typedef unsigned short u16;
#define WARMPOL ""
#define NOKROT
#define OST_POL "sc1"
#define WNT false
#define XPOL ""
#define KVSTART 14
#define KVNT_ true
#define TROT(t) (t)
#define DI __device__ __forceinline__
#define LAS __attribute__((address_space(3)))
#define MFMA16(a, b, c) __builtin_amdgcn_mfma_f32_16x16x32_bf16(a, b, c, 0, 0, 0)
#define SBAR() __builtin_amdgcn_sched_barrier(0)
#define PIN(x) asm volatile("" : "+v"(x))
#define MFMA16_V(acc, a, b) asm volatile("v_mfma_f32_16x16x32_bf16 %0, %1, %2, %0" : "+v"(acc) : "v"(a), "v"(b))
#define MFMA16_Z(d, a, b) asm volatile("v_mfma_f32_16x16x32_bf16 %0, %1, %2, 0" : "=&v"(d) : "v"(a), "v"(b))
#define MFMA16_C(d, a, b, cc) asm volatile("v_mfma_f32_16x16x32_bf16 %0, %1, %2, %3" : "=&v"(d) : "v"(a), "v"(b), "v"(cc))

constexpr int NB = 16, SQ = 144, DM = 576, NH = 32, SK = 512, DH = 128, HD = NH * DH;
constexpr int ITEM_B = 9 * 4 * 1024;
constexpr size_t WS_O = 0;
constexpr size_t WS_X = (size_t)1024 * ITEM_B, WS_F = WS_X + (size_t)256 * 2 * 25 * 1024;
constexpr float QSCALE = 0.08838834764831845f * 1.4426950408889634f;

DI unsigned rfl(unsigned x) { return __builtin_amdgcn_readfirstlane(x); }
DI unsigned lane_id() { unsigned l; asm volatile("v_mbcnt_lo_u32_b32 %0, -1, 0\n\tv_mbcnt_hi_u32_b32 %0, -1, %0" : "=v"(l)); return l; }
DI unsigned cvtpk(float lo, float hi) { f32x2 v = {lo, hi}; bf16x2 b = __builtin_convertvector(v, bf16x2); return __builtin_bit_cast(unsigned, b); }
DI float fadd_s(float a, float b) { float r; asm("v_add_f32_e32 %0, %1, %2" : "=v"(r) : "v"(a), "v"(b)); return r; }
DI bf16x8 lds128(unsigned a) { return *(const LAS bf16x8*)a; }
DI s16x4 ldstr(unsigned a) { return __builtin_bit_cast(s16x4, __builtin_amdgcn_ds_read_tr16_b64_v4i16((LAS s16x4*)a)); }
DI bf16x8 cat8(s16x4 lo, s16x4 hi) { return __builtin_shufflevector(lo, hi, 0, 1, 2, 3, 4, 5, 6, 7); }
DI void dma16(unsigned m0v, unsigned voff, __amdgpu_buffer_rsrc_t srd, unsigned soff) {
  asm volatile("s_mov_b32 m0, %0\n\ts_nop 0\n\tbuffer_load_dwordx4 %1, %2, %3 offen lds" :: "s"(m0v), "v"(voff), "s"(srd), "s"(soff) : "memory");
}
DI void dma16nt(unsigned m0v, unsigned voff, __amdgpu_buffer_rsrc_t srd, unsigned soff) {
  asm volatile("s_mov_b32 m0, %0\n\ts_nop 0\n\tbuffer_load_dwordx4 %1, %2, %3 offen nt lds" :: "s"(m0v), "v"(voff), "s"(srd), "s"(soff) : "memory");
}
DI void dma4(unsigned m0v, unsigned voff, __amdgpu_buffer_rsrc_t srd) {
  asm volatile("s_mov_b32 m0, %0\n\ts_nop 0\n\tbuffer_load_dword %1, %2, 0 offen " WARMPOL " lds" :: "s"(m0v), "v"(voff), "s"(srd) : "memory");
}
#define TILE8_BODY(POL) \
  "s_mov_b32 m0, %1\n\ts_mov_b32 %0, %2\n\t" \
  "buffer_load_dwordx4 %3, %5, %0 offen " POL " lds\n\ts_add_u32 m0, m0, 896\n\ts_nop 0\n\tbuffer_load_dwordx4 %3, %5, %0 offen offset:128 " POL " lds\n\ts_add_u32 m0, m0, 1152\n\ts_add_u32 %0, %0, %6\n\t" \
  "buffer_load_dwordx4 %4, %5, %0 offen " POL " lds\n\ts_add_u32 m0, m0, 896\n\ts_nop 0\n\tbuffer_load_dwordx4 %4, %5, %0 offen offset:128 " POL " lds\n\ts_add_u32 m0, m0, 1152\n\ts_add_u32 %0, %0, %6\n\t" \
  "buffer_load_dwordx4 %3, %5, %0 offen " POL " lds\n\ts_add_u32 m0, m0, 896\n\ts_nop 0\n\tbuffer_load_dwordx4 %3, %5, %0 offen offset:128 " POL " lds\n\ts_add_u32 m0, m0, 1152\n\ts_add_u32 %0, %0, %6\n\t" \
  "buffer_load_dwordx4 %4, %5, %0 offen " POL " lds\n\ts_add_u32 m0, m0, 896\n\ts_nop 0\n\tbuffer_load_dwordx4 %4, %5, %0 offen offset:128 " POL " lds"
template <bool NT, int D2> DI void dma_tile8(unsigned lo, unsigned so, unsigned va, unsigned vb, __amdgpu_buffer_rsrc_t srd) {
  unsigned t;
  if constexpr (NT) asm volatile(TILE8_BODY("nt") : "=&s"(t) : "s"(lo), "s"(so), "v"(va), "v"(vb), "s"(srd), "i"(D2) : "memory", "scc");
  else              asm volatile(TILE8_BODY("")   : "=&s"(t) : "s"(lo), "s"(so), "v"(va), "v"(vb), "s"(srd), "i"(D2) : "memory", "scc");
}
template <int D2> DI void dma_tile9(unsigned lo, unsigned so, unsigned va, __amdgpu_buffer_rsrc_t srd) {
  unsigned t;
  asm volatile("s_mov_b32 m0, %1\n\ts_mov_b32 %0, %2\n\t.rept 9\n\t"
               "buffer_load_dwordx4 %3, %4, %0 offen " XPOL " lds\n\ts_add_u32 m0, m0, 2048\n\ts_add_u32 %0, %0, %5\n\t.endr"
               : "=&s"(t) : "s"(lo), "s"(so), "v"(va), "s"(srd), "i"(D2) : "memory", "scc");
}
#define WAITV(n) asm volatile("s_waitcnt vmcnt(" #n ")" ::: "memory")
#define BAR() do { asm volatile("" ::: "memory"); __builtin_amdgcn_s_waitcnt(0xC07F); asm volatile("s_barrier" ::: "memory"); } while (0)
#define RBAR() asm volatile("s_barrier" ::: "memory")
constexpr unsigned GRP = 65536, WRING = 3 * 8192, XSLOT = 18432, XB = 126976, XA = XB + XSLOT, LDS_BYTES = 163840;
constexpr unsigned kslot(int i) { return 8192 * ((i & 3) == 0 ? 0 : (i & 3) == 1 ? 1 : (i & 3) == 2 ? 5 : 3); }
constexpr unsigned vslot(int i) { return 8192 * ((i & 3) == 0 ? 2 : (i & 3) == 1 ? 6 : (i & 3) == 2 ? 4 : 7); }
static_assert(XA + XSLOT == LDS_BYTES, "LDS map");

#define STAMP2(k)
#define STAMP(k)
#define STAMPW(k)

DI float red_g_max(float m) {
  auto r = __builtin_amdgcn_permlane32_swap(__float_as_uint(m), __float_as_uint(m), false, false);
  m = fmaxf(__uint_as_float(r[0]), __uint_as_float(r[1]));
  auto s = __builtin_amdgcn_permlane16_swap(__float_as_uint(m), __float_as_uint(m), false, false);
  return fmaxf(__uint_as_float(s[0]), __uint_as_float(s[1]));
}
DI float red_g_sum(float m) {
  auto r = __builtin_amdgcn_permlane32_swap(__float_as_uint(m), __float_as_uint(m), false, false);
  m = __uint_as_float(r[0]) + __uint_as_float(r[1]);
  auto s = __builtin_amdgcn_permlane16_swap(__float_as_uint(m), __float_as_uint(m), false, false);
  return __uint_as_float(s[0]) + __uint_as_float(s[1]);
}

template <int NQ> struct AttC {
  f32x4 O[NQ][8]; bf16x8 Q[NQ][4]; f32x4 S[2][NQ][2]; f32x4 negm[NQ]; float l[NQ];
  unsigned kb, vb0, vb1;
};
template <int NQ> struct Tab;
template <> struct Tab<2> {
  static constexpr int NPRE = 6;
  static constexpr int pre[6] = {100, 101, 102, 103, 104, 105};
  static constexpr int gx[16][2] = {{106, 0}, {107, 0}, {200, 201}, {110, 0}, {202, 203}, {111, 0}, {112, 0}, {113, 0}, {114, 0}, {210, 211}, {115, 0}, {116, 0}, {117, 0}, {300, 310}, {212, 213}, {301, 311}};
  static constexpr int gy[16] = {302, 312, 303, 313, 304, 314, 305, 315, 306, 316, 307, 317, 0, 0, 0, 0};
  static constexpr int all[24] = {100, 101, 102, 103, 104, 105, 106, 107, 110, 200, 201, 202, 203, 111, 112, 113, 114, 115, 116, 117, 210, 211, 212, 213};
  static constexpr int ady[16] = {300, 310, 301, 311, 302, 312, 303, 313, 304, 314, 305, 315, 306, 316, 307, 317};
};
template <> struct Tab<1> {
  static constexpr int NPRE = 4;
  static constexpr int pre[4] = {100, 101, 102, 103};
  static constexpr int gx[8][2] = {{104, 0}, {105, 0}, {200, 201}, {106, 0}, {107, 0}, {300, 301}, {202, 203}, {302, 303}};
  static constexpr int gy[8] = {304, 305, 306, 307, 0, 0, 0, 0};
  static constexpr int all[12] = {100, 101, 102, 103, 104, 105, 106, 107, 200, 201, 202, 203};
  static constexpr int ady[8] = {300, 301, 302, 303, 304, 305, 306, 307};
};
template <int NQ, int P, bool QK> struct Step {
  static constexpr int X = P & 1;
  static constexpr unsigned ks_ = kslot(P + 1), vs_ = vslot(P);
  bf16x8 fr[4]; float e[NQ][8]; unsigned pw[NQ][4]; u32x4 Pw[NQ];
  template <int OP> DI void op(AttC<NQ>& c) {
    constexpr int ty = OP / 100, qt = (OP / 10) % 10, j = OP % 10;
    if constexpr (ty == 1) { e[qt][j] = c.S[X][qt][j >> 2][j & 3]; asm volatile("v_exp_f32 %0, %0" : "+v"(e[qt][j])); }
    if constexpr (ty == 2) asm volatile("v_cvt_pk_bf16_f32 %0, %1, %2" : "=v"(pw[qt][j]) : "v"(e[qt][2 * j]), "v"(e[qt][2 * j + 1]));
    if constexpr (ty == 3) asm volatile("v_add_f32 %0, %0, %1" : "+v"(c.l[qt]) : "v"(e[qt][j]));
  }
  template <int R> DI void rd(AttC<NQ>& c) {
    if constexpr (R < 8) fr[R & 3] = lds128(c.kb + ks_ + 4096 * (R & 1) + 512 * (R >> 1));
    else if constexpr (R < 16) {
      constexpr int dt = R - 8;
      const unsigned vb = ((dt & 1) ? c.vb1 : c.vb0) + vs_ + 512 * (dt >> 1);
      fr[R & 3] = cat8(ldstr(vb), ldstr(vb + 4096));
    }
    SBAR();
  }
  template <int I> DI void gx(AttC<NQ>& c) {
    constexpr int K = I / NQ, qt = I % NQ, s = K >> 1, kt = K & 1;
    if constexpr (s == 0) MFMA16_C(c.S[X ^ 1][qt][kt], fr[K & 3], c.Q[qt][s], c.negm[qt]); else MFMA16_V(c.S[X ^ 1][qt][kt], fr[K & 3], c.Q[qt][s]);
    if constexpr (qt == NQ - 1) { SBAR(); rd<K + 4>(c); }
    op<Tab<NQ>::gx[I][0]>(c); op<Tab<NQ>::gx[I][1]>(c);
    if constexpr (I + 1 < 8 * NQ) gx<I + 1>(c);
  }
  template <int I> DI void gy(AttC<NQ>& c) {
    constexpr int Dt = I / NQ, qt = I % NQ;
    MFMA16_V(c.O[qt][Dt], fr[Dt & 3], Pw[qt]);
    if constexpr (qt == NQ - 1) { SBAR(); rd<Dt + 12>(c); }
    op<(QK ? Tab<NQ>::gy[I] : Tab<NQ>::ady[I])>(c);
    if constexpr (I + 1 < 8 * NQ) gy<I + 1>(c);
  }
  template <int I> DI void pre(AttC<NQ>& c) {
    op<(QK ? Tab<NQ>::pre[I < Tab<NQ>::NPRE ? I : 0] : Tab<NQ>::all[I])>(c);
    if constexpr (I + 1 < (QK ? Tab<NQ>::NPRE : 12 * NQ)) pre<I + 1>(c);
  }
  DI void run(AttC<NQ>& c) {
    if constexpr (QK) {
      rd<0>(c); rd<1>(c); rd<2>(c); rd<3>(c);
      pre<0>(c);
      gx<0>(c);
    } else {
      rd<8>(c); rd<9>(c); rd<10>(c); rd<11>(c);
      pre<0>(c);
      asm volatile("s_nop 1");
    }
#pragma unroll
    for (int qt = 0; qt < NQ; ++qt) Pw[qt] = u32x4{pw[qt][0], pw[qt][1], pw[qt][2], pw[qt][3]};
    gy<0>(c);
  }
};

template <int NQ, int P, bool QK> DI void att_step(AttC<NQ>& c) { BAR(); SBAR(); Step<NQ, P, QK> s; s.run(c); SBAR(); }

template <int NQ> DI void att_prologue(AttC<NQ>& c) {
#pragma unroll
  for (int s = 0; s < 4; ++s)
#pragma unroll
    for (int kt = 0; kt < 2; ++kt) {
      const bf16x8 kf = lds128(c.kb + 4096 * kt + 512 * s);
#pragma unroll
      for (int qt = 0; qt < NQ; ++qt) c.S[0][qt][kt] = MFMA16(kf, c.Q[qt][s], s == 0 ? (f32x4{0, 0, 0, 0}) : c.S[0][qt][kt]);
    }
#pragma unroll
  for (int qt = 0; qt < NQ; ++qt) {
    float mx = c.S[0][qt][0][0];
#pragma unroll
    for (int j = 1; j < 8; ++j) mx = fmaxf(mx, c.S[0][qt][j >> 2][j & 3]);
    mx = red_g_max(mx);
    c.negm[qt] = f32x4{-mx, -mx, -mx, -mx};
    c.S[0][qt][0] += c.negm[qt]; c.S[0][qt][1] += c.negm[qt];
    c.l[qt] = 0.f;
#pragma unroll
    for (int dt = 0; dt < 8; ++dt) c.O[qt][dt] = f32x4{0, 0, 0, 0};
  }
}
template <int NQ> DI void att_redo_true_max(AttC<NQ>& c, const u16* __restrict__ kh, const u16* __restrict__ vh) {
  const unsigned lane = lane_id(), li = lane & 15, g = lane >> 4;
  const u16* kp = kh + li * DH + g * 8;
  float mx[NQ];
#pragma unroll
  for (int qt = 0; qt < NQ; ++qt) mx[qt] = -__builtin_inff();
#pragma unroll 1
  for (int kt = 0; kt < SK / 16; ++kt) {
    f32x4 s4[NQ];
#pragma unroll
    for (int qt = 0; qt < NQ; ++qt) s4[qt] = f32x4{0, 0, 0, 0};
#pragma unroll
    for (int s = 0; s < 4; ++s) {
      const bf16x8 kf = *(const bf16x8*)(kp + kt * 16 * DH + s * 32);
#pragma unroll
      for (int qt = 0; qt < NQ; ++qt) s4[qt] = MFMA16(kf, c.Q[qt][s], s4[qt]);
    }
#pragma unroll
    for (int qt = 0; qt < NQ; ++qt) mx[qt] = fmaxf(fmaxf(fmaxf(mx[qt], s4[qt][0]), fmaxf(s4[qt][1], s4[qt][2])), s4[qt][3]);
  }
#pragma unroll
  for (int qt = 0; qt < NQ; ++qt) {
    mx[qt] = red_g_max(mx[qt]); c.l[qt] = 0.f;
#pragma unroll
    for (int dt = 0; dt < 8; ++dt) c.O[qt][dt] = f32x4{0, 0, 0, 0};
  }
#pragma unroll 1
  for (int st = 0; st < SK / 32; ++st) {
    u32x4 pw[NQ];
#pragma unroll
    for (int kt = 0; kt < 2; ++kt) {
      f32x4 s4[NQ];
#pragma unroll
      for (int qt = 0; qt < NQ; ++qt) s4[qt] = f32x4{-mx[qt], -mx[qt], -mx[qt], -mx[qt]};
#pragma unroll
      for (int s = 0; s < 4; ++s) {
        const bf16x8 kf = *(const bf16x8*)(kp + (2 * st + kt) * 16 * DH + s * 32);
#pragma unroll
        for (int qt = 0; qt < NQ; ++qt) s4[qt] = MFMA16(kf, c.Q[qt][s], s4[qt]);
      }
#pragma unroll
      for (int qt = 0; qt < NQ; ++qt) {
        float e[4];
#pragma unroll
        for (int j = 0; j < 4; ++j) { e[j] = __builtin_amdgcn_exp2f(s4[qt][j]); c.l[qt] += e[j]; }
        pw[qt][2 * kt] = cvtpk(e[0], e[1]); pw[qt][2 * kt + 1] = cvtpk(e[2], e[3]);
      }
    }
    const u16* vp = vh + (32 * st + 4 * g) * DH + li;
#pragma unroll
    for (int dt = 0; dt < 8; ++dt) {
      bf16x8 vf;
#pragma unroll
      for (int j = 0; j < 8; ++j) vf[j] = (short)vp[(16 * (j >> 2) + (j & 3)) * DH + 16 * dt];
#pragma unroll
      for (int qt = 0; qt < NQ; ++qt) c.O[qt][dt] = MFMA16(vf, __builtin_bit_cast(bf16x8, pw[qt]), c.O[qt][dt]);
      SBAR();
    }
  }
#pragma unroll
  for (int qt = 0; qt < NQ; ++qt) c.l[qt] = red_g_sum(c.l[qt]);
}
template <int NQ> DI void att_epilogue(AttC<NQ>& c, char* ob, const u16* kh, const u16* vh) {
  bool bad = false;
#pragma unroll
  for (int qt = 0; qt < NQ; ++qt) { c.l[qt] = red_g_sum(c.l[qt]); bad |= !(c.l[qt] < 16777216.0f); }
  if (__builtin_expect(__any(bad), 0)) att_redo_true_max<NQ>(c, kh, vh);
#pragma unroll
  for (int qt = 0; qt < NQ; ++qt) {
    const float inv = __builtin_amdgcn_rcpf(c.l[qt]);
#pragma unroll
    for (int s = 0; s < 4; ++s) {
      const f32x4 a = c.O[qt][2 * s] * inv, bb = c.O[qt][2 * s + 1] * inv;
      u32x4 w = {cvtpk(a[0], a[1]), cvtpk(a[2], a[3]), cvtpk(bb[0], bb[1]), cvtpk(bb[2], bb[3])};
      asm volatile("global_store_dwordx4 %0, %1, off " OST_POL "\n\ts_nop 1" :: "v"(ob + (qt * 4 + s) * 1024), "v"(w) : "memory");
    }
  }
}

DI bf16x8 q_frag(const f32x4& t0, const f32x4& t1) {
  const f32x4 u0 = t0 * QSCALE, u1 = t1 * QSCALE;
  unsigned a0 = cvtpk(u0[0], u0[1]), a1 = cvtpk(u0[2], u0[3]), b0 = cvtpk(u1[0], u1[1]), b1 = cvtpk(u1[2], u1[3]);
  auto r0 = __builtin_amdgcn_permlane32_swap(a0, b0, false, false);
  auto r1 = __builtin_amdgcn_permlane32_swap(a1, b1, false, false);
  auto s0 = __builtin_amdgcn_permlane16_swap(r0[0], r0[1], false, false);
  auto s1 = __builtin_amdgcn_permlane16_swap(r1[0], r1[1], false, false);
  u32x4 w = {s0[0], s1[0], s0[1], s1[1]};
  return __builtin_bit_cast(bf16x8, w);
}
template <int NQ, int WS, bool RX, bool Z = false> DI void q_unit(f32x4 (&qa)[NQ][8], const bf16x8 (&wc)[8], bf16x8 (&wn)[8], const bf16x8 (&xc)[NQ], bf16x8 (&xn)[2][NQ], unsigned w0, unsigned w1, unsigned xa) {
#pragma unroll
  for (int i = 0; i < 8 * NQ; ++i) {
    const int dt = i / NQ, q = i % NQ;
    if constexpr (Z) MFMA16_Z(qa[q][dt], wc[dt], xc[q]); else MFMA16_V(qa[q][dt], wc[dt], xc[q]);
    SBAR();
#pragma unroll
    for (int u = i * (3 - NQ); u < (i + 1) * (3 - NQ); ++u) {
      if (WS >= 0 && u < 8) { const unsigned a = ((u & 1) ? w1 : w0) + 8192 * WS + 512 * (u >> 1); wn[u] = cat8(ldstr(a), ldstr(a + 256)); }
      if (RX && u >= 8 && u < 8 + 2 * NQ) xn[(u - 8) / NQ][(u - 8) % NQ] = lds128(xa + ((u - 8) % NQ) * 2048 + ((u - 8) / NQ) * 512);
    }
    SBAR();
  }
}

template <int NQ> DI void att_wave(char* ows_pair, const u16* kbase, const u16* vbase, unsigned qt0, unsigned m, unsigned hg0, unsigned lb) {
  asm volatile("; attention wave NQ=%0" :: "i"(NQ) : "memory");
  STAMP(20);
#pragma unroll 1
  for (int r = 0; r < 2; ++r) {
    AttC<NQ> c;
    STAMP(4 * r);
    {
      const unsigned lane = lane_id(), li = lane & 15, g = lane >> 4, pos = lane & 3, tq = li >> 2;
      unsigned w0 = lb + WRING + 2048 * g + 64 * tq + 32 * (g & 1) + 8 * pos, w1 = w0 ^ 32u;
      unsigned xb = XB + qt0 * 2048 + 1024 * (li >> 3) + 64 * (li & 7) + 16 * (g ^ (2 * (li >> 3)));
      bf16x8 w[8], v[8], x0[2][NQ], x1[2][NQ];
      f32x4 qa[NQ][8];
      PIN(w0); PIN(w1); PIN(xb);
      BAR();
      if (r == 0) STAMP(21);
#pragma unroll
      for (int q = 0; q < 2 * NQ; ++q) x0[q / NQ][q % NQ] = lds128(xb + XSLOT + (q % NQ) * 2048 + (q / NQ) * 512);
#pragma unroll
      for (int dt = 0; dt < 8; ++dt) { const unsigned a = ((dt & 1) ? w1 : w0) + 512 * (dt >> 1); w[dt] = cat8(ldstr(a), ldstr(a + 256)); }
      BAR(); q_unit<NQ, 1, false, true>(qa, w, v, x0[0], x1, w0, w1, 0);
#pragma unroll 1
      for (int n = 0; n < 4; ++n) {
        BAR(); if (r == 0 && n == 1) STAMP(22); if (r == 0 && n == 3) STAMP(23);
               q_unit<NQ, 2, true>(qa, v, w, x0[1], x1, w0, w1, xb);
        BAR(); q_unit<NQ, 3, false>(qa, w, v, x1[0], x0, w0, w1, 0);
        BAR(); q_unit<NQ, 0, true>(qa, v, w, x1[1], x0, w0, w1, xb + XSLOT);
        BAR(); q_unit<NQ, 1, false>(qa, w, v, x0[0], x1, w0, w1, 0);
      }
      BAR(); q_unit<NQ, -1, false>(qa, v, w, x0[1], x1, 0, 0, 0);
      c.kb = lb + 2048 * (li >> 3) + 64 * (li & 7) + 16 * (g ^ (2 * (li >> 3)));
      c.vb0 = lb + 2048 * (g >> 1) + 64 * (4 * (g & 1) + tq) + 32 * (g & 1) + 8 * pos; c.vb1 = c.vb0 ^ 32u;
      PIN(c.kb); PIN(c.vb0); PIN(c.vb1);
      asm volatile("s_nop 7" ::: "memory"); SBAR();
#pragma unroll
      for (int q = 0; q < NQ; ++q)
#pragma unroll
        for (int s = 0; s < 4; ++s) c.Q[q][s] = q_frag(qa[q][2 * s], qa[q][2 * s + 1]);
    }
    STAMP(4 * r + 1);
    att_prologue<NQ>(c);
    SBAR();
#pragma unroll 1
    for (int ii = 0; ii < 3; ++ii) { att_step<NQ, 0, true>(c); att_step<NQ, 1, true>(c); att_step<NQ, 2, true>(c); att_step<NQ, 3, true>(c); }
    att_step<NQ, 0, true>(c); att_step<NQ, 1, true>(c); att_step<NQ, 2, true>(c); att_step<NQ, 3, false>(c);
    STAMP(4 * r + 2);
    asm volatile("s_nop 7\n\ts_nop 1" ::: "memory"); SBAR();
    const unsigned h = 8 * ((hg0 + 2 * r) & 3) + m;
    att_epilogue<NQ>(c, ows_pair + (size_t)h * ITEM_B + qt0 * 4096 + lane_id() * 16, kbase + (size_t)h * SK * DH, vbase + (size_t)h * SK * DH);
    STAMP(4 * r + 3);
  }
}

DI void wait_newer(int n) {
  if (n >= 32) { if (n >= 40) WAITV(40); else WAITV(32); }
  else if (n >= 16) { if (n >= 24) WAITV(24); else WAITV(16); }
  else if (n >= 8) WAITV(8); else WAITV(0);
}
DI void loader(const u16* wq, const u16* x, const u16* kbase, const u16* vbase, unsigned m, unsigned hg0, unsigned e0, unsigned half) {
  const unsigned lane = lane_id(), row7 = (lane >> 2) & 7, sh = lane >> 5, pos = lane & 3, lb = half * GRP;
  const __amdgpu_buffer_rsrc_t srdW = __builtin_amdgcn_make_buffer_rsrc((void*)wq, (short)0, DM * HD * 2, 0x00020000);
  const __amdgpu_buffer_rsrc_t srdX = __builtin_amdgcn_make_buffer_rsrc((void*)x, (short)0, SQ * DM * 2, 0x00020000);
  const __amdgpu_buffer_rsrc_t srdK = __builtin_amdgcn_make_buffer_rsrc((void*)kbase, (short)0, NH * SK * DH * 2, 0x00020000);
  const __amdgpu_buffer_rsrc_t srdV = __builtin_amdgcn_make_buffer_rsrc((void*)vbase, (short)0, NH * SK * DH * 2, 0x00020000);
  const unsigned vw0 = row7 * (HD * 2) + sh * 64 + pos * 16, vw1 = vw0 ^ 32u;
  const unsigned vx = (row7 * (DM * 2) + sh * 64 + pos * 16) ^ (32u * half);
  const unsigned vk0 = row7 * 256 + sh * 64 + pos * 16, vk1 = vk0 ^ 32u, vv = row7 * 256 + sh * 64 + ((pos ^ (2 * (row7 >> 2))) * 16);
  int cnt = 0;
  auto issue_w = [&](unsigned h, int s) {
    int ks = s + 2 * e0; if (ks >= 18) ks -= 18;
    dma_tile8<WNT, 8 * HD * 2>(lb + WRING + (s & 3) * 8192, ks * 32 * (HD * 2) + h * (DH * 2), vw0, vw1, srdW);
    return cnt += 8;
  };
  auto issue_x = [&](int e) {
    int ee = e + e0; if (ee >= 9) ee -= 9;
    dma_tile9<16 * DM * 2>(((e & 1) ? XB : XA) + half * 1024, ee * 128 + half * 8 * (DM * 2), vx, srdX);
    return cnt += 9;
  };
  auto issue_k = [&](unsigned h, int t) {
    const unsigned q = t & 3, lo = q == 0 ? kslot(0) : q == 1 ? kslot(1) : q == 2 ? kslot(2) : kslot(3);
    dma_tile8<KVNT_, 2048>(lb + lo, h * (SK * DH * 2) + TROT(t) * 8192, vk0, vk1, srdK);
    return cnt += 8;
  };
  auto issue_v = [&](unsigned h, int t) {
    const unsigned q = t & 3, lo = q == 0 ? vslot(0) : q == 1 ? vslot(1) : q == 2 ? vslot(2) : vslot(3);
    dma_tile8<KVNT_, 2048>(lb + lo, h * (SK * DH * 2) + TROT(t) * 8192, vv, vv, srdV);
    return cnt += 8;
  };
  unsigned h = 8 * hg0 + m;
  int n0, n1, n2, n3;
  issue_w(h, 0); n0 = issue_x(0); n1 = issue_w(h, 1); issue_w(h, 2);
#pragma unroll 1
  for (int r = 0; r < 2; ++r) {
    const unsigned hn = 8 * ((hg0 + 2) & 3) + m;
    wait_newer(cnt - n0); RBAR();
    n2 = issue_x(1);
    if (r == 0) issue_w(h, 3);
    n3 = cnt;
#pragma unroll 1
    for (int G = 1; G < 15; ++G) {
      wait_newer(cnt - n1); RBAR();
      n1 = n2; n2 = n3;
      if (G & 1) issue_x((G + 3) >> 1);
      n3 = issue_w(h, G + 3);
      if (r == 0 && G == KVSTART) { issue_k(h, 0); issue_k(h, 1); issue_v(h, 0); }
    }
    int a0 = cnt, a1, a2;
    wait_newer(cnt - n1); RBAR();
    issue_k(h, 2);
    wait_newer(cnt - n2); RBAR();
    a1 = issue_v(h, 1);
    wait_newer(cnt - n3); RBAR();
    issue_k(h, 3);
    wait_newer(cnt - a0); RBAR();
    a2 = issue_v(h, 2); issue_v(h, 3);
#pragma unroll 1
    for (int i = 0; i < 16; ++i) {
      wait_newer(cnt - a0); RBAR();
      a0 = a1; a1 = a2;
      if (i < 12) issue_k(h, i + 4);
      if (i >= 1 && i < 13) issue_v(h, i + 3);
      a2 = cnt;
      if (r == 0) {
        if (i == 12) issue_k(hn, 0);
        if (i == 13) { issue_k(hn, 1); issue_v(hn, 0); }
        if (i == 14) { issue_x(0); issue_w(hn, 2); issue_w(hn, 3); }
        if (i == 15) { n0 = issue_w(hn, 0); n1 = issue_w(hn, 1); }
      }
    }
    h = hn;
  }
}

__global__ __launch_bounds__(768, 3) void k_attn(const u16* __restrict__ xA, const u16* __restrict__ kA, const u16* __restrict__ vA, const u16* __restrict__ wqA,
                                                 const u16* __restrict__ xB, const u16* __restrict__ kB, const u16* __restrict__ vB, const u16* __restrict__ wqB,
                                                 char* __restrict__ ows) {
  extern __shared__ __attribute__((aligned(16))) char smem[];
  const unsigned wid12 = rfl(threadIdx.x >> 6), half = wid12 >= 6, wid = wid12 - 6 * half, lb = half * GRP;
  const unsigned blk = blockIdx.x, idx = blk >> 3, pair = (blk & 7) * 4 + (idx >> 3), m = idx & 7, br = pair >> 4, b = pair & 15;
  const unsigned hg0 = (blk + half) & 3;
  char* op = ows + (size_t)pair * NH * ITEM_B;
  const u16* kb = (br ? kB : kA) + (size_t)b * NH * SK * DH; const u16* vb = (br ? vB : vA) + (size_t)b * NH * SK * DH;
  if (wid < 4) att_wave<2>(op, kb, vb, 2 * wid, m, hg0, lb);
  else if (wid == 4) {
    if (lane_id() == 0) ((unsigned*)(ows + WS_F))[(blk * 2 + half) * 16] = 0u;
    att_wave<1>(op, kb, vb, 8, m, hg0, lb);
  }
  else {
    const unsigned e0 = 0;
    loader(br ? wqB : wqA, (br ? xB : xA) + (size_t)b * SQ * DM, kb, vb, m, hg0, e0, half);

  }
}

constexpr unsigned OP_SLOT = 36864, OP_LDS = 4 * OP_SLOT;
template <int NQT, int NNT> struct OSet { bf16x8 w[NNT], ol; };
template <int NQT, int NNT, bool RD, bool Z> DI void o_unit(f32x4 (&acc)[NQT][NNT], bf16x8 (&of)[NQT - 1], const OSet<NQT, NNT>& cs, OSet<NQT, NNT>& ns, unsigned oa, unsigned wa) {
#pragma unroll
  for (int q = 0; q < NQT; ++q) {
#pragma unroll
    for (int n = 0; n < NNT; ++n) {
      if constexpr (Z) MFMA16_Z(acc[q][n], cs.w[n], q < NQT - 1 ? of[q < NQT - 1 ? q : 0] : cs.ol); else MFMA16_V(acc[q][n], cs.w[n], q < NQT - 1 ? of[q < NQT - 1 ? q : 0] : cs.ol);
      SBAR();
      if (RD && q == 0) ns.w[n] = cat8(ldstr(wa + 32 * n), ldstr(wa + 32 * n + 16 * 288));
      if (RD && q == 1 && n == 1) ns.ol = lds128(oa + (NQT - 1) * 1024);
      SBAR();
    }
    if (RD && q < NQT - 1) { of[q] = lds128(oa + q * 1024); SBAR(); }
  }
}
template <int NQT, int NNT> DI void oproj_consumer(u16* __restrict__ outp, char* __restrict__ wsb, unsigned kh, unsigned kh2, unsigned blk) {
  asm volatile("; oproj consumer %0 x %1" :: "i"(NQT), "i"(NNT) : "memory");
  constexpr unsigned qh = NQT == 4, nh = NNT == 4, q0 = 5 * qh, n0 = 5 * nh;
  f32x4 acc[NQT][NNT];
  bf16x8 of[NQT - 1]; OSet<NQT, NNT> sa, sb;
  unsigned oa, wa;
  { const unsigned lane = lane_id(), li = lane & 15, g = lane >> 4;
    oa = kh * 18432 + q0 * 1024 + lane * 16; wa = kh * 18432 + 9216 + 288 * (4 * g + (li >> 2)) + 8 * (lane & 3) + 32 * n0; }
  int sd = 2 * OP_SLOT;
#define FLIP() do { oa += sd; wa += sd; sd = -sd; PIN(oa); PIN(wa); } while (0)
  PIN(oa); PIN(wa);
  STAMP2(24);
  BAR();
  STAMP2(25);
#pragma unroll
  for (int n = 0; n < NNT; ++n) sa.w[n] = cat8(ldstr(wa + 32 * n), ldstr(wa + 32 * n + 16 * 288));
#pragma unroll
  for (int q = 0; q < NQT - 1; ++q) of[q] = lds128(oa + q * 1024);
  sa.ol = lds128(oa + (NQT - 1) * 1024);
  BAR(); o_unit<NQT, NNT, true, true>(acc, of, sa, sb, oa + OP_SLOT, wa + OP_SLOT); FLIP();
#pragma unroll 1
  for (int j = 2; j < 32; j += 2) {
    BAR(); o_unit<NQT, NNT, true, false>(acc, of, sb, sa, oa, wa);
    BAR(); o_unit<NQT, NNT, true, false>(acc, of, sa, sb, oa + OP_SLOT, wa + OP_SLOT); FLIP();
  }
  BAR();
  o_unit<NQT, NNT, false, false>(acc, of, sb, sa, 0, 0);
  asm volatile("s_nop 7\n\ts_nop 1" ::: "memory"); SBAR();
  STAMP2(26);
  const unsigned lane = lane_id(), li = lane & 15, g = lane >> 4;
  unsigned ra = (2 * qh + nh) * 25600 + lane * 16; PIN(ra);
  if (kh) {
#pragma unroll
    for (int q = 0; q < NQT; ++q)
#pragma unroll
      for (int n = 0; n < NNT; ++n) *(LAS f32x4*)(ra + (q * NNT + n) * 1024) = acc[q][n];
  }
  BAR();
  if (kh) return;
#pragma unroll
  for (int q = 0; q < NQT; ++q)
#pragma unroll
    for (int n = 0; n < NNT; ++n) acc[q][n] += *(const LAS f32x4*)(ra + (q * NNT + n) * 1024);
  if (qh != kh2) {
    char* xp = wsb + WS_X + (size_t)(blk * 2 + nh) * 25600 + lane * 16;
#pragma unroll
    for (int q = 0; q < NQT; ++q)
#pragma unroll
      for (int n = 0; n < NNT; ++n) asm volatile("global_store_dwordx4 %0, %1, off sc1\n\ts_nop 1" :: "v"(xp + (q * NNT + n) * 1024), "v"(acc[q][n]) : "memory");
    asm volatile("s_waitcnt vmcnt(0)" ::: "memory");
  }
  RBAR();
  if (qh != kh2) {
    if (nh == 0 && lane == 0) __hip_atomic_store((unsigned*)(wsb + WS_F) + blk * 32, 1u, __ATOMIC_RELAXED, __HIP_MEMORY_SCOPE_AGENT);
    return;
  }
  STAMP2(27);
  const unsigned pb = blk ^ 1u;
  { unsigned* fl = (unsigned*)(wsb + WS_F) + pb * 32;
    for (int spin = 0; spin < (1 << 22) && __hip_atomic_load(fl, __ATOMIC_RELAXED, __HIP_MEMORY_SCOPE_AGENT) != 1u; ++spin) __builtin_amdgcn_s_sleep(1);
    __builtin_amdgcn_fence(__ATOMIC_ACQUIRE, "wavefront"); }
  STAMP2(28);
  const __amdgpu_buffer_rsrc_t srdP = __builtin_amdgcn_make_buffer_rsrc((void*)(wsb + WS_X + (size_t)(pb * 2 + nh) * 25600), (short)0, 25600, 0x00020000);
  constexpr unsigned RB = NNT * 32 + 16;
  unsigned ta = (2 * qh + nh) * 25600 + li * RB + g * 8; PIN(ta);
  f32x4 px[NQT * NNT > 16 ? 13 : 16];
#pragma unroll
  for (int q = 0; q < NQT; ++q)
#pragma unroll
    for (int n = 0; n < NNT; ++n) {
      constexpr int NB_ = NQT * NNT > 16 ? 13 : 16;
      const int t = q * NNT + n;
      if (t % NB_ == 0) {
#pragma unroll
        for (int u = 0; u < NB_; ++u) if (t + u < NQT * NNT) px[u] = __builtin_bit_cast(f32x4, __builtin_amdgcn_raw_buffer_load_b128(srdP, lane * 16, (t + u) * 1024, 16));
        SBAR();
      }
      const f32x4 a = acc[q][n] + px[t % NB_];
      *(LAS u32x2*)(ta + q * 16 * RB + n * 32) = u32x2{cvtpk(a[0], a[1]), cvtpk(a[2], a[3])};
    }
  constexpr unsigned CPR = NNT * 2, NCH = NQT * 16 * CPR;
#pragma unroll
  for (unsigned i = 0; i < (NCH + 63) / 64; ++i) {
    const unsigned ch = i * 64 + lane, row = ch / CPR, cc = ch - row * CPR;
    if (ch < NCH) *(u32x4*)(outp + (size_t)(16 * q0 + row) * (2 * DM) + 16 * n0 + cc * 8) = *(const LAS u32x4*)((2 * qh + nh) * 25600 + row * RB + cc * 16);
  }
  STAMP2(29);
}
DI void oproj_loader(const char* op, const u16* wo, unsigned nq, unsigned kh2, unsigned l, unsigned rot) {
  const unsigned lane = lane_id(), kh = l >> 1, isw = l & 1;
  const __amdgpu_buffer_rsrc_t srd = isw ? __builtin_amdgcn_make_buffer_rsrc((void*)wo, (short)0, HD * DM * 2, 0x00020000)
                                         : __builtin_amdgcn_make_buffer_rsrc((void*)op, (short)0, NH * ITEM_B, 0x00020000);
  unsigned vo[9];
#pragma unroll
  for (int p = 0; p < 9; ++p) { const unsigned n = 64 * p + lane, row = n / 18; vo[p] = isw ? row * (DM * 2) + (n - 18 * row) * 16 + nq * 288 : p * 4096 + lane * 16; }
  auto issue = [&](int j) {
    const unsigned h = 16 * kh2 + (((j >> 1) + rot) & 15), s = 2 * (j & 1) + kh;
    const unsigned so = isw ? (h * 4 + s) * 32 * (DM * 2) : h * ITEM_B + s * 1024, lo = (j & 3) * OP_SLOT + l * 9216;
    {
#pragma unroll
      for (int p = 0; p < 9; ++p) dma16(lo + 1024 * p, vo[p], srd, so);
    }
  };
  issue(0); issue(1); issue(2);
#pragma unroll 1
  for (int j = 0; j < 32; ++j) {
    if (j < 30) WAITV(18); else if (j == 30) WAITV(9); else WAITV(0);
    RBAR();
    if (j + 3 < 32) issue(j + 3);
  }
}
#define OROT (pair & 15)
__global__ __launch_bounds__(768, 3) void k_oproj(char* __restrict__ wsb, const u16* __restrict__ woA, const u16* __restrict__ woB, u16* __restrict__ out) {
  extern __shared__ __attribute__((aligned(16))) char smem[];
  const unsigned wid = rfl(threadIdx.x >> 6);
  const unsigned blk = blockIdx.x, idx = blk >> 3, pair = (blk & 6) * 4 + (idx >> 2), nq = idx & 3, kh2 = blk & 1, br = pair >> 4, b = pair & 15;
  u16* outp = out + (size_t)b * SQ * (2 * DM) + br * DM + nq * 144;
  if (wid >= 8) { oproj_loader(wsb + WS_O + (size_t)pair * NH * ITEM_B, br ? woB : woA, nq, kh2, wid - 8, OROT); return; }
  const unsigned kh = wid >> 2, t = kh ? 7 - wid : wid;
  if (t == 0) oproj_consumer<5, 5>(outp, wsb, kh, kh2, blk);
  else if (t == 1) oproj_consumer<5, 4>(outp, wsb, kh, kh2, blk);
  else if (t == 2) oproj_consumer<4, 5>(outp, wsb, kh, kh2, blk);
  else oproj_consumer<4, 4>(outp, wsb, kh, kh2, blk);
}

extern "C" void kernel_launch(void* const* d_in, const int* in_sizes, int n_in, void* d_out, int out_size, void* d_ws, size_t ws_size, hipStream_t stream) {
  (void)hipFuncSetAttribute((const void*)k_attn, hipFuncAttributeMaxDynamicSharedMemorySize, LDS_BYTES);
  (void)hipFuncSetAttribute((const void*)k_oproj, hipFuncAttributeMaxDynamicSharedMemorySize, OP_LDS);
  char* ws = (char*)d_ws;
  k_attn<<<256, 768, LDS_BYTES, stream>>>((const u16*)d_in[0], (const u16*)d_in[1], (const u16*)d_in[2], (const u16*)d_in[3],
                                          (const u16*)d_in[5], (const u16*)d_in[6], (const u16*)d_in[7], (const u16*)d_in[8], ws);
  k_oproj<<<256, 768, OP_LDS, stream>>>(ws, (const u16*)d_in[4], (const u16*)d_in[9], (u16*)d_out);
}
```

```cpp
#include <hip/hip_runtime.h>
#include <stdint.h>

typedef short    bf16x8 __attribute__((ext_vector_type(8)));
typedef short    s16x4  __attribute__((ext_vector_type(4)));
typedef float    f32x4  __attribute__((ext_vector_type(4)));
typedef float    f32x2  __attribute__((ext_vector_type(2)));
typedef __bf16   bf16x2 __attribute__((ext_vector_type(2)));
typedef unsigned u32x4  __attribute__((ext_vector_type(4)));
typedef unsigned u32x2  __attribute__((ext_vector_type(2)));
typedef unsigned short u16;
#define WARMPOL ""
#define NOKROT
#define OST_POL "sc1"
#define WNT false
#define XPOL ""
#define KVSTART 14
#define KVNT_ true
#define TROT(t) (t)
#define DI __device__ __forceinline__
#define LAS __attribute__((address_space(3)))
#define MFMA16(a, b, c) __builtin_amdgcn_mfma_f32_16x16x32_bf16(a, b, c, 0, 0, 0)
#define SBAR() __builtin_amdgcn_sched_barrier(0)
#define PIN(x) asm volatile("" : "+v"(x))
#define MFMA16_V(acc, a, b) asm volatile("v_mfma_f32_16x16x32_bf16 %0, %1, %2, %0" : "+v"(acc) : "v"(a), "v"(b))
#define MFMA16_Z(d, a, b) asm volatile("v_mfma_f32_16x16x32_bf16 %0, %1, %2, 0" : "=&v"(d) : "v"(a), "v"(b))
#define MFMA16_C(d, a, b, cc) asm volatile("v_mfma_f32_16x16x32_bf16 %0, %1, %2, %3" : "=&v"(d) : "v"(a), "v"(b), "v"(cc))

constexpr int NB = 16, SQ = 144, DM = 576, NH = 32, SK = 512, DH = 128, HD = NH * DH;
constexpr int ITEM_B = 9 * 4 * 1024;
constexpr size_t WS_O = 0;
constexpr size_t WS_X = (size_t)1024 * ITEM_B, WS_F = WS_X + (size_t)256 * 2 * 25 * 1024;
constexpr float QSCALE = 0.08838834764831845f * 1.4426950408889634f;

DI unsigned rfl(unsigned x) { return __builtin_amdgcn_readfirstlane(x); }
DI unsigned lane_id() { unsigned l; asm volatile("v_mbcnt_lo_u32_b32 %0, -1, 0\n\tv_mbcnt_hi_u32_b32 %0, -1, %0" : "=v"(l)); return l; }
DI unsigned cvtpk(float lo, float hi) { f32x2 v = {lo, hi}; bf16x2 b = __builtin_convertvector(v, bf16x2); return __builtin_bit_cast(unsigned, b); }
DI float fadd_s(float a, float b) { float r; asm("v_add_f32_e32 %0, %1, %2" : "=v"(r) : "v"(a), "v"(b)); return r; }
DI bf16x8 lds128(unsigned a) { return *(const LAS bf16x8*)a; }
DI s16x4 ldstr(unsigned a) { return __builtin_bit_cast(s16x4, __builtin_amdgcn_ds_read_tr16_b64_v4i16((LAS s16x4*)a)); }
DI bf16x8 cat8(s16x4 lo, s16x4 hi) { return __builtin_shufflevector(lo, hi, 0, 1, 2, 3, 4, 5, 6, 7); }
DI void dma16(unsigned m0v, unsigned voff, __amdgpu_buffer_rsrc_t srd, unsigned soff) {
  asm volatile("s_mov_b32 m0, %0\n\ts_nop 0\n\tbuffer_load_dwordx4 %1, %2, %3 offen lds" :: "s"(m0v), "v"(voff), "s"(srd), "s"(soff) : "memory");
}
DI void dma16nt(unsigned m0v, unsigned voff, __amdgpu_buffer_rsrc_t srd, unsigned soff) {
  asm volatile("s_mov_b32 m0, %0\n\ts_nop 0\n\tbuffer_load_dwordx4 %1, %2, %3 offen nt lds" :: "s"(m0v), "v"(voff), "s"(srd), "s"(soff) : "memory");
}
DI void dma4(unsigned m0v, unsigned voff, __amdgpu_buffer_rsrc_t srd) {
  asm volatile("s_mov_b32 m0, %0\n\ts_nop 0\n\tbuffer_load_dword %1, %2, 0 offen " WARMPOL " lds" :: "s"(m0v), "v"(voff), "s"(srd) : "memory");
}
#define TILE8_BODY(POL) \
  "s_mov_b32 m0, %1\n\ts_mov_b32 %0, %2\n\t" \
  "buffer_load_dwordx4 %3, %5, %0 offen " POL " lds\n\ts_add_u32 m0, m0, 896\n\ts_nop 0\n\tbuffer_load_dwordx4 %3, %5, %0 offen offset:128 " POL " lds\n\ts_add_u32 m0, m0, 1152\n\ts_add_u32 %0, %0, %6\n\t" \
  "buffer_load_dwordx4 %4, %5, %0 offen " POL " lds\n\ts_add_u32 m0, m0, 896\n\ts_nop 0\n\tbuffer_load_dwordx4 %4, %5, %0 offen offset:128 " POL " lds\n\ts_add_u32 m0, m0, 1152\n\ts_add_u32 %0, %0, %6\n\t" \
  "buffer_load_dwordx4 %3, %5, %0 offen " POL " lds\n\ts_add_u32 m0, m0, 896\n\ts_nop 0\n\tbuffer_load_dwordx4 %3, %5, %0 offen offset:128 " POL " lds\n\ts_add_u32 m0, m0, 1152\n\ts_add_u32 %0, %0, %6\n\t" \
  "buffer_load_dwordx4 %4, %5, %0 offen " POL " lds\n\ts_add_u32 m0, m0, 896\n\ts_nop 0\n\tbuffer_load_dwordx4 %4, %5, %0 offen offset:128 " POL " lds"
template <bool NT, int D2> DI void dma_tile8(unsigned lo, unsigned so, unsigned va, unsigned vb, __amdgpu_buffer_rsrc_t srd) {
  unsigned t;
  if constexpr (NT) asm volatile(TILE8_BODY("nt") : "=&s"(t) : "s"(lo), "s"(so), "v"(va), "v"(vb), "s"(srd), "i"(D2) : "memory", "scc");
  else              asm volatile(TILE8_BODY("")   : "=&s"(t) : "s"(lo), "s"(so), "v"(va), "v"(vb), "s"(srd), "i"(D2) : "memory", "scc");
}
template <int D2> DI void dma_tile9(unsigned lo, unsigned so, unsigned va, __amdgpu_buffer_rsrc_t srd) {
  unsigned t;
  asm volatile("s_mov_b32 m0, %1\n\ts_mov_b32 %0, %2\n\t.rept 9\n\t"
               "buffer_load_dwordx4 %3, %4, %0 offen " XPOL " lds\n\ts_add_u32 m0, m0, 2048\n\ts_add_u32 %0, %0, %5\n\t.endr"
               : "=&s"(t) : "s"(lo), "s"(so), "v"(va), "s"(srd), "i"(D2) : "memory", "scc");
}
#define WAITV(n) asm volatile("s_waitcnt vmcnt(" #n ")" ::: "memory")
#define BAR() do { asm volatile("" ::: "memory"); __builtin_amdgcn_s_waitcnt(0xC07F); asm volatile("s_barrier" ::: "memory"); } while (0)
#define RBAR() asm volatile("s_barrier" ::: "memory")
constexpr unsigned GRP = 65536, WRING = 3 * 8192, XSLOT = 18432, XB = 126976, XA = XB + XSLOT, LDS_BYTES = 163840;
constexpr unsigned kslot(int i) { return 8192 * ((i & 3) == 0 ? 0 : (i & 3) == 1 ? 1 : (i & 3) == 2 ? 5 : 3); }
constexpr unsigned vslot(int i) { return 8192 * ((i & 3) == 0 ? 2 : (i & 3) == 1 ? 6 : (i & 3) == 2 ? 4 : 7); }
static_assert(XA + XSLOT == LDS_BYTES, "LDS map");

#define STAMP2(k)
#define STAMP(k)
#define STAMPW(k)

DI float red_g_max(float m) {
  auto r = __builtin_amdgcn_permlane32_swap(__float_as_uint(m), __float_as_uint(m), false, false);
  m = fmaxf(__uint_as_float(r[0]), __uint_as_float(r[1]));
  auto s = __builtin_amdgcn_permlane16_swap(__float_as_uint(m), __float_as_uint(m), false, false);
  return fmaxf(__uint_as_float(s[0]), __uint_as_float(s[1]));
}
DI float red_g_sum(float m) {
  auto r = __builtin_amdgcn_permlane32_swap(__float_as_uint(m), __float_as_uint(m), false, false);
  m = __uint_as_float(r[0]) + __uint_as_float(r[1]);
  auto s = __builtin_amdgcn_permlane16_swap(__float_as_uint(m), __float_as_uint(m), false, false);
  return __uint_as_float(s[0]) + __uint_as_float(s[1]);
}

template <int NQ> struct AttC {
  f32x4 O[NQ][8]; bf16x8 Q[NQ][4]; f32x4 S[2][NQ][2]; f32x4 negm[NQ]; float l[NQ];
  unsigned kb, vb0, vb1;
};
template <int NQ> struct Tab;
template <> struct Tab<2> {
  static constexpr int NPRE = 6;
  static constexpr int pre[6] = {100, 101, 102, 103, 104, 105};
  static constexpr int gx[16][2] = {{106, 0}, {107, 0}, {200, 201}, {110, 0}, {202, 203}, {111, 0}, {112, 0}, {113, 0}, {114, 0}, {210, 211}, {115, 0}, {116, 0}, {117, 0}, {300, 310}, {212, 213}, {301, 311}};
  static constexpr int gy[16] = {302, 312, 303, 313, 304, 314, 305, 315, 306, 316, 307, 317, 0, 0, 0, 0};
  static constexpr int all[24] = {100, 101, 102, 103, 104, 105, 106, 107, 110, 200, 201, 202, 203, 111, 112, 113, 114, 115, 116, 117, 210, 211, 212, 213};
  static constexpr int ady[16] = {300, 310, 301, 311, 302, 312, 303, 313, 304, 314, 305, 315, 306, 316, 307, 317};
};
template <> struct Tab<1> {
  static constexpr int NPRE = 4;
  static constexpr int pre[4] = {100, 101, 102, 103};
  static constexpr int gx[8][2] = {{104, 0}, {105, 0}, {200, 201}, {106, 0}, {107, 0}, {300, 301}, {202, 203}, {302, 303}};
  static constexpr int gy[8] = {304, 305, 306, 307, 0, 0, 0, 0};
  static constexpr int all[12] = {100, 101, 102, 103, 104, 105, 106, 107, 200, 201, 202, 203};
  static constexpr int ady[8] = {300, 301, 302, 303, 304, 305, 306, 307};
};
template <int NQ, int P, bool QK> struct Step {
  static constexpr int X = P & 1;
  static constexpr unsigned ks_ = kslot(P + 1), vs_ = vslot(P);
  bf16x8 fr[4]; float e[NQ][8]; unsigned pw[NQ][4]; u32x4 Pw[NQ];
  template <int OP> DI void op(AttC<NQ>& c) {
    constexpr int ty = OP / 100, qt = (OP / 10) % 10, j = OP % 10;
    if constexpr (ty == 1) { e[qt][j] = c.S[X][qt][j >> 2][j & 3]; asm volatile("v_exp_f32 %0, %0" : "+v"(e[qt][j])); }
    if constexpr (ty == 2) asm volatile("v_cvt_pk_bf16_f32 %0, %1, %2" : "=v"(pw[qt][j]) : "v"(e[qt][2 * j]), "v"(e[qt][2 * j + 1]));
    if constexpr (ty == 3) asm volatile("v_add_f32 %0, %0, %1" : "+v"(c.l[qt]) : "v"(e[qt][j]));
  }
  template <int R> DI void rd(AttC<NQ>& c) {
    if constexpr (R < 8) fr[R & 3] = lds128(c.kb + ks_ + 4096 * (R & 1) + 512 * (R >> 1));
    else if constexpr (R < 16) {
      constexpr int dt = R - 8;
      const unsigned vb = ((dt & 1) ? c.vb1 : c.vb0) + vs_ + 512 * (dt >> 1);
      fr[R & 3] = cat8(ldstr(vb), ldstr(vb + 4096));
    }
    SBAR();
  }
  template <int I> DI void gx(AttC<NQ>& c) {
    constexpr int K = I / NQ, qt = I % NQ, s = K >> 1, kt = K & 1;
    if constexpr (s == 0) MFMA16_C(c.S[X ^ 1][qt][kt], fr[K & 3], c.Q[qt][s], c.negm[qt]); else MFMA16_V(c.S[X ^ 1][qt][kt], fr[K & 3], c.Q[qt][s]);
    if constexpr (qt == NQ - 1) { SBAR(); rd<K + 4>(c); }
    op<Tab<NQ>::gx[I][0]>(c); op<Tab<NQ>::gx[I][1]>(c);
    if constexpr (I + 1 < 8 * NQ) gx<I + 1>(c);
  }
  template <int I> DI void gy(AttC<NQ>& c) {
    constexpr int Dt = I / NQ, qt = I % NQ;
    MFMA16_V(c.O[qt][Dt], fr[Dt & 3], Pw[qt]);
    if constexpr (qt == NQ - 1) { SBAR(); rd<Dt + 12>(c); }
    op<(QK ? Tab<NQ>::gy[I] : Tab<NQ>::ady[I])>(c);
    if constexpr (I + 1 < 8 * NQ) gy<I + 1>(c);
  }
  template <int I> DI void pre(AttC<NQ>& c) {
    op<(QK ? Tab<NQ>::pre[I < Tab<NQ>::NPRE ? I : 0] : Tab<NQ>::all[I])>(c);
    if constexpr (I + 1 < (QK ? Tab<NQ>::NPRE : 12 * NQ)) pre<I + 1>(c);
  }
  DI void run(AttC<NQ>& c) {
    if constexpr (QK) {
      rd<0>(c); rd<1>(c); rd<2>(c); rd<3>(c);
      pre<0>(c);
      gx<0>(c);
    } else {
      rd<8>(c); rd<9>(c); rd<10>(c); rd<11>(c);
      pre<0>(c);
      asm volatile("s_nop 1");
    }
#pragma unroll
    for (int qt = 0; qt < NQ; ++qt) Pw[qt] = u32x4{pw[qt][0], pw[qt][1], pw[qt][2], pw[qt][3]};
    gy<0>(c);
  }
};

template <int NQ, int P, bool QK> DI void att_step(AttC<NQ>& c) { BAR(); SBAR(); Step<NQ, P, QK> s; s.run(c); SBAR(); }

template <int NQ> DI void att_prologue(AttC<NQ>& c) {
#pragma unroll
  for (int s = 0; s < 4; ++s)
#pragma unroll
    for (int kt = 0; kt < 2; ++kt) {
      const bf16x8 kf = lds128(c.kb + 4096 * kt + 512 * s);
#pragma unroll
      for (int qt = 0; qt < NQ; ++qt) c.S[0][qt][kt] = MFMA16(kf, c.Q[qt][s], s == 0 ? (f32x4{0, 0, 0, 0}) : c.S[0][qt][kt]);
    }
#pragma unroll
  for (int qt = 0; qt < NQ; ++qt) {
    float mx = c.S[0][qt][0][0];
#pragma unroll
    for (int j = 1; j < 8; ++j) mx = fmaxf(mx, c.S[0][qt][j >> 2][j & 3]);
    mx = red_g_max(mx);
    c.negm[qt] = f32x4{-mx, -mx, -mx, -mx};
    c.S[0][qt][0] += c.negm[qt]; c.S[0][qt][1] += c.negm[qt];
    c.l[qt] = 0.f;
#pragma unroll
    for (int dt = 0; dt < 8; ++dt) c.O[qt][dt] = f32x4{0, 0, 0, 0};
  }
}
template <int NQ> DI void att_redo_true_max(AttC<NQ>& c, const u16* __restrict__ kh, const u16* __restrict__ vh) {
  const unsigned lane = lane_id(), li = lane & 15, g = lane >> 4;
  const u16* kp = kh + li * DH + g * 8;
  float mx[NQ];
#pragma unroll
  for (int qt = 0; qt < NQ; ++qt) mx[qt] = -__builtin_inff();
#pragma unroll 1
  for (int kt = 0; kt < SK / 16; ++kt) {
    f32x4 s4[NQ];
#pragma unroll
    for (int qt = 0; qt < NQ; ++qt) s4[qt] = f32x4{0, 0, 0, 0};
#pragma unroll
    for (int s = 0; s < 4; ++s) {
      const bf16x8 kf = *(const bf16x8*)(kp + kt * 16 * DH + s * 32);
#pragma unroll
      for (int qt = 0; qt < NQ; ++qt) s4[qt] = MFMA16(kf, c.Q[qt][s], s4[qt]);
    }
#pragma unroll
    for (int qt = 0; qt < NQ; ++qt) mx[qt] = fmaxf(fmaxf(fmaxf(mx[qt], s4[qt][0]), fmaxf(s4[qt][1], s4[qt][2])), s4[qt][3]);
  }
#pragma unroll
  for (int qt = 0; qt < NQ; ++qt) {
    mx[qt] = red_g_max(mx[qt]); c.l[qt] = 0.f;
#pragma unroll
    for (int dt = 0; dt < 8; ++dt) c.O[qt][dt] = f32x4{0, 0, 0, 0};
  }
#pragma unroll 1
  for (int st = 0; st < SK / 32; ++st) {
    u32x4 pw[NQ];
#pragma unroll
    for (int kt = 0; kt < 2; ++kt) {
      f32x4 s4[NQ];
#pragma unroll
      for (int qt = 0; qt < NQ; ++qt) s4[qt] = f32x4{-mx[qt], -mx[qt], -mx[qt], -mx[qt]};
#pragma unroll
      for (int s = 0; s < 4; ++s) {
        const bf16x8 kf = *(const bf16x8*)(kp + (2 * st + kt) * 16 * DH + s * 32);
#pragma unroll
        for (int qt = 0; qt < NQ; ++qt) s4[qt] = MFMA16(kf, c.Q[qt][s], s4[qt]);
      }
#pragma unroll
      for (int qt = 0; qt < NQ; ++qt) {
        float e[4];
#pragma unroll
        for (int j = 0; j < 4; ++j) { e[j] = __builtin_amdgcn_exp2f(s4[qt][j]); c.l[qt] += e[j]; }
        pw[qt][2 * kt] = cvtpk(e[0], e[1]); pw[qt][2 * kt + 1] = cvtpk(e[2], e[3]);
      }
    }
    const u16* vp = vh + (32 * st + 4 * g) * DH + li;
#pragma unroll
    for (int dt = 0; dt < 8; ++dt) {
      bf16x8 vf;
#pragma unroll
      for (int j = 0; j < 8; ++j) vf[j] = (short)vp[(16 * (j >> 2) + (j & 3)) * DH + 16 * dt];
#pragma unroll
      for (int qt = 0; qt < NQ; ++qt) c.O[qt][dt] = MFMA16(vf, __builtin_bit_cast(bf16x8, pw[qt]), c.O[qt][dt]);
      SBAR();
    }
  }
#pragma unroll
  for (int qt = 0; qt < NQ; ++qt) c.l[qt] = red_g_sum(c.l[qt]);
}
template <int NQ> DI void att_epilogue(AttC<NQ>& c, char* ob, const u16* kh, const u16* vh) {
  bool bad = false;
#pragma unroll
  for (int qt = 0; qt < NQ; ++qt) { c.l[qt] = red_g_sum(c.l[qt]); bad |= !(c.l[qt] < 16777216.0f); }
  if (__builtin_expect(__any(bad), 0)) att_redo_true_max<NQ>(c, kh, vh);
#pragma unroll
  for (int qt = 0; qt < NQ; ++qt) {
    const float inv = __builtin_amdgcn_rcpf(c.l[qt]);
#pragma unroll
    for (int s = 0; s < 4; ++s) {
      const f32x4 a = c.O[qt][2 * s] * inv, bb = c.O[qt][2 * s + 1] * inv;
      u32x4 w = {cvtpk(a[0], a[1]), cvtpk(a[2], a[3]), cvtpk(bb[0], bb[1]), cvtpk(bb[2], bb[3])};
      asm volatile("global_store_dwordx4 %0, %1, off " OST_POL "\n\ts_nop 1" :: "v"(ob + (qt * 4 + s) * 1024), "v"(w) : "memory");
    }
  }
}

DI bf16x8 q_frag(const f32x4& t0, const f32x4& t1) {
  const f32x4 u0 = t0 * QSCALE, u1 = t1 * QSCALE;
  unsigned a0 = cvtpk(u0[0], u0[1]), a1 = cvtpk(u0[2], u0[3]), b0 = cvtpk(u1[0], u1[1]), b1 = cvtpk(u1[2], u1[3]);
  auto r0 = __builtin_amdgcn_permlane32_swap(a0, b0, false, false);
  auto r1 = __builtin_amdgcn_permlane32_swap(a1, b1, false, false);
  auto s0 = __builtin_amdgcn_permlane16_swap(r0[0], r0[1], false, false);
  auto s1 = __builtin_amdgcn_permlane16_swap(r1[0], r1[1], false, false);
  u32x4 w = {s0[0], s1[0], s0[1], s1[1]};
  return __builtin_bit_cast(bf16x8, w);
}
template <int NQ, int WS, bool RX, bool Z = false> DI void q_unit(f32x4 (&qa)[NQ][8], const bf16x8 (&wc)[8], bf16x8 (&wn)[8], const bf16x8 (&xc)[NQ], bf16x8 (&xn)[2][NQ], unsigned w0, unsigned w1, unsigned xa) {
#pragma unroll
  for (int i = 0; i < 8 * NQ; ++i) {
    const int dt = i / NQ, q = i % NQ;
    if constexpr (Z) MFMA16_Z(qa[q][dt], wc[dt], xc[q]); else MFMA16_V(qa[q][dt], wc[dt], xc[q]);
    SBAR();
#pragma unroll
    for (int u = i * (3 - NQ); u < (i + 1) * (3 - NQ); ++u) {
      if (WS >= 0 && u < 8) { const unsigned a = ((u & 1) ? w1 : w0) + 8192 * WS + 512 * (u >> 1); wn[u] = cat8(ldstr(a), ldstr(a + 256)); }
      if (RX && u >= 8 && u < 8 + 2 * NQ) xn[(u - 8) / NQ][(u - 8) % NQ] = lds128(xa + ((u - 8) % NQ) * 2048 + ((u - 8) / NQ) * 512);
    }
    SBAR();
  }
}

#define LDA(p) __hip_atomic_load((p), __ATOMIC_RELAXED, __HIP_MEMORY_SCOPE_AGENT)
#define STA(p, v) __hip_atomic_store((p), (v), __ATOMIC_RELAXED, __HIP_MEMORY_SCOPE_AGENT)
constexpr int SPIN_CAP = 1 << 21;
struct Hs { unsigned* mine; unsigned* peer; unsigned me, x, e; bool ok; };
DI void hs_post(const unsigned* p, unsigned box) { asm volatile("s_mov_b32 m0, %0\n\ts_nop 0\n\tglobal_load_lds_dword %1, off sc1" :: "s"(box), "v"(p) : "memory"); }
DI unsigned hs_take(unsigned box) { WAITV(0); return *(volatile LAS unsigned*)(box + 4 * lane_id()); }
DI void hs_entry(Hs& h, char* wsb, unsigned blk) {
  const unsigned lane = lane_id();
  h.me = (blk >> 3) & 7;
  h.mine = (unsigned*)(wsb + WS_F) + blk * 64;
  h.peer = (unsigned*)(wsb + WS_F) + ((blk & ~56u) + 8 * (lane & 7)) * 64;
  if (lane >= 1 && lane <= 3) STA(h.mine + lane, 0u);
  const unsigned y0 = LDA(h.peer + 32 + h.me);
  unsigned x = rfl(y0) + 1;
  while (__any(y0 == x)) ++x;
  h.x = x; h.e = 0; h.ok = false;
  WAITV(0);
  if (lane == 0) STA(h.mine, x);
}
DI void hs_wait_flag(const Hs& h, unsigned v, int word) {
  for (int spin = 0; !__all(v == 1u) && spin < SPIN_CAP; ++spin) { __builtin_amdgcn_s_sleep(2); v = LDA(h.peer + word); }
  __builtin_amdgcn_fence(__ATOMIC_ACQUIRE, "agent");
}
constexpr unsigned HBOX = XB + 8192;
DI void hs_hook(Hs& h, int k) {
  const unsigned lane = lane_id();
  if (k == 0) hs_post(h.peer, HBOX);
  else if (k == 1) { h.e = hs_take(HBOX); if (lane < 8) STA(h.mine + 32 + lane, h.e); }
  else if (k == 4) hs_post(h.peer + 32 + h.me, HBOX);
  else if (k == 5) { h.ok = __all(hs_take(HBOX) == h.x); hs_post(h.peer, HBOX); }
  else if (k == 6) { const unsigned e = hs_take(HBOX); if (lane < 8 && e != h.e) STA(h.mine + 32 + lane, e); hs_post(h.peer + 1, HBOX); }
  else if (k == 7) {
    unsigned f = hs_take(HBOX);
    if (!h.ok) {
      for (int spin = 0; spin < SPIN_CAP; ++spin) {
        const unsigned e = LDA(h.peer);
        if (lane < 8) STA(h.mine + 32 + lane, e);
        if (__all(LDA(h.peer + 32 + h.me) == h.x)) break;
        __builtin_amdgcn_s_sleep(2);
      }
      WAITV(0);
      const unsigned e = LDA(h.peer);
      if (lane < 8) STA(h.mine + 32 + lane, e);
      f = 0;
    }
    hs_wait_flag(h, f, 1);
  }
}

template <int NQ> DI void att_wave(char* ows_pair, const u16* kbase, const u16* vbase, unsigned qt0, unsigned m, unsigned hg0, unsigned lb, Hs& hs) {
  asm volatile("; attention wave NQ=%0" :: "i"(NQ) : "memory");
  STAMP(20);
#pragma unroll 1
  for (int r = 0; r < 2; ++r) {
    AttC<NQ> c;
    STAMP(4 * r);
    {
      const unsigned lane = lane_id(), li = lane & 15, g = lane >> 4, pos = lane & 3, tq = li >> 2;
      unsigned w0 = lb + WRING + 2048 * g + 64 * tq + 32 * (g & 1) + 8 * pos, w1 = w0 ^ 32u;
      unsigned xb = XB + qt0 * 2048 + 1024 * (li >> 3) + 64 * (li & 7) + 16 * (g ^ (2 * (li >> 3)));
      bf16x8 w[8], v[8], x0[2][NQ], x1[2][NQ];
      f32x4 qa[NQ][8];
      PIN(w0); PIN(w1); PIN(xb);
      BAR();
      if (r == 0) STAMP(21);
#pragma unroll
      for (int q = 0; q < 2 * NQ; ++q) x0[q / NQ][q % NQ] = lds128(xb + XSLOT + (q % NQ) * 2048 + (q / NQ) * 512);
#pragma unroll
      for (int dt = 0; dt < 8; ++dt) { const unsigned a = ((dt & 1) ? w1 : w0) + 512 * (dt >> 1); w[dt] = cat8(ldstr(a), ldstr(a + 256)); }
      BAR(); q_unit<NQ, 1, false, true>(qa, w, v, x0[0], x1, w0, w1, 0);
#pragma unroll 1
      for (int n = 0; n < 4; ++n) {
        WAITV(0);
        BAR(); if (r == 0 && n == 1) STAMP(22); if (r == 0 && n == 3) STAMP(23);
        if (NQ == 1 && lb == 0 && r == 1 && n == 0 && lane == 0) STA(hs.mine + 1, 1u);
               q_unit<NQ, 2, true>(qa, v, w, x0[1], x1, w0, w1, xb);
        BAR(); q_unit<NQ, 3, false>(qa, w, v, x1[0], x0, w0, w1, 0);
        BAR(); q_unit<NQ, 0, true>(qa, v, w, x1[1], x0, w0, w1, xb + XSLOT);
        BAR(); q_unit<NQ, 1, false>(qa, w, v, x0[0], x1, w0, w1, 0);
      }
      BAR(); q_unit<NQ, -1, false>(qa, v, w, x0[1], x1, 0, 0, 0);
      c.kb = lb + 2048 * (li >> 3) + 64 * (li & 7) + 16 * (g ^ (2 * (li >> 3)));
      c.vb0 = lb + 2048 * (g >> 1) + 64 * (4 * (g & 1) + tq) + 32 * (g & 1) + 8 * pos; c.vb1 = c.vb0 ^ 32u;
      PIN(c.kb); PIN(c.vb0); PIN(c.vb1);
      asm volatile("s_nop 7" ::: "memory"); SBAR();
#pragma unroll
      for (int q = 0; q < NQ; ++q)
#pragma unroll
        for (int s = 0; s < 4; ++s) c.Q[q][s] = q_frag(qa[q][2 * s], qa[q][2 * s + 1]);
    }
    STAMP(4 * r + 1);
    if (NQ == 1 && lb == 0) hs_hook(hs, 4 * r);
    att_prologue<NQ>(c);
    SBAR();
#pragma unroll 1
    for (int ii = 0; ii < 3; ++ii) { att_step<NQ, 0, true>(c); att_step<NQ, 1, true>(c); att_step<NQ, 2, true>(c); att_step<NQ, 3, true>(c);
      if (NQ == 1 && lb == 0) hs_hook(hs, 4 * r + 1 + ii); }
    att_step<NQ, 0, true>(c); att_step<NQ, 1, true>(c); att_step<NQ, 2, true>(c); att_step<NQ, 3, false>(c);
    STAMP(4 * r + 2);
    if (r == 1) { if (NQ == 1) WAITV(0); BAR(); }
    asm volatile("s_nop 7\n\ts_nop 1" ::: "memory"); SBAR();
    const unsigned h = 8 * ((hg0 + 2 * r) & 3) + m;
    att_epilogue<NQ>(c, ows_pair + (size_t)h * ITEM_B + qt0 * 4096 + lane_id() * 16, kbase + (size_t)h * SK * DH, vbase + (size_t)h * SK * DH);
    STAMP(4 * r + 3);
  }
}

DI void wait_newer(int n) {
  if (n >= 32) { if (n >= 40) WAITV(40); else WAITV(32); }
  else if (n >= 16) { if (n >= 24) WAITV(24); else WAITV(16); }
  else if (n >= 8) WAITV(8); else WAITV(0);
}
DI void loader(const u16* wq, const u16* x, const u16* kbase, const u16* vbase, unsigned m, unsigned hg0, unsigned e0, unsigned half, const char* wtouch, unsigned tsel) {
  const unsigned lane = lane_id(), row7 = (lane >> 2) & 7, sh = lane >> 5, pos = lane & 3, lb = half * GRP;
  const __amdgpu_buffer_rsrc_t srdW = __builtin_amdgcn_make_buffer_rsrc((void*)wq, (short)0, DM * HD * 2, 0x00020000);
  const __amdgpu_buffer_rsrc_t srdX = __builtin_amdgcn_make_buffer_rsrc((void*)x, (short)0, SQ * DM * 2, 0x00020000);
  const __amdgpu_buffer_rsrc_t srdK = __builtin_amdgcn_make_buffer_rsrc((void*)kbase, (short)0, NH * SK * DH * 2, 0x00020000);
  const __amdgpu_buffer_rsrc_t srdV = __builtin_amdgcn_make_buffer_rsrc((void*)vbase, (short)0, NH * SK * DH * 2, 0x00020000);
  const unsigned vw0 = row7 * (HD * 2) + sh * 64 + pos * 16, vw1 = vw0 ^ 32u;
  const unsigned vx = (row7 * (DM * 2) + sh * 64 + pos * 16) ^ (32u * half);
  const unsigned vk0 = row7 * 256 + sh * 64 + pos * 16, vk1 = vk0 ^ 32u, vv = row7 * 256 + sh * 64 + ((pos ^ (2 * (row7 >> 2))) * 16);
  int cnt = 0;
  auto issue_w = [&](unsigned h, int s) {
    int ks = s + 2 * e0; if (ks >= 18) ks -= 18;
    dma_tile8<WNT, 8 * HD * 2>(lb + WRING + (s & 3) * 8192, ks * 32 * (HD * 2) + h * (DH * 2), vw0, vw1, srdW);
    return cnt += 8;
  };
  auto issue_x = [&](int e) {
    int ee = e + e0; if (ee >= 9) ee -= 9;
    dma_tile9<16 * DM * 2>(((e & 1) ? XB : XA) + half * 1024, ee * 128 + half * 8 * (DM * 2), vx, srdX);
    return cnt += 9;
  };
  auto issue_k = [&](unsigned h, int t) {
    const unsigned q = t & 3, lo = q == 0 ? kslot(0) : q == 1 ? kslot(1) : q == 2 ? kslot(2) : kslot(3);
    dma_tile8<KVNT_, 2048>(lb + lo, h * (SK * DH * 2) + TROT(t) * 8192, vk0, vk1, srdK);
    return cnt += 8;
  };
  auto issue_v = [&](unsigned h, int t) {
    const unsigned q = t & 3, lo = q == 0 ? vslot(0) : q == 1 ? vslot(1) : q == 2 ? vslot(2) : vslot(3);
    dma_tile8<KVNT_, 2048>(lb + lo, h * (SK * DH * 2) + TROT(t) * 8192, vv, vv, srdV);
    return cnt += 8;
  };
  unsigned h = 8 * hg0 + m;
  int n0, n1, n2, n3;
  issue_w(h, 0); n0 = issue_x(0); n1 = issue_w(h, 1); issue_w(h, 2);
#pragma unroll 1
  for (int r = 0; r < 2; ++r) {
    const unsigned hn = 8 * ((hg0 + 2) & 3) + m;
    wait_newer(cnt - n0); RBAR();
    n2 = issue_x(1);
    if (r == 0) issue_w(h, 3);
    n3 = cnt;
#pragma unroll 1
    for (int G = 1; G < 15; ++G) {
      wait_newer(cnt - n1); RBAR();
      n1 = n2; n2 = n3;
      if (G & 1) issue_x((G + 3) >> 1);
      n3 = issue_w(h, G + 3);
      if (r == 0 && G == KVSTART) { issue_k(h, 0); issue_k(h, 1); issue_v(h, 0); }
    }
    int a0 = cnt, a1, a2;
    wait_newer(cnt - n1); RBAR();
    issue_k(h, 2);
    wait_newer(cnt - n2); RBAR();
    a1 = issue_v(h, 1);
    wait_newer(cnt - n3); RBAR();
    issue_k(h, 3);
    wait_newer(cnt - a0); RBAR();
    a2 = issue_v(h, 2); issue_v(h, 3);
#pragma unroll 1
    for (int i = 0; i < 16; ++i) {
      wait_newer(cnt - a0); RBAR();
      a0 = a1; a1 = a2;
      if (i < 12) issue_k(h, i + 4);
      if (i >= 1 && i < 13) issue_v(h, i + 3);
      a2 = cnt;
      if (r == 0) {
        if (i == 12) issue_k(hn, 0);
        if (i == 13) { issue_k(hn, 1); issue_v(hn, 0); }
        if (i == 14) { issue_x(0); issue_w(hn, 2); issue_w(hn, 3); }
        if (i == 15) { n0 = issue_w(hn, 0); n1 = issue_w(hn, 1); }
      }
    }
    h = hn;
  }
}

constexpr unsigned OP_SLOT = 36864, OP_LDS = 4 * OP_SLOT;
template <int NQT, int NNT> struct OSet { bf16x8 w[NNT], ol; };
template <int NQT, int NNT, bool RD, bool Z> DI void o_unit(f32x4 (&acc)[NQT][NNT], bf16x8 (&of)[NQT - 1], const OSet<NQT, NNT>& cs, OSet<NQT, NNT>& ns, unsigned oa, unsigned wa) {
#pragma unroll
  for (int q = 0; q < NQT; ++q) {
#pragma unroll
    for (int n = 0; n < NNT; ++n) {
      if constexpr (Z) MFMA16_Z(acc[q][n], cs.w[n], q < NQT - 1 ? of[q < NQT - 1 ? q : 0] : cs.ol); else MFMA16_V(acc[q][n], cs.w[n], q < NQT - 1 ? of[q < NQT - 1 ? q : 0] : cs.ol);
      SBAR();
      if (RD && q == 0) ns.w[n] = cat8(ldstr(wa + 32 * n), ldstr(wa + 32 * n + 16 * 288));
      if (RD && q == 1 && n == 1) ns.ol = lds128(oa + (NQT - 1) * 1024);
      SBAR();
    }
    if (RD && q < NQT - 1) { of[q] = lds128(oa + q * 1024); SBAR(); }
  }
}
template <int NQT, int NNT> DI void oproj_consumer(u16* __restrict__ outp, char* __restrict__ wsb, unsigned kh, unsigned kh2, unsigned blk) {
  asm volatile("; oproj consumer %0 x %1" :: "i"(NQT), "i"(NNT) : "memory");
  constexpr unsigned qh = NQT == 4, nh = NNT == 4, q0 = 5 * qh, n0 = 5 * nh;
  f32x4 acc[NQT][NNT];
  bf16x8 of[NQT - 1]; OSet<NQT, NNT> sa, sb;
  unsigned oa, wa;
  { const unsigned lane = lane_id(), li = lane & 15, g = lane >> 4;
    oa = kh * 18432 + q0 * 1024 + lane * 16; wa = kh * 18432 + 9216 + 288 * (4 * g + (li >> 2)) + 8 * (lane & 3) + 32 * n0; }
  int sd = 2 * OP_SLOT;
#define FLIP() do { oa += sd; wa += sd; sd = -sd; PIN(oa); PIN(wa); } while (0)
  PIN(oa); PIN(wa);
  STAMP2(24);
  BAR();
  STAMP2(25);
#pragma unroll
  for (int n = 0; n < NNT; ++n) sa.w[n] = cat8(ldstr(wa + 32 * n), ldstr(wa + 32 * n + 16 * 288));
#pragma unroll
  for (int q = 0; q < NQT - 1; ++q) of[q] = lds128(oa + q * 1024);
  sa.ol = lds128(oa + (NQT - 1) * 1024);
  WAITV(0);
  BAR(); o_unit<NQT, NNT, true, true>(acc, of, sa, sb, oa + OP_SLOT, wa + OP_SLOT); FLIP();
#pragma unroll 1
  for (int j = 2; j < 32; j += 2) {
    BAR(); o_unit<NQT, NNT, true, false>(acc, of, sb, sa, oa, wa);
    BAR(); o_unit<NQT, NNT, true, false>(acc, of, sa, sb, oa + OP_SLOT, wa + OP_SLOT); FLIP();
  }
  BAR();
  o_unit<NQT, NNT, false, false>(acc, of, sb, sa, 0, 0);
  asm volatile("s_nop 7\n\ts_nop 1" ::: "memory"); SBAR();
  STAMP2(26);
  const unsigned lane = lane_id(), li = lane & 15, g = lane >> 4;
  unsigned ra = (2 * qh + nh) * 25600 + lane * 16; PIN(ra);
  if (kh) {
#pragma unroll
    for (int q = 0; q < NQT; ++q)
#pragma unroll
      for (int n = 0; n < NNT; ++n) *(LAS f32x4*)(ra + (q * NNT + n) * 1024) = acc[q][n];
  }
  BAR();
  if (kh) return;
#pragma unroll
  for (int q = 0; q < NQT; ++q)
#pragma unroll
    for (int n = 0; n < NNT; ++n) acc[q][n] += *(const LAS f32x4*)(ra + (q * NNT + n) * 1024);
  if (qh != kh2) {
    char* xp = wsb + WS_X + (size_t)(blk * 2 + nh) * 25600 + lane * 16;
#pragma unroll
    for (int q = 0; q < NQT; ++q)
#pragma unroll
      for (int n = 0; n < NNT; ++n) asm volatile("global_store_dwordx4 %0, %1, off sc1\n\ts_nop 1" :: "v"(xp + (q * NNT + n) * 1024), "v"(acc[q][n]) : "memory");
    asm volatile("s_waitcnt vmcnt(0)" ::: "memory");
  }
  RBAR();
  if (qh != kh2) {
    if (nh == 0 && lane == 0) STA((unsigned*)(wsb + WS_F) + blk * 64 + 3, 1u);
    return;
  }
  STAMP2(27);
  const unsigned pb = blk ^ 8u;
  { unsigned* fl = (unsigned*)(wsb + WS_F) + pb * 64 + 3;
    for (int spin = 0; spin < SPIN_CAP && LDA(fl) != 1u; ++spin) __builtin_amdgcn_s_sleep(1);
    __builtin_amdgcn_fence(__ATOMIC_ACQUIRE, "wavefront"); }
  STAMP2(28);
  const __amdgpu_buffer_rsrc_t srdP = __builtin_amdgcn_make_buffer_rsrc((void*)(wsb + WS_X + (size_t)(pb * 2 + nh) * 25600), (short)0, 25600, 0x00020000);
  constexpr unsigned RB = NNT * 32 + 16;
  unsigned ta = (2 * qh + nh) * 25600 + li * RB + g * 8; PIN(ta);
  f32x4 px[NQT * NNT > 16 ? 13 : 16];
#pragma unroll
  for (int q = 0; q < NQT; ++q)
#pragma unroll
    for (int n = 0; n < NNT; ++n) {
      constexpr int NB_ = NQT * NNT > 16 ? 13 : 16;
      const int t = q * NNT + n;
      if (t % NB_ == 0) {
#pragma unroll
        for (int u = 0; u < NB_; ++u) if (t + u < NQT * NNT) px[u] = __builtin_bit_cast(f32x4, __builtin_amdgcn_raw_buffer_load_b128(srdP, lane * 16, (t + u) * 1024, 16));
        SBAR();
      }
      const f32x4 a = acc[q][n] + px[t % NB_];
      *(LAS u32x2*)(ta + q * 16 * RB + n * 32) = u32x2{cvtpk(a[0], a[1]), cvtpk(a[2], a[3])};
    }
  constexpr unsigned CPR = NNT * 2, NCH = NQT * 16 * CPR;
#pragma unroll
  for (unsigned i = 0; i < (NCH + 63) / 64; ++i) {
    const unsigned ch = i * 64 + lane, row = ch / CPR, cc = ch - row * CPR;
    if (ch < NCH) *(u32x4*)(outp + (size_t)(16 * q0 + row) * (2 * DM) + 16 * n0 + cc * 8) = *(const LAS u32x4*)((2 * qh + nh) * 25600 + row * RB + cc * 16);
  }
  STAMP2(29);
}
DI void oproj_loader(const char* op, const u16* wo, unsigned nq, unsigned g0, unsigned isw, unsigned rot) {
  const unsigned lane = lane_id();
  const __amdgpu_buffer_rsrc_t srd = isw ? __builtin_amdgcn_make_buffer_rsrc((void*)wo, (short)0, HD * DM * 2, 0x00020000)
                                         : __builtin_amdgcn_make_buffer_rsrc((void*)op, (short)0, NH * ITEM_B, 0x00020000);
  unsigned vo[9];
#pragma unroll
  for (int p = 0; p < 9; ++p) { const unsigned n = 64 * p + lane, row = n / 18; vo[p] = isw ? row * (DM * 2) + (n - 18 * row) * 16 + nq * 288 : p * 4096 + lane * 16; }
  const unsigned sstep = isw ? 32 * (DM * 2) : 1024;
  auto issue = [&](int j) {
    const unsigned h = 8 * ((g0 + ((j >> 3) & 2)) & 3) + (((j >> 1) + rot) & 7);
    const unsigned so = h * (isw ? 4 * 32 * (DM * 2) : ITEM_B) + 2 * (j & 1) * sstep, lo = (j & 3) * OP_SLOT + isw * 9216;
#pragma unroll
    for (int p = 0; p < 9; ++p) dma16(lo + 1024 * p, vo[p], srd, so);
#pragma unroll
    for (int p = 0; p < 9; ++p) dma16(lo + 18432 + 1024 * p, vo[p], srd, so + sstep);
  };
  RBAR();
  issue(0); issue(1); issue(2);
#pragma unroll 1
  for (int j = 0; j < 32; ++j) {
    if (j < 30) WAITV(36); else if (j == 30) WAITV(18); else WAITV(0);
    RBAR();
    if (j + 3 < 32) issue(j + 3);
  }
}

__global__ __launch_bounds__(768, 3) void k_fused(const u16* __restrict__ xA, const u16* __restrict__ kA, const u16* __restrict__ vA, const u16* __restrict__ wqA, const u16* __restrict__ woA,
                                                  const u16* __restrict__ xB, const u16* __restrict__ kB, const u16* __restrict__ vB, const u16* __restrict__ wqB, const u16* __restrict__ woB,
                                                  char* __restrict__ wsb, u16* __restrict__ out) {
  extern __shared__ __attribute__((aligned(16))) char smem[];
  const unsigned wid12 = rfl(threadIdx.x >> 6), half = wid12 >= 6, wid = wid12 - 6 * half, lb = half * GRP;
  const unsigned blk = blockIdx.x, idx = blk >> 3, pair = (blk & 7) * 4 + (idx >> 3), m = idx & 7, br = pair >> 4, b = pair & 15;
  const unsigned hg0 = (blk + half) & 3, nq = m >> 1, kh2 = m & 1;
  char* op = wsb + WS_O + (size_t)pair * NH * ITEM_B;
  const u16* kb = (br ? kB : kA) + (size_t)b * NH * SK * DH; const u16* vb = (br ? vB : vA) + (size_t)b * NH * SK * DH;
  Hs hs;
  if (wid < 4) {
    att_wave<2>(op, kb, vb, 2 * wid, m, hg0, lb, hs);
    u16* outp = out + (size_t)b * SQ * (2 * DM) + br * DM + nq * 144;
    const unsigned t = half ? 3 - ((wid + 2) & 3) : wid;
    if (t == 0) oproj_consumer<5, 5>(outp, wsb, half, kh2, blk);
    else if (t == 1) oproj_consumer<5, 4>(outp, wsb, half, kh2, blk);
    else if (t == 2) oproj_consumer<4, 5>(outp, wsb, half, kh2, blk);
    else oproj_consumer<4, 4>(outp, wsb, half, kh2, blk);
  } else if (wid == 4) {
    if (!half) hs_entry(hs, wsb, blk);
    att_wave<1>(op, kb, vb, 8, m, hg0, lb, hs);
    WAITV(0);
    if (!half) {
      RBAR(); RBAR();
      if (lane_id() == 0) STA(hs.mine + 2, 1u);
#pragma unroll 1
      for (int j = 2; j < 9; ++j) RBAR();
      hs_post(hs.peer + 2, OP_LDS);
      RBAR(); RBAR();
      hs_wait_flag(hs, hs_take(OP_LDS), 2);
      RBAR(); RBAR();
      WAITV(0);
    }
  } else {
    const unsigned e0 = 0;
    loader(br ? wqB : wqA, (br ? xB : xA) + (size_t)b * SQ * DM, kb, vb, m, hg0, e0, half, (const char*)((blk & 128) ? woB : woA) + (size_t)((blk & 127) * 2 + half) * 18432, idx + 5 * half);
    oproj_loader(op, br ? woB : woA, nq, ((blk ^ kh2) & 1) ? (blk + 1) & 3 : blk & 3, half ^ 1, 2 * (pair & 3));
  }
}

extern "C" void kernel_launch(void* const* d_in, const int* in_sizes, int n_in, void* d_out, int out_size, void* d_ws, size_t ws_size, hipStream_t stream) {
  (void)hipFuncSetAttribute((const void*)k_fused, hipFuncAttributeMaxDynamicSharedMemorySize, LDS_BYTES);
  k_fused<<<256, 768, LDS_BYTES, stream>>>((const u16*)d_in[0], (const u16*)d_in[1], (const u16*)d_in[2], (const u16*)d_in[3], (const u16*)d_in[4],
                                           (const u16*)d_in[5], (const u16*)d_in[6], (const u16*)d_in[7], (const u16*)d_in[8], (const u16*)d_in[9], (char*)d_ws, (u16*)d_out);
}
```

```cpp
#include <hip/hip_runtime.h>
#include <stdint.h>

typedef short    bf16x8 __attribute__((ext_vector_type(8)));
typedef short    s16x4  __attribute__((ext_vector_type(4)));
typedef float    f32x4  __attribute__((ext_vector_type(4)));
typedef float    f32x2  __attribute__((ext_vector_type(2)));
typedef __bf16   bf16x2 __attribute__((ext_vector_type(2)));
typedef unsigned u32x4  __attribute__((ext_vector_type(4)));
typedef unsigned u32x2  __attribute__((ext_vector_type(2)));
typedef unsigned short u16;
#define WARMPOL ""
#define NOKROT
#define OST_POL "sc1"
#define WNT false
#define XPOL ""
#define KVSTART 14
#define KVNT_ true
#define TROT(t) (t)
#define RPM_OLD 1
#define RPM_YOUNG 12
#define XAUX 16
#define TPI 0
#define TOFF 0
#define TPOL ""
#define PBF16
#define KS0 8
#define KS1 11
#define KS2 14
#define DI __device__ __forceinline__
#define LAS __attribute__((address_space(3)))
#define MFMA16(a, b, c) __builtin_amdgcn_mfma_f32_16x16x32_bf16(a, b, c, 0, 0, 0)
#define SBAR() __builtin_amdgcn_sched_barrier(0)
#define PIN(x) asm volatile("" : "+v"(x))
#define MFMA16_V(acc, a, b) asm volatile("v_mfma_f32_16x16x32_bf16 %0, %1, %2, %0" : "+v"(acc) : "v"(a), "v"(b))
#define MFMA16_Z(d, a, b) asm volatile("v_mfma_f32_16x16x32_bf16 %0, %1, %2, 0" : "=&v"(d) : "v"(a), "v"(b))
#define MFMA16_C(d, a, b, cc) asm volatile("v_mfma_f32_16x16x32_bf16 %0, %1, %2, %3" : "=&v"(d) : "v"(a), "v"(b), "v"(cc))

constexpr int NB = 16, SQ = 144, DM = 576, NH = 32, SK = 512, DH = 128, HD = NH * DH;
constexpr int ITEM_B = 9 * 4 * 1024;
constexpr size_t WS_O = 0;
constexpr size_t WS_X = (size_t)1024 * ITEM_B, WS_F = WS_X + (size_t)256 * 4 * 46080;
constexpr float QSCALE = 0.08838834764831845f * 1.4426950408889634f;

DI unsigned rfl(unsigned x) { return __builtin_amdgcn_readfirstlane(x); }
DI unsigned lane_id() { unsigned l; asm volatile("v_mbcnt_lo_u32_b32 %0, -1, 0\n\tv_mbcnt_hi_u32_b32 %0, -1, %0" : "=v"(l)); return l; }
DI unsigned cvtpk(float lo, float hi) { f32x2 v = {lo, hi}; bf16x2 b = __builtin_convertvector(v, bf16x2); return __builtin_bit_cast(unsigned, b); }
DI float fadd_s(float a, float b) { float r; asm("v_add_f32_e32 %0, %1, %2" : "=v"(r) : "v"(a), "v"(b)); return r; }
DI bf16x8 lds128(unsigned a) { return *(const LAS bf16x8*)a; }
DI s16x4 ldstr(unsigned a) { return __builtin_bit_cast(s16x4, __builtin_amdgcn_ds_read_tr16_b64_v4i16((LAS s16x4*)a)); }
DI bf16x8 cat8(s16x4 lo, s16x4 hi) { return __builtin_shufflevector(lo, hi, 0, 1, 2, 3, 4, 5, 6, 7); }
DI void dma16(unsigned m0v, unsigned voff, __amdgpu_buffer_rsrc_t srd, unsigned soff) {
  asm volatile("s_mov_b32 m0, %0\n\ts_nop 0\n\tbuffer_load_dwordx4 %1, %2, %3 offen lds" :: "s"(m0v), "v"(voff), "s"(srd), "s"(soff) : "memory");
}
DI void dma16nt(unsigned m0v, unsigned voff, __amdgpu_buffer_rsrc_t srd, unsigned soff) {
  asm volatile("s_mov_b32 m0, %0\n\ts_nop 0\n\tbuffer_load_dwordx4 %1, %2, %3 offen nt lds" :: "s"(m0v), "v"(voff), "s"(srd), "s"(soff) : "memory");
}
DI void dma4(unsigned m0v, unsigned voff, __amdgpu_buffer_rsrc_t srd) {
  asm volatile("s_mov_b32 m0, %0\n\ts_nop 0\n\tbuffer_load_dword %1, %2, 0 offen " WARMPOL " lds" :: "s"(m0v), "v"(voff), "s"(srd) : "memory");
}
#define TILE8_BODY(POL) \
  "s_mov_b32 m0, %1\n\ts_mov_b32 %0, %2\n\t" \
  "buffer_load_dwordx4 %3, %5, %0 offen " POL " lds\n\ts_add_u32 m0, m0, 896\n\ts_nop 0\n\tbuffer_load_dwordx4 %3, %5, %0 offen offset:128 " POL " lds\n\ts_add_u32 m0, m0, 1152\n\ts_add_u32 %0, %0, %6\n\t" \
  "buffer_load_dwordx4 %4, %5, %0 offen " POL " lds\n\ts_add_u32 m0, m0, 896\n\ts_nop 0\n\tbuffer_load_dwordx4 %4, %5, %0 offen offset:128 " POL " lds\n\ts_add_u32 m0, m0, 1152\n\ts_add_u32 %0, %0, %6\n\t" \
  "buffer_load_dwordx4 %3, %5, %0 offen " POL " lds\n\ts_add_u32 m0, m0, 896\n\ts_nop 0\n\tbuffer_load_dwordx4 %3, %5, %0 offen offset:128 " POL " lds\n\ts_add_u32 m0, m0, 1152\n\ts_add_u32 %0, %0, %6\n\t" \
  "buffer_load_dwordx4 %4, %5, %0 offen " POL " lds\n\ts_add_u32 m0, m0, 896\n\ts_nop 0\n\tbuffer_load_dwordx4 %4, %5, %0 offen offset:128 " POL " lds"
template <bool NT, int D2> DI void dma_tile8(unsigned lo, unsigned so, unsigned va, unsigned vb, __amdgpu_buffer_rsrc_t srd) {
  unsigned t;
  if constexpr (NT) asm volatile(TILE8_BODY("nt") : "=&s"(t) : "s"(lo), "s"(so), "v"(va), "v"(vb), "s"(srd), "i"(D2) : "memory", "scc");
  else              asm volatile(TILE8_BODY("")   : "=&s"(t) : "s"(lo), "s"(so), "v"(va), "v"(vb), "s"(srd), "i"(D2) : "memory", "scc");
}
template <int D2> DI void dma_tile9(unsigned lo, unsigned so, unsigned va, __amdgpu_buffer_rsrc_t srd) {
  unsigned t;
  asm volatile("s_mov_b32 m0, %1\n\ts_mov_b32 %0, %2\n\t.rept 9\n\t"
               "buffer_load_dwordx4 %3, %4, %0 offen " XPOL " lds\n\ts_add_u32 m0, m0, 2048\n\ts_add_u32 %0, %0, %5\n\t.endr"
               : "=&s"(t) : "s"(lo), "s"(so), "v"(va), "s"(srd), "i"(D2) : "memory", "scc");
}
#define WAITV(n) asm volatile("s_waitcnt vmcnt(" #n ")" ::: "memory")
#define BAR() do { asm volatile("" ::: "memory"); __builtin_amdgcn_s_waitcnt(0xC07F); asm volatile("s_barrier" ::: "memory"); } while (0)
#define RBAR() asm volatile("s_barrier" ::: "memory")
constexpr unsigned GRP = 65536, WRING = 3 * 8192, XSLOT = 18432, XB = 126976, XA = XB + XSLOT, LDS_BYTES = 163840;
constexpr unsigned kslot(int i) { return 8192 * ((i & 3) == 0 ? 0 : (i & 3) == 1 ? 1 : (i & 3) == 2 ? 5 : 3); }
constexpr unsigned vslot(int i) { return 8192 * ((i & 3) == 0 ? 2 : (i & 3) == 1 ? 6 : (i & 3) == 2 ? 4 : 7); }
static_assert(XA + XSLOT == LDS_BYTES, "LDS map");

#define STAMP2(k)
#define STAMP(k)
#define STAMPW(k)

DI float red_g_max(float m) {
  auto r = __builtin_amdgcn_permlane32_swap(__float_as_uint(m), __float_as_uint(m), false, false);
  m = fmaxf(__uint_as_float(r[0]), __uint_as_float(r[1]));
  auto s = __builtin_amdgcn_permlane16_swap(__float_as_uint(m), __float_as_uint(m), false, false);
  return fmaxf(__uint_as_float(s[0]), __uint_as_float(s[1]));
}
DI float red_g_sum(float m) {
  auto r = __builtin_amdgcn_permlane32_swap(__float_as_uint(m), __float_as_uint(m), false, false);
  m = __uint_as_float(r[0]) + __uint_as_float(r[1]);
  auto s = __builtin_amdgcn_permlane16_swap(__float_as_uint(m), __float_as_uint(m), false, false);
  return __uint_as_float(s[0]) + __uint_as_float(s[1]);
}

template <int NQ> struct AttC {
  f32x4 O[NQ][8]; bf16x8 Q[NQ][4]; f32x4 S[2][NQ][2]; f32x4 negm[NQ]; float l[NQ];
  unsigned kb, vb0, vb1;
};
template <int NQ> struct Tab;
template <> struct Tab<2> {
  static constexpr int NPRE = 6;
  static constexpr int pre[6] = {100, 101, 102, 103, 104, 105};
  static constexpr int gx[16][2] = {{106, 0}, {107, 0}, {200, 201}, {110, 0}, {202, 203}, {111, 0}, {112, 0}, {113, 0}, {114, 0}, {210, 211}, {115, 0}, {116, 0}, {117, 0}, {300, 310}, {212, 213}, {301, 311}};
  static constexpr int gy[16] = {302, 312, 303, 313, 304, 314, 305, 315, 306, 316, 307, 317, 0, 0, 0, 0};
  static constexpr int all[24] = {100, 101, 102, 103, 104, 105, 106, 107, 110, 200, 201, 202, 203, 111, 112, 113, 114, 115, 116, 117, 210, 211, 212, 213};
  static constexpr int ady[16] = {300, 310, 301, 311, 302, 312, 303, 313, 304, 314, 305, 315, 306, 316, 307, 317};
};
template <> struct Tab<1> {
  static constexpr int NPRE = 4;
  static constexpr int pre[4] = {100, 101, 102, 103};
  static constexpr int gx[8][2] = {{104, 0}, {105, 0}, {200, 201}, {106, 0}, {107, 0}, {300, 301}, {202, 203}, {302, 303}};
  static constexpr int gy[8] = {304, 305, 306, 307, 0, 0, 0, 0};
  static constexpr int all[12] = {100, 101, 102, 103, 104, 105, 106, 107, 200, 201, 202, 203};
  static constexpr int ady[8] = {300, 301, 302, 303, 304, 305, 306, 307};
};
template <int NQ, int P, bool QK> struct Step {
  static constexpr int X = P & 1;
  static constexpr unsigned ks_ = kslot(P + 1), vs_ = vslot(P);
  bf16x8 fr[4]; float e[NQ][8]; unsigned pw[NQ][4]; u32x4 Pw[NQ];
  template <int OP> DI void op(AttC<NQ>& c) {
    constexpr int ty = OP / 100, qt = (OP / 10) % 10, j = OP % 10;
    if constexpr (ty == 1) { e[qt][j] = c.S[X][qt][j >> 2][j & 3]; asm volatile("v_exp_f32 %0, %0" : "+v"(e[qt][j])); }
    if constexpr (ty == 2) asm volatile("v_cvt_pk_bf16_f32 %0, %1, %2" : "=v"(pw[qt][j]) : "v"(e[qt][2 * j]), "v"(e[qt][2 * j + 1]));
    if constexpr (ty == 3) asm volatile("v_add_f32 %0, %0, %1" : "+v"(c.l[qt]) : "v"(e[qt][j]));
  }
  template <int R> DI void rd(AttC<NQ>& c) {
    if constexpr (R < 8) fr[R & 3] = lds128(c.kb + ks_ + 4096 * (R & 1) + 512 * (R >> 1));
    else if constexpr (R < 16) {
      constexpr int dt = R - 8;
      const unsigned vb = ((dt & 1) ? c.vb1 : c.vb0) + vs_ + 512 * (dt >> 1);
      fr[R & 3] = cat8(ldstr(vb), ldstr(vb + 4096));
    }
    SBAR();
  }
  template <int I> DI void gx(AttC<NQ>& c) {
    constexpr int K = I / NQ, qt = I % NQ, s = K >> 1, kt = K & 1;
    if constexpr (s == 0) MFMA16_C(c.S[X ^ 1][qt][kt], fr[K & 3], c.Q[qt][s], c.negm[qt]); else MFMA16_V(c.S[X ^ 1][qt][kt], fr[K & 3], c.Q[qt][s]);
    if constexpr (qt == NQ - 1) { SBAR(); rd<K + 4>(c); }
    op<Tab<NQ>::gx[I][0]>(c); op<Tab<NQ>::gx[I][1]>(c);
    if constexpr (I + 1 < 8 * NQ) gx<I + 1>(c);
  }
  template <int I> DI void gy(AttC<NQ>& c) {
    constexpr int Dt = I / NQ, qt = I % NQ;
    MFMA16_V(c.O[qt][Dt], fr[Dt & 3], Pw[qt]);
    if constexpr (qt == NQ - 1) { SBAR(); rd<Dt + 12>(c); }
    op<(QK ? Tab<NQ>::gy[I] : Tab<NQ>::ady[I])>(c);
    if constexpr (I + 1 < 8 * NQ) gy<I + 1>(c);
  }
  template <int I> DI void pre(AttC<NQ>& c) {
    op<(QK ? Tab<NQ>::pre[I < Tab<NQ>::NPRE ? I : 0] : Tab<NQ>::all[I])>(c);
    if constexpr (I + 1 < (QK ? Tab<NQ>::NPRE : 12 * NQ)) pre<I + 1>(c);
  }
  DI void run(AttC<NQ>& c) {
    if constexpr (QK) {
      rd<0>(c); rd<1>(c); rd<2>(c); rd<3>(c);
      pre<0>(c);
      gx<0>(c);
    } else {
      rd<8>(c); rd<9>(c); rd<10>(c); rd<11>(c);
      pre<0>(c);
      asm volatile("s_nop 1");
    }
#pragma unroll
    for (int qt = 0; qt < NQ; ++qt) Pw[qt] = u32x4{pw[qt][0], pw[qt][1], pw[qt][2], pw[qt][3]};
    gy<0>(c);
  }
};

template <int NQ, int P, bool QK> DI void att_step(AttC<NQ>& c) { BAR(); SBAR();
  Step<NQ, P, QK> s; s.run(c); SBAR(); }

template <int NQ> DI void att_prologue(AttC<NQ>& c) {
#pragma unroll
  for (int s = 0; s < 4; ++s)
#pragma unroll
    for (int kt = 0; kt < 2; ++kt) {
      const bf16x8 kf = lds128(c.kb + 4096 * kt + 512 * s);
#pragma unroll
      for (int qt = 0; qt < NQ; ++qt) c.S[0][qt][kt] = MFMA16(kf, c.Q[qt][s], s == 0 ? (f32x4{0, 0, 0, 0}) : c.S[0][qt][kt]);
    }
#pragma unroll
  for (int qt = 0; qt < NQ; ++qt) {
    float mx = c.S[0][qt][0][0];
#pragma unroll
    for (int j = 1; j < 8; ++j) mx = fmaxf(mx, c.S[0][qt][j >> 2][j & 3]);
    mx = red_g_max(mx);
    c.negm[qt] = f32x4{-mx, -mx, -mx, -mx};
    c.S[0][qt][0] += c.negm[qt]; c.S[0][qt][1] += c.negm[qt];
    c.l[qt] = 0.f;
#pragma unroll
    for (int dt = 0; dt < 8; ++dt) c.O[qt][dt] = f32x4{0, 0, 0, 0};
  }
}
template <int NQ> DI void att_redo_true_max(AttC<NQ>& c, const u16* __restrict__ kh, const u16* __restrict__ vh) {
  const unsigned lane = lane_id(), li = lane & 15, g = lane >> 4;
  const u16* kp = kh + li * DH + g * 8;
  float mx[NQ];
#pragma unroll
  for (int qt = 0; qt < NQ; ++qt) mx[qt] = -__builtin_inff();
#pragma unroll 1
  for (int kt = 0; kt < SK / 16; ++kt) {
    f32x4 s4[NQ];
#pragma unroll
    for (int qt = 0; qt < NQ; ++qt) s4[qt] = f32x4{0, 0, 0, 0};
#pragma unroll
    for (int s = 0; s < 4; ++s) {
      const bf16x8 kf = *(const bf16x8*)(kp + kt * 16 * DH + s * 32);
#pragma unroll
      for (int qt = 0; qt < NQ; ++qt) s4[qt] = MFMA16(kf, c.Q[qt][s], s4[qt]);
    }
#pragma unroll
    for (int qt = 0; qt < NQ; ++qt) mx[qt] = fmaxf(fmaxf(fmaxf(mx[qt], s4[qt][0]), fmaxf(s4[qt][1], s4[qt][2])), s4[qt][3]);
  }
#pragma unroll
  for (int qt = 0; qt < NQ; ++qt) {
    mx[qt] = red_g_max(mx[qt]); c.l[qt] = 0.f;
#pragma unroll
    for (int dt = 0; dt < 8; ++dt) c.O[qt][dt] = f32x4{0, 0, 0, 0};
  }
#pragma unroll 1
  for (int st = 0; st < SK / 32; ++st) {
    u32x4 pw[NQ];
#pragma unroll
    for (int kt = 0; kt < 2; ++kt) {
      f32x4 s4[NQ];
#pragma unroll
      for (int qt = 0; qt < NQ; ++qt) s4[qt] = f32x4{-mx[qt], -mx[qt], -mx[qt], -mx[qt]};
#pragma unroll
      for (int s = 0; s < 4; ++s) {
        const bf16x8 kf = *(const bf16x8*)(kp + (2 * st + kt) * 16 * DH + s * 32);
#pragma unroll
        for (int qt = 0; qt < NQ; ++qt) s4[qt] = MFMA16(kf, c.Q[qt][s], s4[qt]);
      }
#pragma unroll
      for (int qt = 0; qt < NQ; ++qt) {
        float e[4];
#pragma unroll
        for (int j = 0; j < 4; ++j) { e[j] = __builtin_amdgcn_exp2f(s4[qt][j]); c.l[qt] += e[j]; }
        pw[qt][2 * kt] = cvtpk(e[0], e[1]); pw[qt][2 * kt + 1] = cvtpk(e[2], e[3]);
      }
    }
    const u16* vp = vh + (32 * st + 4 * g) * DH + li;
#pragma unroll
    for (int dt = 0; dt < 8; ++dt) {
      bf16x8 vf;
#pragma unroll
      for (int j = 0; j < 8; ++j) vf[j] = (short)vp[(16 * (j >> 2) + (j & 3)) * DH + 16 * dt];
#pragma unroll
      for (int qt = 0; qt < NQ; ++qt) c.O[qt][dt] = MFMA16(vf, __builtin_bit_cast(bf16x8, pw[qt]), c.O[qt][dt]);
      SBAR();
    }
  }
#pragma unroll
  for (int qt = 0; qt < NQ; ++qt) c.l[qt] = red_g_sum(c.l[qt]);
}
template <int NQ> DI void att_epilogue(AttC<NQ>& c, char* ob, const u16* kh, const u16* vh) {
  bool bad = false;
#pragma unroll
  for (int qt = 0; qt < NQ; ++qt) { c.l[qt] = red_g_sum(c.l[qt]); bad |= !(c.l[qt] < 16777216.0f); }
  if (__builtin_expect(__any(bad), 0)) att_redo_true_max<NQ>(c, kh, vh);
#pragma unroll
  for (int qt = 0; qt < NQ; ++qt) {
    const float inv = __builtin_amdgcn_rcpf(c.l[qt]);
#pragma unroll
    for (int s = 0; s < 4; ++s) {
      const f32x4 a = c.O[qt][2 * s] * inv, bb = c.O[qt][2 * s + 1] * inv;
      u32x4 w = {cvtpk(a[0], a[1]), cvtpk(a[2], a[3]), cvtpk(bb[0], bb[1]), cvtpk(bb[2], bb[3])};
      asm volatile("global_store_dwordx4 %0, %1, off " OST_POL "\n\ts_nop 1" :: "v"(ob + (qt * 4 + s) * 1024), "v"(w) : "memory");
    }
  }
}

DI bf16x8 q_frag(const f32x4& t0, const f32x4& t1) {
  const f32x4 u0 = t0 * QSCALE, u1 = t1 * QSCALE;
  unsigned a0 = cvtpk(u0[0], u0[1]), a1 = cvtpk(u0[2], u0[3]), b0 = cvtpk(u1[0], u1[1]), b1 = cvtpk(u1[2], u1[3]);
  auto r0 = __builtin_amdgcn_permlane32_swap(a0, b0, false, false);
  auto r1 = __builtin_amdgcn_permlane32_swap(a1, b1, false, false);
  auto s0 = __builtin_amdgcn_permlane16_swap(r0[0], r0[1], false, false);
  auto s1 = __builtin_amdgcn_permlane16_swap(r1[0], r1[1], false, false);
  u32x4 w = {s0[0], s1[0], s0[1], s1[1]};
  return __builtin_bit_cast(bf16x8, w);
}
template <int NQ, int RPM, int WS, bool RX, bool Z = false> DI void q_unit(f32x4 (&qa)[NQ][8], const bf16x8 (&wc)[8], bf16x8 (&wn)[8], const bf16x8 (&xc)[NQ], bf16x8 (&xn)[2][NQ], unsigned w0, unsigned w1, unsigned xa) {
#pragma unroll
  for (int i = 0; i < 8 * NQ; ++i) {
    const int dt = i / NQ, q = i % NQ;
    if (RPM < 12) { if constexpr (Z) MFMA16_Z(qa[q][dt], wc[dt], xc[q]); else MFMA16_V(qa[q][dt], wc[dt], xc[q]); SBAR(); }
#pragma unroll
    for (int u = i * RPM; u < (i + 1) * RPM; ++u) {
      if (WS >= 0 && u < 8) { const unsigned a = ((u & 1) ? w1 : w0) + 8192 * WS + 512 * (u >> 1); wn[u] = cat8(ldstr(a), ldstr(a + 256)); }
      if (RX && u >= 8 && u < 8 + 2 * NQ) xn[(u - 8) / NQ][(u - 8) % NQ] = lds128(xa + ((u - 8) % NQ) * 2048 + ((u - 8) / NQ) * 512);
    }
    SBAR();
    if (RPM >= 12) { if constexpr (Z) MFMA16_Z(qa[q][dt], wc[dt], xc[q]); else MFMA16_V(qa[q][dt], wc[dt], xc[q]); SBAR(); }
  }
}

#define LDA(p) __hip_atomic_load((p), __ATOMIC_RELAXED, __HIP_MEMORY_SCOPE_AGENT)
#define STA(p, v) __hip_atomic_store((p), (v), __ATOMIC_RELAXED, __HIP_MEMORY_SCOPE_AGENT)
constexpr int SPIN_CAP = 1 << 21;
struct Hs { unsigned* mine; unsigned* peer; unsigned me, x, e; bool ok; };
DI void hs_post(const unsigned* p, unsigned box) { asm volatile("s_mov_b32 m0, %0\n\ts_nop 0\n\tglobal_load_lds_dword %1, off sc1" :: "s"(box), "v"(p) : "memory"); }
DI unsigned hs_take(unsigned box) { WAITV(0); return *(volatile LAS unsigned*)(box + 4 * lane_id()); }
DI void hs_entry(Hs& h, char* wsb, unsigned blk) {
  const unsigned lane = lane_id();
  h.me = (blk >> 3) & 7;
  h.mine = (unsigned*)(wsb + WS_F) + blk * 64;
  h.peer = (unsigned*)(wsb + WS_F) + ((blk & ~56u) + 8 * (lane & 7)) * 64;
  if (lane >= 1 && lane <= 3) STA(h.mine + lane, 0u);
  const unsigned y0 = LDA(h.peer + 32 + h.me);
  unsigned x = rfl(y0) + 1;
  while (__any(y0 == x)) ++x;
  h.x = x; h.e = 0; h.ok = false;
  WAITV(0);
  if (lane == 0) STA(h.mine, x);
}
DI void hs_wait_flag(const Hs& h, unsigned v, int word) {
  for (int spin = 0; !__all(v == 1u) && spin < SPIN_CAP; ++spin) { __builtin_amdgcn_s_sleep(2); v = LDA(h.peer + word); }
  __builtin_amdgcn_fence(__ATOMIC_ACQUIRE, "agent");
}
constexpr unsigned HBOX = 161792;
DI void hs_hook(Hs& h, int k) {
  const unsigned lane = lane_id();
  if (k == 0) hs_post(h.peer, HBOX);
  else if (k == 1) { h.e = hs_take(HBOX); if (lane < 8) STA(h.mine + 32 + lane, h.e); }
  else if (k == 4) hs_post(h.peer + 32 + h.me, HBOX);
  else if (k == 5) { h.ok = __all(hs_take(HBOX) == h.x); hs_post(h.peer, HBOX); }
  else if (k == 6) { const unsigned e = hs_take(HBOX); if (lane < 8 && e != h.e) STA(h.mine + 32 + lane, e); hs_post(h.peer + 1, HBOX); }
  else if (k == 7) {
    unsigned f = hs_take(HBOX);
    if (!h.ok) {
      for (int spin = 0; spin < SPIN_CAP; ++spin) {
        const unsigned e = LDA(h.peer);
        if (lane < 8) STA(h.mine + 32 + lane, e);
        if (__all(LDA(h.peer + 32 + h.me) == h.x)) break;
        __builtin_amdgcn_s_sleep(2);
      }
      WAITV(0);
      const unsigned e = LDA(h.peer);
      if (lane < 8) STA(h.mine + 32 + lane, e);
      f = 0;
    }
    hs_wait_flag(h, f, 1);
  }
}

template <int NQ, int RPM> DI void q_phase(AttC<NQ>& c, unsigned qt0, unsigned lb, int r, Hs& hs) {
  asm volatile("; q phase %0" :: "i"(RPM) : "memory");
    {
      const unsigned lane = lane_id(), li = lane & 15, g = lane >> 4, pos = lane & 3, tq = li >> 2;
      unsigned w0 = lb + WRING + 2048 * g + 64 * tq + 32 * (g & 1) + 8 * pos, w1 = w0 ^ 32u;
      unsigned xb = XB + qt0 * 2048 + 1024 * (li >> 3) + 64 * (li & 7) + 16 * (g ^ (2 * (li >> 3)));
      bf16x8 w[8], v[8], x0[2][NQ], x1[2][NQ];
      f32x4 qa[NQ][8];
      PIN(w0); PIN(w1); PIN(xb);
      BAR();
      if (r == 0) STAMP(21);
#pragma unroll
      for (int q = 0; q < 2 * NQ; ++q) x0[q / NQ][q % NQ] = lds128(xb + XSLOT + (q % NQ) * 2048 + (q / NQ) * 512);
#pragma unroll
      for (int dt = 0; dt < 8; ++dt) { const unsigned a = ((dt & 1) ? w1 : w0) + 512 * (dt >> 1); w[dt] = cat8(ldstr(a), ldstr(a + 256)); }
      BAR(); q_unit<NQ, RPM, 1, false, true>(qa, w, v, x0[0], x1, w0, w1, 0);
#pragma unroll 1
      for (int n = 0; n < 4; ++n) {
        WAITV(0);
        BAR(); if (r == 0 && n == 1) STAMP(22); if (r == 0 && n == 3) STAMP(23);
        if (NQ == 1 && lb == 0 && r == 1 && n == 0 && lane == 0) STA(hs.mine + 1, 1u);
               q_unit<NQ, RPM, 2, true>(qa, v, w, x0[1], x1, w0, w1, xb);
        BAR(); q_unit<NQ, RPM, 3, false>(qa, w, v, x1[0], x0, w0, w1, 0);
        BAR(); q_unit<NQ, RPM, 0, true>(qa, v, w, x1[1], x0, w0, w1, xb + XSLOT);
        BAR(); q_unit<NQ, RPM, 1, false>(qa, w, v, x0[0], x1, w0, w1, 0);
      }
      BAR(); q_unit<NQ, RPM, -1, false>(qa, v, w, x0[1], x1, 0, 0, 0);
      c.kb = lb + 2048 * (li >> 3) + 64 * (li & 7) + 16 * (g ^ (2 * (li >> 3)));
      c.vb0 = lb + 2048 * (g >> 1) + 64 * (4 * (g & 1) + tq) + 32 * (g & 1) + 8 * pos; c.vb1 = c.vb0 ^ 32u;
      PIN(c.kb); PIN(c.vb0); PIN(c.vb1);
      asm volatile("s_nop 7" ::: "memory"); SBAR();
#pragma unroll
      for (int q = 0; q < NQ; ++q)
#pragma unroll
        for (int s = 0; s < 4; ++s) c.Q[q][s] = q_frag(qa[q][2 * s], qa[q][2 * s + 1]);
    }
}

template <int NQ> DI void att_wave(char* ows_pair, const u16* kbase, const u16* vbase, unsigned qt0, unsigned m, unsigned hg0, unsigned lb, Hs& hs) {
  asm volatile("; attention wave NQ=%0" :: "i"(NQ) : "memory");
  STAMP(20);
#pragma unroll 1
  for (int r = 0; r < 2; ++r) {
    AttC<NQ> c;
    STAMP(4 * r);
    q_phase<NQ, 3 - NQ>(c, qt0, lb, r, hs);
    STAMP(4 * r + 1);
    if (NQ == 1 && lb == 0) hs_hook(hs, 4 * r);
    att_prologue<NQ>(c);
    SBAR();
#pragma unroll 1
    for (int ii = 0; ii < 3; ++ii) { att_step<NQ, 0, true>(c); att_step<NQ, 1, true>(c); att_step<NQ, 2, true>(c); att_step<NQ, 3, true>(c);
      if (NQ == 1 && lb == 0) hs_hook(hs, 4 * r + 1 + ii); }
    att_step<NQ, 0, true>(c); att_step<NQ, 1, true>(c);
    if (NQ == 1) WAITV(0);
    att_step<NQ, 2, true>(c); att_step<NQ, 3, false>(c);
    STAMP(4 * r + 2);
    asm volatile("s_nop 7\n\ts_nop 1" ::: "memory"); SBAR();
    const unsigned h = 8 * ((hg0 + 2 * r) & 3) + m;
    att_epilogue<NQ>(c, ows_pair + (size_t)h * ITEM_B + qt0 * 4096 + lane_id() * 16, kbase + (size_t)h * SK * DH, vbase + (size_t)h * SK * DH);
    STAMP(4 * r + 3);
  }
}

DI void wait_newer(int n) {
  if (n >= 32) { if (n >= 40) WAITV(40); else WAITV(32); }
  else if (n >= 16) { if (n >= 24) WAITV(24); else WAITV(16); }
  else if (n >= 8) WAITV(8); else WAITV(0);
}
DI unsigned oslot_o(unsigned s) { return s == 0 ? 131072 : GRP - 9216 + 9216 * s; }
DI unsigned oslot_w(unsigned s) { return s == 0 ? 140288 : 18432 * (s - 1); }
constexpr unsigned OPART = 93184;
constexpr unsigned OBOX = 55296;
struct OLd {
  __amdgpu_buffer_rsrc_t srd; unsigned vo[9], h0, isw, rot;
  DI void init(const char* op, const u16* wo, unsigned m, unsigned g0, unsigned isw_, unsigned rot_) {
    const unsigned lane = lane_id(); h0 = 8 * g0 + (m & 4); isw = isw_; rot = rot_;
    srd = isw ? __builtin_amdgcn_make_buffer_rsrc((void*)wo, (short)0, HD * DM * 2, 0x00020000) : __builtin_amdgcn_make_buffer_rsrc((void*)op, (short)0, NH * ITEM_B, 0x00020000);
#pragma unroll
    for (int p = 0; p < 9; ++p) { const unsigned n = 64 * p + lane, row = n / 36, ch = n - 36 * row;
      vo[p] = isw ? row * (DM * 2) + ((ch ^ ((row >> 1) & 2)) * 16) + (m & 1) * 576 : p * 4096 + lane * 16; }
  }
#define OPIECE(k) "buffer_load_dwordx4 %" #k ", %1, %2 offen lds\n\ts_add_u32 m0, m0, 1024\n\ts_nop 0\n\t"
  DI void issue(int j, unsigned part) const {
    const unsigned h = ((h0 + (j & 16)) & 31) + (((j >> 2) + rot) & 3), s = j & 3;
    const unsigned so = isw ? ((h * 4 + s) * 32 + 16 * part) * (DM * 2) : h * ITEM_B + s * 1024, lo = isw ? oslot_w(j & 3) + part * 9216 : oslot_o(j & 3);
    asm volatile("s_mov_b32 m0, %0\n\ts_nop 0\n\t" OPIECE(3) OPIECE(4) OPIECE(5) OPIECE(6) OPIECE(7) OPIECE(8) OPIECE(9) OPIECE(10) OPIECE(11)
                 :: "s"(lo), "s"(srd), "s"(so), "v"(vo[0]), "v"(vo[1]), "v"(vo[2]), "v"(vo[3]), "v"(vo[4]), "v"(vo[5]), "v"(vo[6]), "v"(vo[7]), "v"(vo[8]) : "memory", "scc");
  }
  DI void run(unsigned part) const {
#pragma unroll 1
    for (int j = 0; j < 32; ++j) {
      if (j == 0 || j == 30) WAITV(9); else if (j < 30) WAITV(18); else WAITV(0);
      RBAR();
      if (j == 0) issue(2, part);
      if (j + 3 < 32) issue(j + 3, part);
    }
  }
};

DI void loader(const u16* wq, const u16* x, const u16* kbase, const u16* vbase, unsigned m, unsigned hg0, unsigned e0, unsigned half, OLd& ol, const char* op, const u16* wo, unsigned g0, unsigned rot) {
  const unsigned lane = lane_id(), row7 = (lane >> 2) & 7, sh = lane >> 5, pos = lane & 3, lb = half * GRP;
  const __amdgpu_buffer_rsrc_t srdW = __builtin_amdgcn_make_buffer_rsrc((void*)wq, (short)0, DM * HD * 2, 0x00020000);
  const __amdgpu_buffer_rsrc_t srdX = __builtin_amdgcn_make_buffer_rsrc((void*)x, (short)0, SQ * DM * 2, 0x00020000);
  const __amdgpu_buffer_rsrc_t srdK = __builtin_amdgcn_make_buffer_rsrc((void*)kbase, (short)0, NH * SK * DH * 2, 0x00020000);
  const __amdgpu_buffer_rsrc_t srdV = __builtin_amdgcn_make_buffer_rsrc((void*)vbase, (short)0, NH * SK * DH * 2, 0x00020000);
  const unsigned vw0 = row7 * (HD * 2) + sh * 64 + pos * 16, vw1 = vw0 ^ 32u;
  const unsigned vx = (row7 * (DM * 2) + sh * 64 + pos * 16) ^ (32u * half);
  const unsigned vk0 = row7 * 256 + sh * 64 + pos * 16, vk1 = vk0 ^ 32u, vv = row7 * 256 + sh * 64 + ((pos ^ (2 * (row7 >> 2))) * 16);
  int cnt = 0;
  auto issue_w = [&](unsigned h, int s) {
    int ks = s + 2 * e0; if (ks >= 18) ks -= 18;
    dma_tile8<WNT, 8 * HD * 2>(lb + WRING + (s & 3) * 8192, ks * 32 * (HD * 2) + h * (DH * 2), vw0, vw1, srdW);
    return cnt += 8;
  };
  auto issue_x = [&](int e) {
    int ee = e + e0; if (ee >= 9) ee -= 9;
    dma_tile9<16 * DM * 2>(((e & 1) ? XB : XA) + half * 1024, ee * 128 + half * 8 * (DM * 2), vx, srdX);
    return cnt += 9;
  };
  auto issue_k = [&](unsigned h, int t) {
    const unsigned q = t & 3, lo = q == 0 ? kslot(0) : q == 1 ? kslot(1) : q == 2 ? kslot(2) : kslot(3);
    dma_tile8<KVNT_, 2048>(lb + lo, h * (SK * DH * 2) + TROT(t) * 8192, vk0, vk1, srdK);
    return cnt += 8;
  };
  auto issue_v = [&](unsigned h, int t) {
    const unsigned q = t & 3, lo = q == 0 ? vslot(0) : q == 1 ? vslot(1) : q == 2 ? vslot(2) : vslot(3);
    dma_tile8<KVNT_, 2048>(lb + lo, h * (SK * DH * 2) + TROT(t) * 8192, vv, vv, srdV);
    return cnt += 8;
  };
  unsigned h = 8 * hg0 + m;
  int kx = 0;
  int n0, n1, n2, n3;
  issue_w(h, 0); n0 = issue_x(0); n1 = issue_w(h, 1); issue_w(h, 2);
  ol.init(op, wo, m, g0, half ^ 1, rot);
#pragma unroll 1
  for (int r = 0; r < 2; ++r) {
    const unsigned hn = 8 * ((hg0 + 2) & 3) + m;
    wait_newer(cnt - n0); RBAR();
    n2 = issue_x(1);
    if (r == 0) issue_w(h, 3);
    n3 = cnt;
#pragma unroll 1
    for (int G = 1; G < 15; ++G) {
      wait_newer(cnt - n1); RBAR();
      n1 = n2; n2 = n3;
      if (G & 1) issue_x((G + 3) >> 1);
      issue_w(h, G + 3);
      n3 = cnt;
      if (r == 0) { if (G == KS0) kx = issue_k(h, 0); if (G == KS1) issue_k(h, 1); if (G == KS2) issue_v(h, 0); }
    }
    int a0 = cnt, a1, a2;
    wait_newer(cnt - n1); RBAR();
    issue_k(h, 2);
    wait_newer(cnt - n2); RBAR();
    a1 = issue_v(h, 1);
    wait_newer(cnt - n3); RBAR();
    issue_k(h, 3);
    wait_newer(cnt - kx); RBAR();
    a2 = issue_v(h, 2); issue_v(h, 3);
#pragma unroll 1
    for (int i = 0; i < 16; ++i) {
      wait_newer(cnt - a0); RBAR();
      a0 = a1; a1 = a2;
      if (i < 12) issue_k(h, i + 4);
      if (i >= 1 && i < 13) issue_v(h, i + 3);
      a2 = cnt;
      if (r == 0) {
        if (i == 12) issue_k(hn, 0);
        if (i == 13) { issue_k(hn, 1); issue_v(hn, 0); }
        if (i == 14) { issue_x(0); issue_w(hn, 2); issue_w(hn, 3); }
        if (i == 15) { n0 = issue_w(hn, 0); n1 = issue_w(hn, 1); }
      } else {
        if (i == 13 && ol.isw) { ol.issue(0, 0); ol.issue(0, 1); ol.issue(1, 0); ol.issue(1, 1); cnt += 36; }
        if (i == 14 && !ol.isw) { ol.issue(0, 0); ol.issue(1, 0); cnt += 18; }
      }
    }
    h = hn;
  }
}

template <int NQT, int NNT> struct OSet { bf16x8 w[NNT], ol; };
template <int NQT, int NNT, bool RD, bool Z> DI void o_unit(f32x4 (&acc)[NQT][NNT], bf16x8 (&of)[NQT - 1], const OSet<NQT, NNT>& cs, OSet<NQT, NNT>& ns, unsigned oa, unsigned wa0, unsigned wa1) {
#pragma unroll
  for (int q = 0; q < NQT; ++q) {
#pragma unroll
    for (int n = 0; n < NNT; ++n) {
      if constexpr (Z) MFMA16_Z(acc[q][n], cs.w[n], q < NQT - 1 ? of[q < NQT - 1 ? q : 0] : cs.ol); else MFMA16_V(acc[q][n], cs.w[n], q < NQT - 1 ? of[q < NQT - 1 ? q : 0] : cs.ol);
      SBAR();
      if (RD && q == 0) { const unsigned a = ((n & 1) ? wa1 : wa0) + 64 * (n >> 1); ns.w[n] = cat8(ldstr(a), ldstr(a + 9216)); }
      if (RD && q == 1 && n == 1) ns.ol = lds128(oa + (NQT - 1) * 1024);
      SBAR();
    }
    if (RD && q < NQT - 1) { of[q] = lds128(oa + q * 1024); SBAR(); }
  }
}
template <int NQT, int NNT> DI void oproj_consumer(char* __restrict__ wsb, unsigned nb, unsigned kq, unsigned blk) {
  asm volatile("; oproj consumer %0 x %1" :: "i"(NQT), "i"(NNT) : "memory");
  constexpr unsigned qh = NQT == 4, q0 = 5 * qh;
  f32x4 acc[NQT][NNT];
  bf16x8 of[NQT - 1]; OSet<NQT, NNT> sa, sb;
  unsigned oa, wa0, wa1;
  { const unsigned lane = lane_id(), li = lane & 15, g = lane >> 4, n0 = (9 * nb + 1) >> 1;
    const unsigned rb = oslot_w(0) + 576 * (4 * g + (li >> 2)) + 8 * (lane & 3);
    oa = oslot_o(0) + q0 * 1024 + lane * 16;
    wa0 = rb + 64 * (n0 >> 1) + 32 * ((n0 ^ g) & 1); wa1 = rb + 64 * ((n0 + 1) >> 1) + 32 * ((n0 ^ g ^ 1) & 1); }
#define NEXTO(j) do { const unsigned d_ = oslot_w(((j) + 1) & 3) - oslot_w((j) & 3); oa += oslot_o(((j) + 1) & 3) - oslot_o((j) & 3); wa0 += d_; wa1 += d_; PIN(oa); PIN(wa0); PIN(wa1); } while (0)
  PIN(oa); PIN(wa0); PIN(wa1);
  STAMP2(24);
  BAR();
  STAMP2(25);
#pragma unroll
  for (int n = 0; n < NNT; ++n) { const unsigned a = ((n & 1) ? wa1 : wa0) + 64 * (n >> 1); sa.w[n] = cat8(ldstr(a), ldstr(a + 9216)); }
#pragma unroll
  for (int q = 0; q < NQT - 1; ++q) of[q] = lds128(oa + q * 1024);
  sa.ol = lds128(oa + (NQT - 1) * 1024);
  NEXTO(0);
  BAR(); o_unit<NQT, NNT, true, true>(acc, of, sa, sb, oa, wa0, wa1); NEXTO(1);
  WAITV(0);
#pragma unroll 1
  for (int j = 2; j < 32; j += 2) {
    BAR(); o_unit<NQT, NNT, true, false>(acc, of, sb, sa, oa, wa0, wa1); NEXTO(j);
    BAR(); o_unit<NQT, NNT, true, false>(acc, of, sa, sb, oa, wa0, wa1); NEXTO(j + 1);
  }
  o_unit<NQT, NNT, false, false>(acc, of, sb, sa, 0, 0, 0);
  asm volatile("s_nop 7\n\ts_nop 1" ::: "memory"); SBAR();
  STAMP2(26);
  const unsigned lane = lane_id();
  if (nb == kq) {
    unsigned ra = OPART + q0 * 5120 + lane * 16; PIN(ra);
#pragma unroll
    for (int q = 0; q < NQT; ++q)
#pragma unroll
      for (int n = 0; n < NNT; ++n) *(LAS f32x4*)(ra + (q * 5 + n) * 1024) = acc[q][n];
  } else {
    const __amdgpu_buffer_rsrc_t srdS = __builtin_amdgcn_make_buffer_rsrc((void*)(wsb + WS_X + (size_t)(blk * 4 + nb) * 46080), (short)0, 46080, 0x00020000);
#pragma unroll
    for (int q = 0; q < NQT; ++q)
#pragma unroll
      for (int u = 0; u < (NNT + 1) / 2; ++u) {
        const f32x4 a = acc[q][2 * u], c2 = acc[q][2 * u + 1 < NNT ? 2 * u + 1 : 2 * u];
        __builtin_amdgcn_raw_buffer_store_b128(u32x4{cvtpk(a[0], a[1]), cvtpk(a[2], a[3]), cvtpk(c2[0], c2[1]), cvtpk(c2[2], c2[3])}, srdS, lane * 16, ((q0 + q) * 3 + u) * 1024, XAUX);
      }
    WAITV(0);
  }
}
DI void oproj_finish(u16* __restrict__ outp, char* __restrict__ wsb, unsigned q, unsigned kq, unsigned blk) {
  const unsigned lane = lane_id(), li = lane & 15, g = lane >> 4, nnt = 5 - (kq & 1);
  BAR();
  STAMP2(27);
  unsigned* fl = (unsigned*)(wsb + WS_F);
  if (q == 8 && lane == 0) STA(fl + blk * 64 + 3, 1u);
  { const unsigned* pf = fl + ((blk & ~48u) + 16 * (lane & 3)) * 64 + 3;
    for (int spin = 0; spin < SPIN_CAP && !__all(LDA(pf) == 1u); ++spin) __builtin_amdgcn_s_sleep(1);
    __builtin_amdgcn_fence(__ATOMIC_ACQUIRE, "wavefront"); }
  STAMP2(28);
  f32x4 px[3][5];
  u32x4 pw[3][3];
#pragma unroll
  for (int p = 0; p < 3; ++p) {
    const unsigned pb = (blk & ~48u) + 16 * ((kq + 1 + p) & 3);
    const __amdgpu_buffer_rsrc_t srdP = __builtin_amdgcn_make_buffer_rsrc((void*)(wsb + WS_X + (size_t)(pb * 4 + kq) * 46080), (short)0, 46080, 0x00020000);
#pragma unroll
    for (int u = 0; u < 3; ++u) pw[p][u] = __builtin_amdgcn_raw_buffer_load_b128(srdP, lane * 16, (q * 3 + (u < 2 ? u : nnt - 3)) * 1024, 16);
  }
  SBAR();
#pragma unroll
  for (int p = 0; p < 3; ++p)
#pragma unroll
    for (int n = 0; n < 5; ++n) { const unsigned lo = pw[p][n >> 1][2 * (n & 1)], hi = pw[p][n >> 1][2 * (n & 1) + 1];
      px[p][n] = f32x4{__uint_as_float(lo << 16), __uint_as_float(lo & 0xffff0000u), __uint_as_float(hi << 16), __uint_as_float(hi & 0xffff0000u)}; }
  const unsigned tb = OPART + q * 5120;
  f32x4 own[5];
#pragma unroll
  for (int n = 0; n < 5; ++n) own[n] = *(const LAS f32x4*)(tb + (n < 4 ? n : nnt - 1) * 1024 + lane * 16);
#pragma unroll
  for (int n = 0; n < 5; ++n) {
    const f32x4 a = (own[n] + px[0][n]) + (px[1][n] + px[2][n]);
    *(LAS u32x2*)(tb + li * 176 + g * 8 + n * 32) = u32x2{cvtpk(a[0], a[1]), cvtpk(a[2], a[3])};
  }
#pragma unroll
  for (unsigned i = 0; i < 3; ++i) {
    const unsigned ch = i * 64 + lane, row = ch / 10, cc = ch - row * 10;
    if (row < 16 && cc < 2 * nnt) *(u32x4*)(outp + (size_t)(16 * q + row) * (2 * DM) + cc * 8) = *(const LAS u32x4*)(tb + row * 176 + cc * 16);
  }
}

__global__ __launch_bounds__(768, 3) void k_fused(const u16* __restrict__ xA, const u16* __restrict__ kA, const u16* __restrict__ vA, const u16* __restrict__ wqA, const u16* __restrict__ woA,
                                                  const u16* __restrict__ xB, const u16* __restrict__ kB, const u16* __restrict__ vB, const u16* __restrict__ wqB, const u16* __restrict__ woB,
                                                  char* __restrict__ wsb, u16* __restrict__ out) {
  extern __shared__ __attribute__((aligned(16))) char smem[];
  const unsigned wid12 = rfl(threadIdx.x >> 6), half = wid12 >= 6, wid = wid12 - 6 * half, lb = half * GRP;
  const unsigned blk = blockIdx.x, idx = blk >> 3, pair = (blk & 7) * 4 + (idx >> 3), m = idx & 7, br = pair >> 4, b = pair & 15;
  const unsigned hg0 = (blk + half) & 3, kq = m >> 1, g0 = ((blk ^ kq) & 1) ? (blk + 1) & 3 : blk & 3;
  char* op = wsb + WS_O + (size_t)pair * NH * ITEM_B;
  const u16* kb = (br ? kB : kA) + (size_t)b * NH * SK * DH; const u16* vb = (br ? vB : vA) + (size_t)b * NH * SK * DH;
  Hs hs;
  if (wid < 4) {
    att_wave<2>(op, kb, vb, 2 * wid, m, hg0, lb, hs);
    const unsigned t = half ? 3 - ((wid + 2) & 3) : wid, nb = 2 * half + (t & 1);
    if (t == 0) oproj_consumer<5, 5>(wsb, nb, kq, blk);
    else if (t == 1) oproj_consumer<5, 4>(wsb, nb, kq, blk);
    else if (t == 2) oproj_consumer<4, 5>(wsb, nb, kq, blk);
    else oproj_consumer<4, 4>(wsb, nb, kq, blk);
  } else if (wid == 4) {
    if (!half) hs_entry(hs, wsb, blk);
    att_wave<1>(op, kb, vb, 8, m, hg0, lb, hs);
    if (half) {
      OLd ol; ol.init(op, br ? woB : woA, m, g0, 1, pair & 3);
      WAITV(0);
      ol.run(1);
    } else {
      WAITV(0);
      RBAR(); RBAR(); RBAR();
      if (lane_id() == 0) STA(hs.mine + 2, 1u);
#pragma unroll 1
      for (int j = 3; j < 8; ++j) RBAR();
      hs_post(hs.peer + 2, OBOX);
      RBAR(); RBAR();
      hs_wait_flag(hs, hs_take(OBOX), 2);
      RBAR(); RBAR(); RBAR();
      WAITV(0);
    }
    return;
  } else {
    OLd ol;
    loader(br ? wqB : wqA, (br ? xB : xA) + (size_t)b * SQ * DM, kb, vb, m, hg0, 0, half, ol, op, br ? woB : woA, g0, pair & 3);
    ol.run(0);
    if (half) return;
  }
  oproj_finish(out + (size_t)b * SQ * (2 * DM) + br * DM + (m & 1) * 288 + 16 * ((9 * kq + 1) >> 1), wsb, wid < 4 ? wid + 4 * half : 8, kq, blk);
}

extern "C" void kernel_launch(void* const* d_in, const int* in_sizes, int n_in, void* d_out, int out_size, void* d_ws, size_t ws_size, hipStream_t stream) {
  (void)hipFuncSetAttribute((const void*)k_fused, hipFuncAttributeMaxDynamicSharedMemorySize, LDS_BYTES);
  k_fused<<<256, 768, LDS_BYTES, stream>>>((const u16*)d_in[0], (const u16*)d_in[1], (const u16*)d_in[2], (const u16*)d_in[3], (const u16*)d_in[4],
                                           (const u16*)d_in[5], (const u16*)d_in[6], (const u16*)d_in[7], (const u16*)d_in[8], (const u16*)d_in[9], (char*)d_ws, (u16*)d_out);
}
```

```cpp
#include <hip/hip_runtime.h>
#include <stdint.h>

typedef short    bf16x8 __attribute__((ext_vector_type(8)));
typedef short    s16x4  __attribute__((ext_vector_type(4)));
typedef float    f32x4  __attribute__((ext_vector_type(4)));
typedef float    f32x2  __attribute__((ext_vector_type(2)));
typedef __bf16   bf16x2 __attribute__((ext_vector_type(2)));
typedef unsigned u32x4  __attribute__((ext_vector_type(4)));
typedef unsigned u32x2  __attribute__((ext_vector_type(2)));
typedef unsigned short u16;
#define DI __device__ __forceinline__
#define LAS __attribute__((address_space(3)))
#define MFMA16(a, b, c) __builtin_amdgcn_mfma_f32_16x16x32_bf16(a, b, c, 0, 0, 0)
#define SBAR() __builtin_amdgcn_sched_barrier(0)
#define PIN(x) asm volatile("" : "+v"(x))
#define MFMA16_V(acc, a, b) asm volatile("v_mfma_f32_16x16x32_bf16 %0, %1, %2, %0" : "+v"(acc) : "v"(a), "v"(b))
#define MFMA16_Z(d, a, b) asm volatile("v_mfma_f32_16x16x32_bf16 %0, %1, %2, 0" : "=&v"(d) : "v"(a), "v"(b))
#define MFMA16_C(d, a, b, cc) asm volatile("v_mfma_f32_16x16x32_bf16 %0, %1, %2, %3" : "=&v"(d) : "v"(a), "v"(b), "v"(cc))

constexpr int NB = 16, SQ = 144, DM = 576, NH = 32, SK = 512, DH = 128, HD = NH * DH;
constexpr int ITEM_B = 9 * 4 * 1024;
constexpr size_t WS_O = 0;
constexpr size_t WS_X = (size_t)1024 * ITEM_B, WS_F = WS_X + (size_t)256 * 4 * 46080;
constexpr float QSCALE = 0.08838834764831845f * 1.4426950408889634f;

DI unsigned rfl(unsigned x) { return __builtin_amdgcn_readfirstlane(x); }
DI unsigned lane_id() { unsigned l; asm volatile("v_mbcnt_lo_u32_b32 %0, -1, 0\n\tv_mbcnt_hi_u32_b32 %0, -1, %0" : "=v"(l)); return l; }
DI unsigned cvtpk(float lo, float hi) { f32x2 v = {lo, hi}; bf16x2 b = __builtin_convertvector(v, bf16x2); return __builtin_bit_cast(unsigned, b); }
DI bf16x8 lds128(unsigned a) { return *(const LAS bf16x8*)a; }
DI s16x4 ldstr(unsigned a) { return __builtin_bit_cast(s16x4, __builtin_amdgcn_ds_read_tr16_b64_v4i16((LAS s16x4*)a)); }
DI bf16x8 cat8(s16x4 lo, s16x4 hi) { return __builtin_shufflevector(lo, hi, 0, 1, 2, 3, 4, 5, 6, 7); }
DI void dma4(unsigned m0v, unsigned voff, __amdgpu_buffer_rsrc_t srd) {
  asm volatile("s_mov_b32 m0, %0\n\ts_nop 3\n\tbuffer_load_dword %1, %2, 0 offen lds" :: "s"(m0v), "v"(voff), "s"(srd) : "memory");
}
#define TILE8_BODY(POL) \
  "s_nop 3\n\ts_mov_b32 m0, %1\n\ts_mov_b32 %0, %2\n\t" \
  "buffer_load_dwordx4 %3, %5, %0 offen " POL " lds\n\ts_add_u32 m0, m0, 896\n\ts_nop 0\n\tbuffer_load_dwordx4 %3, %5, %0 offen offset:128 " POL " lds\n\ts_add_u32 m0, m0, 1152\n\ts_add_u32 %0, %0, %6\n\t" \
  "buffer_load_dwordx4 %4, %5, %0 offen " POL " lds\n\ts_add_u32 m0, m0, 896\n\ts_nop 0\n\tbuffer_load_dwordx4 %4, %5, %0 offen offset:128 " POL " lds\n\ts_add_u32 m0, m0, 1152\n\ts_add_u32 %0, %0, %6\n\t" \
  "buffer_load_dwordx4 %3, %5, %0 offen " POL " lds\n\ts_add_u32 m0, m0, 896\n\ts_nop 0\n\tbuffer_load_dwordx4 %3, %5, %0 offen offset:128 " POL " lds\n\ts_add_u32 m0, m0, 1152\n\ts_add_u32 %0, %0, %6\n\t" \
  "buffer_load_dwordx4 %4, %5, %0 offen " POL " lds\n\ts_add_u32 m0, m0, 896\n\ts_nop 0\n\tbuffer_load_dwordx4 %4, %5, %0 offen offset:128 " POL " lds"
template <bool NT, int D2> DI void dma_tile8(unsigned lo, unsigned so, unsigned va, unsigned vb, __amdgpu_buffer_rsrc_t srd) {
  unsigned t;
  if constexpr (NT) asm volatile(TILE8_BODY("nt") : "=&s"(t) : "s"(lo), "s"(so), "v"(va), "v"(vb), "s"(srd), "i"(D2) : "memory", "scc");
  else              asm volatile(TILE8_BODY("")   : "=&s"(t) : "s"(lo), "s"(so), "v"(va), "v"(vb), "s"(srd), "i"(D2) : "memory", "scc");
}
template <int D2> DI void dma_tile9(unsigned lo, unsigned so, unsigned va, __amdgpu_buffer_rsrc_t srd) {
  unsigned t;
  asm volatile("s_nop 3\n\ts_mov_b32 m0, %1\n\ts_mov_b32 %0, %2\n\t.rept 9\n\t"
               "buffer_load_dwordx4 %3, %4, %0 offen lds\n\ts_add_u32 m0, m0, 2048\n\ts_add_u32 %0, %0, %5\n\t.endr"
               : "=&s"(t) : "s"(lo), "s"(so), "v"(va), "s"(srd), "i"(D2) : "memory", "scc");
}
#define WAITV(n) asm volatile("s_waitcnt vmcnt(" #n ")" ::: "memory")
#define BAR() do { asm volatile("" ::: "memory"); __builtin_amdgcn_s_waitcnt(0xC07F); asm volatile("s_barrier" ::: "memory"); } while (0)
#define RBAR() asm volatile("s_barrier" ::: "memory")
constexpr unsigned GRP = 65536, WRING = 3 * 8192, XSLOT = 18432, XB = 126976, XA = XB + XSLOT, LDS_BYTES = 163840;
constexpr unsigned kslot(int i) { return 8192 * ((i & 3) == 0 ? 0 : (i & 3) == 1 ? 1 : (i & 3) == 2 ? 5 : 3); }
constexpr unsigned vslot(int i) { return 8192 * ((i & 3) == 0 ? 2 : (i & 3) == 1 ? 6 : (i & 3) == 2 ? 4 : 7); }
static_assert(XA + XSLOT == LDS_BYTES, "LDS map");


DI float red_g_max(float m) {
  auto r = __builtin_amdgcn_permlane32_swap(__float_as_uint(m), __float_as_uint(m), false, false);
  m = fmaxf(__uint_as_float(r[0]), __uint_as_float(r[1]));
  auto s = __builtin_amdgcn_permlane16_swap(__float_as_uint(m), __float_as_uint(m), false, false);
  return fmaxf(__uint_as_float(s[0]), __uint_as_float(s[1]));
}
DI float red_g_sum(float m) {
  auto r = __builtin_amdgcn_permlane32_swap(__float_as_uint(m), __float_as_uint(m), false, false);
  m = __uint_as_float(r[0]) + __uint_as_float(r[1]);
  auto s = __builtin_amdgcn_permlane16_swap(__float_as_uint(m), __float_as_uint(m), false, false);
  return __uint_as_float(s[0]) + __uint_as_float(s[1]);
}

template <int NQ> struct AttC {
  f32x4 O[NQ][8]; bf16x8 Q[NQ][4]; f32x4 S[2][NQ][2]; f32x4 negm[NQ]; float l[NQ];
  unsigned kb, vb0, vb1;
};
template <int NQ> struct Tab;
template <> struct Tab<2> {
  static constexpr int NPRE = 6;
  static constexpr int pre[6] = {100, 101, 102, 103, 104, 105};
  static constexpr int gx[16][2] = {{106, 0}, {107, 0}, {200, 201}, {110, 0}, {202, 203}, {111, 0}, {112, 0}, {113, 0}, {114, 0}, {210, 211}, {115, 0}, {116, 0}, {117, 0}, {300, 310}, {212, 213}, {301, 311}};
  static constexpr int gy[16] = {302, 312, 303, 313, 304, 314, 305, 315, 306, 316, 307, 317, 0, 0, 0, 0};
  static constexpr int all[24] = {100, 101, 102, 103, 104, 105, 106, 107, 110, 200, 201, 202, 203, 111, 112, 113, 114, 115, 116, 117, 210, 211, 212, 213};
  static constexpr int ady[16] = {300, 310, 301, 311, 302, 312, 303, 313, 304, 314, 305, 315, 306, 316, 307, 317};
};
template <> struct Tab<1> {
  static constexpr int NPRE = 4;
  static constexpr int pre[4] = {100, 101, 102, 103};
  static constexpr int gx[8][2] = {{104, 0}, {105, 0}, {200, 201}, {106, 0}, {107, 0}, {300, 301}, {202, 203}, {302, 303}};
  static constexpr int gy[8] = {304, 305, 306, 307, 0, 0, 0, 0};
  static constexpr int all[12] = {100, 101, 102, 103, 104, 105, 106, 107, 200, 201, 202, 203};
  static constexpr int ady[8] = {300, 301, 302, 303, 304, 305, 306, 307};
};
template <int NQ, int P, bool QK> struct Step {
  static constexpr int X = P & 1;
  static constexpr unsigned ks_ = kslot(P + 1), vs_ = vslot(P);
  bf16x8 fr[4]; float e[NQ][8]; unsigned pw[NQ][4]; u32x4 Pw[NQ];
  template <int OP> DI void op(AttC<NQ>& c) {
    constexpr int ty = OP / 100, qt = (OP / 10) % 10, j = OP % 10;
    if constexpr (ty == 1) { e[qt][j] = c.S[X][qt][j >> 2][j & 3]; asm volatile("v_exp_f32 %0, %0" : "+v"(e[qt][j])); }
    if constexpr (ty == 2) asm volatile("v_cvt_pk_bf16_f32 %0, %1, %2" : "=v"(pw[qt][j]) : "v"(e[qt][2 * j]), "v"(e[qt][2 * j + 1]));
    if constexpr (ty == 3) asm volatile("v_add_f32 %0, %0, %1" : "+v"(c.l[qt]) : "v"(e[qt][j]));
  }
  template <int R> DI void rd(AttC<NQ>& c) {
    if constexpr (R < 8) fr[R & 3] = lds128(c.kb + ks_ + 4096 * (R & 1) + 512 * (R >> 1));
    else if constexpr (R < 16) {
      constexpr int dt = R - 8;
      const unsigned vb = ((dt & 1) ? c.vb1 : c.vb0) + vs_ + 512 * (dt >> 1);
      fr[R & 3] = cat8(ldstr(vb), ldstr(vb + 4096));
    }
    SBAR();
  }
  template <int I> DI void gx(AttC<NQ>& c) {
    constexpr int K = I / NQ, qt = I % NQ, s = K >> 1, kt = K & 1;
    if constexpr (s == 0) MFMA16_C(c.S[X ^ 1][qt][kt], fr[K & 3], c.Q[qt][s], c.negm[qt]); else MFMA16_V(c.S[X ^ 1][qt][kt], fr[K & 3], c.Q[qt][s]);
    if constexpr (qt == NQ - 1) { SBAR(); rd<K + 4>(c); }
    op<Tab<NQ>::gx[I][0]>(c); op<Tab<NQ>::gx[I][1]>(c);
    if constexpr (I + 1 < 8 * NQ) gx<I + 1>(c);
  }
  template <int I> DI void gy(AttC<NQ>& c) {
    constexpr int Dt = I / NQ, qt = I % NQ;
    MFMA16_V(c.O[qt][Dt], fr[Dt & 3], Pw[qt]);
    if constexpr (qt == NQ - 1) { SBAR(); rd<Dt + 12>(c); }
    op<(QK ? Tab<NQ>::gy[I] : Tab<NQ>::ady[I])>(c);
    if constexpr (I + 1 < 8 * NQ) gy<I + 1>(c);
  }
  template <int I> DI void pre(AttC<NQ>& c) {
    op<(QK ? Tab<NQ>::pre[I < Tab<NQ>::NPRE ? I : 0] : Tab<NQ>::all[I])>(c);
    if constexpr (I + 1 < (QK ? Tab<NQ>::NPRE : 12 * NQ)) pre<I + 1>(c);
  }
  DI void run(AttC<NQ>& c) {
    if constexpr (QK) {
      rd<0>(c); rd<1>(c); rd<2>(c); rd<3>(c);
      pre<0>(c);
      gx<0>(c);
    } else {
      rd<8>(c); rd<9>(c); rd<10>(c); rd<11>(c);
      pre<0>(c);
      asm volatile("s_nop 1");
    }
#pragma unroll
    for (int qt = 0; qt < NQ; ++qt) Pw[qt] = u32x4{pw[qt][0], pw[qt][1], pw[qt][2], pw[qt][3]};
    gy<0>(c);
  }
};

template <int NQ, int P, bool QK> DI void att_step(AttC<NQ>& c) { BAR(); SBAR();
  Step<NQ, P, QK> s; s.run(c); SBAR(); }

template <int NQ> DI void att_prologue(AttC<NQ>& c) {
#pragma unroll
  for (int s = 0; s < 4; ++s)
#pragma unroll
    for (int kt = 0; kt < 2; ++kt) {
      const bf16x8 kf = lds128(c.kb + 4096 * kt + 512 * s);
#pragma unroll
      for (int qt = 0; qt < NQ; ++qt) c.S[0][qt][kt] = MFMA16(kf, c.Q[qt][s], s == 0 ? (f32x4{0, 0, 0, 0}) : c.S[0][qt][kt]);
    }
#pragma unroll
  for (int qt = 0; qt < NQ; ++qt) {
    float mx = c.S[0][qt][0][0];
#pragma unroll
    for (int j = 1; j < 8; ++j) mx = fmaxf(mx, c.S[0][qt][j >> 2][j & 3]);
    mx = red_g_max(mx);
    c.negm[qt] = f32x4{-mx, -mx, -mx, -mx};
    c.S[0][qt][0] += c.negm[qt]; c.S[0][qt][1] += c.negm[qt];
    c.l[qt] = 0.f;
#pragma unroll
    for (int dt = 0; dt < 8; ++dt) c.O[qt][dt] = f32x4{0, 0, 0, 0};
  }
}
template <int NQ> DI void att_redo_true_max(AttC<NQ>& c, const u16* __restrict__ kh, const u16* __restrict__ vh) {
  const unsigned lane = lane_id(), li = lane & 15, g = lane >> 4;
  const u16* kp = kh + li * DH + g * 8;
  float mx[NQ];
#pragma unroll
  for (int qt = 0; qt < NQ; ++qt) mx[qt] = -__builtin_inff();
#pragma unroll 1
  for (int kt = 0; kt < SK / 16; ++kt) {
    f32x4 s4[NQ];
#pragma unroll
    for (int qt = 0; qt < NQ; ++qt) s4[qt] = f32x4{0, 0, 0, 0};
#pragma unroll
    for (int s = 0; s < 4; ++s) {
      const bf16x8 kf = *(const bf16x8*)(kp + kt * 16 * DH + s * 32);
#pragma unroll
      for (int qt = 0; qt < NQ; ++qt) s4[qt] = MFMA16(kf, c.Q[qt][s], s4[qt]);
    }
#pragma unroll
    for (int qt = 0; qt < NQ; ++qt) mx[qt] = fmaxf(fmaxf(fmaxf(mx[qt], s4[qt][0]), fmaxf(s4[qt][1], s4[qt][2])), s4[qt][3]);
  }
#pragma unroll
  for (int qt = 0; qt < NQ; ++qt) {
    mx[qt] = red_g_max(mx[qt]); c.l[qt] = 0.f;
#pragma unroll
    for (int dt = 0; dt < 8; ++dt) c.O[qt][dt] = f32x4{0, 0, 0, 0};
  }
#pragma unroll 1
  for (int st = 0; st < SK / 32; ++st) {
    u32x4 pw[NQ];
#pragma unroll
    for (int kt = 0; kt < 2; ++kt) {
      f32x4 s4[NQ];
#pragma unroll
      for (int qt = 0; qt < NQ; ++qt) s4[qt] = f32x4{-mx[qt], -mx[qt], -mx[qt], -mx[qt]};
#pragma unroll
      for (int s = 0; s < 4; ++s) {
        const bf16x8 kf = *(const bf16x8*)(kp + (2 * st + kt) * 16 * DH + s * 32);
#pragma unroll
        for (int qt = 0; qt < NQ; ++qt) s4[qt] = MFMA16(kf, c.Q[qt][s], s4[qt]);
      }
#pragma unroll
      for (int qt = 0; qt < NQ; ++qt) {
        float e[4];
#pragma unroll
        for (int j = 0; j < 4; ++j) { e[j] = __builtin_amdgcn_exp2f(s4[qt][j]); c.l[qt] += e[j]; }
        pw[qt][2 * kt] = cvtpk(e[0], e[1]); pw[qt][2 * kt + 1] = cvtpk(e[2], e[3]);
      }
    }
    const u16* vp = vh + (32 * st + 4 * g) * DH + li;
#pragma unroll
    for (int dt = 0; dt < 8; ++dt) {
      bf16x8 vf;
#pragma unroll
      for (int j = 0; j < 8; ++j) vf[j] = (short)vp[(16 * (j >> 2) + (j & 3)) * DH + 16 * dt];
#pragma unroll
      for (int qt = 0; qt < NQ; ++qt) c.O[qt][dt] = MFMA16(vf, __builtin_bit_cast(bf16x8, pw[qt]), c.O[qt][dt]);
      SBAR();
    }
  }
#pragma unroll
  for (int qt = 0; qt < NQ; ++qt) c.l[qt] = red_g_sum(c.l[qt]);
}
template <int NQ> DI void att_epilogue(AttC<NQ>& c, char* ob, const u16* kh, const u16* vh) {
  bool bad = false;
#pragma unroll
  for (int qt = 0; qt < NQ; ++qt) { c.l[qt] = red_g_sum(c.l[qt]); bad |= !(c.l[qt] < 16777216.0f); }
  if (__builtin_expect(__any(bad), 0)) att_redo_true_max<NQ>(c, kh, vh);
#pragma unroll
  for (int qt = 0; qt < NQ; ++qt) {
    const float inv = __builtin_amdgcn_rcpf(c.l[qt]);
#pragma unroll
    for (int s = 0; s < 4; ++s) {
      const f32x4 a = c.O[qt][2 * s] * inv, bb = c.O[qt][2 * s + 1] * inv;
      u32x4 w = {cvtpk(a[0], a[1]), cvtpk(a[2], a[3]), cvtpk(bb[0], bb[1]), cvtpk(bb[2], bb[3])};
      asm volatile("global_store_dwordx4 %0, %1, off sc1\n\ts_nop 1" :: "v"(ob + (qt * 4 + s) * 1024), "v"(w) : "memory");
    }
  }
}

DI bf16x8 q_frag(const f32x4& t0, const f32x4& t1) {
  const f32x4 u0 = t0 * QSCALE, u1 = t1 * QSCALE;
  unsigned a0 = cvtpk(u0[0], u0[1]), a1 = cvtpk(u0[2], u0[3]), b0 = cvtpk(u1[0], u1[1]), b1 = cvtpk(u1[2], u1[3]);
  auto r0 = __builtin_amdgcn_permlane32_swap(a0, b0, false, false);
  auto r1 = __builtin_amdgcn_permlane32_swap(a1, b1, false, false);
  auto s0 = __builtin_amdgcn_permlane16_swap(r0[0], r0[1], false, false);
  auto s1 = __builtin_amdgcn_permlane16_swap(r1[0], r1[1], false, false);
  u32x4 w = {s0[0], s1[0], s0[1], s1[1]};
  return __builtin_bit_cast(bf16x8, w);
}
template <int NR, int WS, int XO, bool Z = false>
DI void q_unit(f32x4 (&qa)[NR][4], const bf16x8 (&wc)[4], bf16x8 (&wn)[4], bf16x8 (&xc)[NR], const bf16x8& lc, bf16x8 (&xn)[NR], bf16x8& ln, unsigned w0, unsigned w1, const unsigned (&xa)[3]) {
#define XADDR(row) (xa[(row) >> 1] + ((row) & 1) * 2048 + XO)
#pragma unroll
  for (int q = 0; q < NR; ++q) {
#pragma unroll
    for (int d = 0; d < 4; ++d) {
      if constexpr (Z) MFMA16_Z(qa[q][d], wc[d], q < NR - 1 ? xc[q] : lc); else MFMA16_V(qa[q][d], wc[d], q < NR - 1 ? xc[q] : lc);
      SBAR();
      const int i = 4 * q + d;
      if (WS >= 0 && i < 8 && !(i & 1)) { const int u = i >> 1; const unsigned a = ((u & 1) ? w1 : w0) + 8192 * WS + 512 * (u >> 1); wn[u] = cat8(ldstr(a), ldstr(a + 256)); }
      if (XO >= 0 && (i & 1) && (i >> 1) < NR) xn[i >> 1] = lds128(XADDR(i >> 1));
      if (XO >= 0 && i == 10) ln = lds128(XADDR(NR - 1) + 512);
      SBAR();
    }
    if (XO >= 0 && q < NR - 1) { xc[q] = lds128(XADDR(q) + 512); SBAR(); }
  }
#undef XADDR
}

#define LDA(p) __hip_atomic_load((p), __ATOMIC_RELAXED, __HIP_MEMORY_SCOPE_AGENT)
#define STA(p, v) __hip_atomic_store((p), (v), __ATOMIC_RELAXED, __HIP_MEMORY_SCOPE_AGENT)
constexpr int SPIN_CAP = 1 << 21;
struct Hs { unsigned* mine; unsigned* peer; unsigned me, x, e; bool ok; };
DI void hs_post(const unsigned* p, unsigned box) { asm volatile("s_mov_b32 m0, %0\n\ts_nop 3\n\tglobal_load_lds_dword %1, off sc1" :: "s"(box), "v"(p) : "memory"); }
DI unsigned hs_take(unsigned box) { WAITV(0); return *(volatile LAS unsigned*)(box + 4 * lane_id()); }
DI void hs_entry(Hs& h, char* wsb, unsigned blk) {
  const unsigned lane = lane_id();
  h.me = (blk >> 3) & 7;
  h.mine = (unsigned*)(wsb + WS_F) + blk * 64;
  h.peer = (unsigned*)(wsb + WS_F) + ((blk & ~56u) + 8 * (lane & 7)) * 64;
  if (lane >= 1 && lane <= 3) STA(h.mine + lane, 0u);
  const unsigned y0 = LDA(h.peer + 32 + h.me);
  unsigned x = rfl(y0) + 1;
  while (__any(y0 == x)) ++x;
  h.x = x; h.e = 0; h.ok = false;
  WAITV(0);
  if (lane == 0) STA(h.mine, x);
}
DI void hs_wait_flag(const Hs& h, unsigned v, int word) {
  for (int spin = 0; !__all(v == 1u) && spin < SPIN_CAP; ++spin) { __builtin_amdgcn_s_sleep(2); v = LDA(h.peer + word); }
  __builtin_amdgcn_fence(__ATOMIC_ACQUIRE, "agent");
}
constexpr unsigned HBOX = 161792;
DI void hs_hook(Hs& h, int k) {
  const unsigned lane = lane_id();
  if (k == 0) hs_post(h.peer, HBOX);
  else if (k == 1) { h.e = hs_take(HBOX); if (lane < 8) STA(h.mine + 32 + lane, h.e); }
  else if (k == 4) hs_post(h.peer + 32 + h.me, HBOX);
  else if (k == 5) { h.ok = __all(hs_take(HBOX) == h.x); hs_post(h.peer, HBOX); }
  else if (k == 6) { const unsigned e = hs_take(HBOX); if (lane < 8 && e != h.e) STA(h.mine + 32 + lane, e); hs_post(h.peer + 1, HBOX); }
  else if (k == 7) {
    unsigned f = hs_take(HBOX);
    if (!h.ok) {
      for (int spin = 0; spin < SPIN_CAP; ++spin) {
        const unsigned e = LDA(h.peer);
        if (lane < 8) STA(h.mine + 32 + lane, e);
        if (__all(LDA(h.peer + 32 + h.me) == h.x)) break;
        __builtin_amdgcn_s_sleep(2);
      }
      WAITV(0);
      const unsigned e = LDA(h.peer);
      if (lane < 8) STA(h.mine + 32 + lane, e);
      f = 0;
    }
    hs_wait_flag(h, f, 1);
  }
}

constexpr unsigned EX = 122880;
template <int NR> DI void q_phase(bf16x8 (&own)[2][2], unsigned wid, unsigned lb) {
  asm volatile("; q phase %0" :: "i"(NR) : "memory");
  const unsigned lane = lane_id(), li = lane & 15, g = lane >> 4, pos = lane & 3, tq = li >> 2;
  unsigned w0 = lb + WRING + 1024 * (wid & 1) + 2048 * g + 64 * tq + 32 * (g & 1) + 8 * pos, w1 = w0 ^ 32u;
  const unsigned xl = XB + 1024 * (li >> 3) + 64 * (li & 7) + 16 * (g ^ (2 * (li >> 3)));
  unsigned xa[3] = {xl + wid * 4096, xl + (wid ^ 1) * 4096, xl + 16384};
  bf16x8 w[4], v[4], xA[NR], xB[NR], l0, l1;
  f32x4 qa[NR][4];
  PIN(w0); PIN(w1); PIN(xa[0]); PIN(xa[1]); if (NR == 5) PIN(xa[2]);
  BAR();
#pragma unroll
  for (int u = 0; u < 4; ++u) { const unsigned a = ((u & 1) ? w1 : w0) + 512 * (u >> 1); w[u] = cat8(ldstr(a), ldstr(a + 256)); }
#pragma unroll
  for (int q = 0; q < NR; ++q) { const unsigned a = xa[q >> 1] + (q & 1) * 2048 + XSLOT; xB[q] = lds128(a); if (q < NR - 1) xA[q] = lds128(a + 512); else l0 = lds128(a + 512); }
  BAR(); q_unit<NR, 1, -1, true>(qa, w, v, xB, xB[NR - 1], xA, l1, w0, w1, xa);
#pragma unroll 1
  for (int n = 0; n < 4; ++n) {
    WAITV(0);
    BAR(); q_unit<NR, 2, 0>(qa, v, w, xA, l0, xB, l1, w0, w1, xa);
    BAR(); q_unit<NR, 3, -1>(qa, w, v, xB, xB[NR - 1], xA, l0, w0, w1, xa);
    BAR(); q_unit<NR, 0, XSLOT>(qa, v, w, xA, l1, xB, l0, w0, w1, xa);
    BAR(); q_unit<NR, 1, -1>(qa, w, v, xB, xB[NR - 1], xA, l1, w0, w1, xa);
  }
  BAR(); q_unit<NR, -1, -1>(qa, v, w, xA, l0, xB, l1, w0, w1, xa);
  asm volatile("s_nop 7" ::: "memory"); SBAR();
  unsigned ea = EX + (lb ? 0 : 20480) + lane * 16; PIN(ea);
#pragma unroll
  for (int s = 0; s < 2; ++s) {
    *(LAS bf16x8*)(ea + wid * 4096 + s * 1024) = q_frag(qa[2][2 * s], qa[2][2 * s + 1]);
    *(LAS bf16x8*)(ea + wid * 4096 + 2048 + s * 1024) = q_frag(qa[3][2 * s], qa[3][2 * s + 1]);
    if constexpr (NR == 5) *(LAS bf16x8*)(ea + 16384 + wid * 2048 + s * 1024) = q_frag(qa[4][2 * s], qa[4][2 * s + 1]);
  }
#pragma unroll
  for (int q = 0; q < 2; ++q)
#pragma unroll
    for (int s = 0; s < 2; ++s) own[q][s] = q_frag(qa[q][2 * s], qa[q][2 * s + 1]);
}

template <int NQ> DI void att_wave(char* ows_pair, const u16* kbase, const u16* vbase, unsigned qt0, unsigned m, unsigned hg0, unsigned lb, Hs& hs, const u16* x) {
  asm volatile("; attention wave NQ=%0" :: "i"(NQ) : "memory");
#pragma unroll 1
  for (int r = 0; r < 2; ++r) {
    AttC<NQ> c;
    if constexpr (NQ == 2) {
      bf16x8 own[2][2];
      if (qt0 < 4) q_phase<5>(own, qt0 >> 1, lb); else q_phase<4>(own, qt0 >> 1, lb);
      unsigned ea = EX + (lb ? 0 : 20480) + (qt0 ^ 2) * 2048 + lane_id() * 16; PIN(ea);
      BAR();
      const bool hi = qt0 & 2;
#pragma unroll
      for (int q = 0; q < 2; ++q)
#pragma unroll
        for (int s = 0; s < 2; ++s) { const bf16x8 rc = lds128(ea + q * 2048 + s * 1024); c.Q[q][s] = hi ? rc : own[q][s]; c.Q[q][2 + s] = hi ? own[q][s] : rc; }
    } else {
      const __amdgpu_buffer_rsrc_t srdX = __builtin_amdgcn_make_buffer_rsrc((void*)x, (short)0, SQ * DM * 2, 0x00020000);
      const unsigned lane = lane_id(), half = lb != 0, vx = ((((lane >> 2) & 7) + 8 * half) * (DM * 2) + (lane >> 5) * 64 + (lane & 3) * 16) ^ (32u * half);
      BAR();
#pragma unroll 1
      for (int n = 1; n < 19; n += 2) {
        BAR();
        if (n < 14) { dma_tile9<16 * DM * 2>(((n & 2) ? XB : XA) + half * 1024, ((n + 3) >> 1) * 128, vx, srdX); WAITV(9); } else WAITV(0);
        BAR();         if (n == 1 && lb == 0 && r == 1 && lane == 0) STA(hs.mine + 1, 1u);
      }
      BAR();
      unsigned ea = EX + (lb ? 0 : 20480) + 16384 + lane_id() * 16; PIN(ea);
#pragma unroll
      for (int s = 0; s < 4; ++s) c.Q[0][s] = lds128(ea + s * 1024);
      __builtin_amdgcn_s_waitcnt(0xC07F);
    }
    { const unsigned lane = lane_id(), li = lane & 15, g = lane >> 4;
      c.kb = lb + 2048 * (li >> 3) + 64 * (li & 7) + 16 * (g ^ (2 * (li >> 3)));
      c.vb0 = lb + 2048 * (g >> 1) + 64 * (4 * (g & 1) + (li >> 2)) + 32 * (g & 1) + 8 * (lane & 3); c.vb1 = c.vb0 ^ 32u;
      PIN(c.kb); PIN(c.vb0); PIN(c.vb1); }
    if (NQ == 1 && lb == 0) hs_hook(hs, 4 * r);
    att_prologue<NQ>(c);
    SBAR();
#pragma unroll 1
    for (int ii = 0; ii < 3; ++ii) { att_step<NQ, 0, true>(c); att_step<NQ, 1, true>(c); att_step<NQ, 2, true>(c); att_step<NQ, 3, true>(c);
      if (NQ == 1 && lb == 0) hs_hook(hs, 4 * r + 1 + ii); }
    att_step<NQ, 0, true>(c); att_step<NQ, 1, true>(c);
    if (NQ == 1) WAITV(0);
    att_step<NQ, 2, true>(c); att_step<NQ, 3, false>(c);
    asm volatile("s_nop 7\n\ts_nop 1" ::: "memory"); SBAR();
    const unsigned h = 8 * ((hg0 + 2 * r) & 3) + m;
    att_epilogue<NQ>(c, ows_pair + (size_t)h * ITEM_B + qt0 * 4096 + lane_id() * 16, kbase + (size_t)h * SK * DH, vbase + (size_t)h * SK * DH);
  }
}

DI void wait_newer(int n) {
  if (n >= 32) { if (n >= 40) WAITV(40); else WAITV(32); }
  else if (n >= 16) { if (n >= 24) WAITV(24); else WAITV(16); }
  else if (n >= 8) WAITV(8); else WAITV(0);
}
DI unsigned oslot_o(unsigned s) { return s == 0 ? 131072 : GRP - 9216 + 9216 * s; }
DI unsigned oslot_w(unsigned s) { return s == 0 ? 140288 : 18432 * (s - 1); }
constexpr unsigned OPART = 93184;
constexpr unsigned OBOX = 55296;
struct OLd {
  __amdgpu_buffer_rsrc_t srd; unsigned vo[9], h0, isw, rot;
  DI void init(const char* op, const u16* wo, unsigned m, unsigned g0, unsigned isw_, unsigned rot_) {
    const unsigned lane = lane_id(); h0 = 8 * g0 + (m & 4); isw = isw_; rot = rot_;
    srd = isw ? __builtin_amdgcn_make_buffer_rsrc((void*)wo, (short)0, HD * DM * 2, 0x00020000) : __builtin_amdgcn_make_buffer_rsrc((void*)op, (short)0, NH * ITEM_B, 0x00020000);
#pragma unroll
    for (int p = 0; p < 9; ++p) { const unsigned n = 64 * p + lane, row = n / 36, ch = n - 36 * row;
      vo[p] = isw ? row * (DM * 2) + ((ch ^ ((row >> 1) & 2)) * 16) + (m & 1) * 576 : p * 4096 + lane * 16; }
  }
#define OPIECE(k) "buffer_load_dwordx4 %" #k ", %1, %2 offen lds\n\ts_add_u32 m0, m0, 1024\n\ts_nop 0\n\t"
  DI void issue(int j, unsigned part) const {
    const unsigned h = ((h0 + (j & 16)) & 31) + (((j >> 2) + rot) & 3), s = j & 3;
    const unsigned so = isw ? ((h * 4 + s) * 32 + 16 * part) * (DM * 2) : h * ITEM_B + s * 1024, lo = isw ? oslot_w(j & 3) + part * 9216 : oslot_o(j & 3);
    asm volatile("s_mov_b32 m0, %0\n\ts_nop 3\n\t" OPIECE(3) OPIECE(4) OPIECE(5) OPIECE(6) OPIECE(7) OPIECE(8) OPIECE(9) OPIECE(10) OPIECE(11)
                 :: "s"(lo), "s"(srd), "s"(so), "v"(vo[0]), "v"(vo[1]), "v"(vo[2]), "v"(vo[3]), "v"(vo[4]), "v"(vo[5]), "v"(vo[6]), "v"(vo[7]), "v"(vo[8]) : "memory", "scc");
  }
  DI void run(unsigned part) const {
#pragma unroll 1
    for (int j = 0; j < 32; ++j) {
      if (j == 0 || j == 30) WAITV(9); else if (j < 30) WAITV(18); else WAITV(0);
      RBAR();
      if (j == 0) issue(2, part);
      if (j + 3 < 32) issue(j + 3, part);
    }
  }
};

DI void loader(const u16* wq, const u16* x, const u16* kbase, const u16* vbase, unsigned m, unsigned hg0, unsigned half, OLd& ol, const char* op, const u16* wo, unsigned g0, unsigned rot) {
  const unsigned lane = lane_id(), row7 = (lane >> 2) & 7, sh = lane >> 5, pos = lane & 3, lb = half * GRP;
  const __amdgpu_buffer_rsrc_t srdW = __builtin_amdgcn_make_buffer_rsrc((void*)wq, (short)0, DM * HD * 2, 0x00020000);
  const __amdgpu_buffer_rsrc_t srdX = __builtin_amdgcn_make_buffer_rsrc((void*)x, (short)0, SQ * DM * 2, 0x00020000);
  const __amdgpu_buffer_rsrc_t srdK = __builtin_amdgcn_make_buffer_rsrc((void*)kbase, (short)0, NH * SK * DH * 2, 0x00020000);
  const __amdgpu_buffer_rsrc_t srdV = __builtin_amdgcn_make_buffer_rsrc((void*)vbase, (short)0, NH * SK * DH * 2, 0x00020000);
  const unsigned vw0 = row7 * (HD * 2) + sh * 64 + pos * 16, vw1 = vw0 ^ 32u;
  const unsigned vx = (row7 * (DM * 2) + sh * 64 + pos * 16) ^ (32u * half);
  const unsigned vk0 = row7 * 256 + sh * 64 + pos * 16, vk1 = vk0 ^ 32u, vv = row7 * 256 + sh * 64 + ((pos ^ (2 * (row7 >> 2))) * 16);
  int cnt = 0;
  auto issue_w = [&](unsigned h, int s) {
    dma_tile8<false, 8 * HD * 2>(lb + WRING + (s & 3) * 8192, s * 32 * (HD * 2) + h * (DH * 2), vw0, vw1, srdW);
    return cnt += 8;
  };
  auto issue_x = [&](int e) {
    dma_tile9<16 * DM * 2>(((e & 1) ? XB : XA) + half * 1024, e * 128 + half * 8 * (DM * 2), vx, srdX);
    return cnt += 9;
  };
  auto issue_k = [&](unsigned h, int t) {
    const unsigned q = t & 3, lo = q == 0 ? kslot(0) : q == 1 ? kslot(1) : q == 2 ? kslot(2) : kslot(3);
    dma_tile8<true, 2048>(lb + lo, h * (SK * DH * 2) + t * 8192, vk0, vk1, srdK);
    return cnt += 8;
  };
  auto issue_v = [&](unsigned h, int t) {
    const unsigned q = t & 3, lo = q == 0 ? vslot(0) : q == 1 ? vslot(1) : q == 2 ? vslot(2) : vslot(3);
    dma_tile8<true, 2048>(lb + lo, h * (SK * DH * 2) + t * 8192, vv, vv, srdV);
    return cnt += 8;
  };
  unsigned h = 8 * hg0 + m;
  int kx = 0;
  int n0, n1, n2, n3;
  issue_w(h, 0); n0 = issue_x(0); n1 = issue_w(h, 1); issue_w(h, 2);
  ol.init(op, wo, m, g0, half ^ 1, rot);
#pragma unroll 1
  for (int r = 0; r < 2; ++r) {
    const unsigned hn = 8 * ((hg0 + 2) & 3) + m;
    wait_newer(cnt - n0); RBAR();
    n2 = issue_x(1);
    if (r == 0) issue_w(h, 3);
    n3 = cnt;
#pragma unroll 1
    for (int G = 1; G < 15; ++G) {
      wait_newer(cnt - n1); RBAR();
      n1 = n2; n2 = n3;
      issue_w(h, G + 3);
      n3 = cnt;
      if (r == 0) { if (G == 8) kx = issue_k(h, 0); if (G == 11) issue_k(h, 1); if (G == 14) issue_v(h, 0); }
    }
    int a0 = cnt, a1, a2;
    wait_newer(cnt - n1); RBAR();
    issue_k(h, 2);
    wait_newer(cnt - n2); RBAR();
    a1 = issue_v(h, 1);
    wait_newer(cnt - n3); RBAR();
    issue_k(h, 3);
    wait_newer(cnt - kx); RBAR();
    a2 = issue_v(h, 2);
    RBAR();
#pragma unroll 1
    for (int i = 0; i < 16; ++i) {
      wait_newer(cnt - a0); RBAR();
      a0 = a1; a1 = a2;
      if (i < 12) issue_k(h, i + 4);
      if (i < 13) issue_v(h, i + 3);
      a2 = cnt;
      if (r == 0) {
        if (i == 12) issue_k(hn, 0);
        if (i == 13) { issue_k(hn, 1); issue_v(hn, 0); }
        if (i == 14) { issue_x(0); issue_w(hn, 2); issue_w(hn, 3); }
        if (i == 15) { n0 = issue_w(hn, 0); n1 = issue_w(hn, 1); }
      } else {
        if (i == 13 && ol.isw) { ol.issue(0, 0); ol.issue(0, 1); ol.issue(1, 0); ol.issue(1, 1); cnt += 36; }
        if (i == 14 && !ol.isw) { ol.issue(0, 0); ol.issue(1, 0); cnt += 18; }
      }
    }
    h = hn;
  }
}

template <int NQT, int NNT> struct OSet { bf16x8 w[NNT], ol; };
template <int NQT, int NNT, bool RD, bool Z> DI void o_unit(f32x4 (&acc)[NQT][NNT], bf16x8 (&of)[NQT - 1], const OSet<NQT, NNT>& cs, OSet<NQT, NNT>& ns, unsigned oa, unsigned wa0, unsigned wa1) {
#pragma unroll
  for (int q = 0; q < NQT; ++q) {
#pragma unroll
    for (int n = 0; n < NNT; ++n) {
      if constexpr (Z) MFMA16_Z(acc[q][n], cs.w[n], q < NQT - 1 ? of[q < NQT - 1 ? q : 0] : cs.ol); else MFMA16_V(acc[q][n], cs.w[n], q < NQT - 1 ? of[q < NQT - 1 ? q : 0] : cs.ol);
      SBAR();
      if (RD && q == 0) { const unsigned a = ((n & 1) ? wa1 : wa0) + 64 * (n >> 1); ns.w[n] = cat8(ldstr(a), ldstr(a + 9216)); }
      if (RD && q == 1 && n == 1) ns.ol = lds128(oa + (NQT - 1) * 1024);
      SBAR();
    }
    if (RD && q < NQT - 1) { of[q] = lds128(oa + q * 1024); SBAR(); }
  }
}
template <int NQT, int NNT> DI void oproj_consumer(char* __restrict__ wsb, unsigned nb, unsigned kq, unsigned blk) {
  asm volatile("; oproj consumer %0 x %1" :: "i"(NQT), "i"(NNT) : "memory");
  constexpr unsigned qh = NQT == 4, q0 = 5 * qh;
  f32x4 acc[NQT][NNT];
  bf16x8 of[NQT - 1]; OSet<NQT, NNT> sa, sb;
  unsigned oa, wa0, wa1;
  { const unsigned lane = lane_id(), li = lane & 15, g = lane >> 4, n0 = (9 * nb + 1) >> 1;
    const unsigned rb = oslot_w(0) + 576 * (4 * g + (li >> 2)) + 8 * (lane & 3);
    oa = oslot_o(0) + q0 * 1024 + lane * 16;
    wa0 = rb + 64 * (n0 >> 1) + 32 * ((n0 ^ g) & 1); wa1 = rb + 64 * ((n0 + 1) >> 1) + 32 * ((n0 ^ g ^ 1) & 1); }
#define NEXTO(j) do { const unsigned d_ = oslot_w(((j) + 1) & 3) - oslot_w((j) & 3); oa += oslot_o(((j) + 1) & 3) - oslot_o((j) & 3); wa0 += d_; wa1 += d_; PIN(oa); PIN(wa0); PIN(wa1); } while (0)
  PIN(oa); PIN(wa0); PIN(wa1);
  BAR();
#pragma unroll
  for (int n = 0; n < NNT; ++n) { const unsigned a = ((n & 1) ? wa1 : wa0) + 64 * (n >> 1); sa.w[n] = cat8(ldstr(a), ldstr(a + 9216)); }
#pragma unroll
  for (int q = 0; q < NQT - 1; ++q) of[q] = lds128(oa + q * 1024);
  sa.ol = lds128(oa + (NQT - 1) * 1024);
  NEXTO(0);
  BAR(); o_unit<NQT, NNT, true, true>(acc, of, sa, sb, oa, wa0, wa1); NEXTO(1);
  WAITV(0);
#pragma unroll 1
  for (int j = 2; j < 32; j += 2) {
    BAR(); o_unit<NQT, NNT, true, false>(acc, of, sb, sa, oa, wa0, wa1); NEXTO(j);
    BAR(); o_unit<NQT, NNT, true, false>(acc, of, sa, sb, oa, wa0, wa1); NEXTO(j + 1);
  }
  o_unit<NQT, NNT, false, false>(acc, of, sb, sa, 0, 0, 0);
  asm volatile("s_nop 7\n\ts_nop 1" ::: "memory"); SBAR();
  const unsigned lane = lane_id();
  if (nb == kq) {
    unsigned ra = OPART + q0 * 5120 + lane * 16; PIN(ra);
#pragma unroll
    for (int q = 0; q < NQT; ++q)
#pragma unroll
      for (int n = 0; n < NNT; ++n) *(LAS f32x4*)(ra + (q * 5 + n) * 1024) = acc[q][n];
  } else {
    const __amdgpu_buffer_rsrc_t srdS = __builtin_amdgcn_make_buffer_rsrc((void*)(wsb + WS_X + (size_t)(blk * 4 + nb) * 46080), (short)0, 46080, 0x00020000);
#pragma unroll
    for (int q = 0; q < NQT; ++q)
#pragma unroll
      for (int u = 0; u < (NNT + 1) / 2; ++u) {
        const f32x4 a = acc[q][2 * u], c2 = acc[q][2 * u + 1 < NNT ? 2 * u + 1 : 2 * u];
        __builtin_amdgcn_raw_buffer_store_b128(u32x4{cvtpk(a[0], a[1]), cvtpk(a[2], a[3]), cvtpk(c2[0], c2[1]), cvtpk(c2[2], c2[3])}, srdS, lane * 16, ((q0 + q) * 3 + u) * 1024, 16);
      }
    WAITV(0);
  }
}
DI void oproj_finish(u16* __restrict__ outp, char* __restrict__ wsb, unsigned q, unsigned kq, unsigned blk) {
  const unsigned lane = lane_id(), li = lane & 15, g = lane >> 4, nnt = 5 - (kq & 1);
  BAR();
  unsigned* fl = (unsigned*)(wsb + WS_F);
  if (q == 8 && lane == 0) STA(fl + blk * 64 + 3, 1u);
  { const unsigned* pf = fl + ((blk & ~48u) + 16 * (lane & 3)) * 64 + 3;
    for (int spin = 0; spin < SPIN_CAP && !__all(LDA(pf) == 1u); ++spin) __builtin_amdgcn_s_sleep(1);
    __builtin_amdgcn_fence(__ATOMIC_ACQUIRE, "wavefront"); }
  f32x4 px[3][5];
  u32x4 pw[3][3];
#pragma unroll
  for (int p = 0; p < 3; ++p) {
    const unsigned pb = (blk & ~48u) + 16 * ((kq + 1 + p) & 3);
    const __amdgpu_buffer_rsrc_t srdP = __builtin_amdgcn_make_buffer_rsrc((void*)(wsb + WS_X + (size_t)(pb * 4 + kq) * 46080), (short)0, 46080, 0x00020000);
#pragma unroll
    for (int u = 0; u < 3; ++u) pw[p][u] = __builtin_amdgcn_raw_buffer_load_b128(srdP, lane * 16, (q * 3 + (u < 2 ? u : nnt - 3)) * 1024, 16);
  }
  SBAR();
#pragma unroll
  for (int p = 0; p < 3; ++p)
#pragma unroll
    for (int n = 0; n < 5; ++n) { const unsigned lo = pw[p][n >> 1][2 * (n & 1)], hi = pw[p][n >> 1][2 * (n & 1) + 1];
      px[p][n] = f32x4{__uint_as_float(lo << 16), __uint_as_float(lo & 0xffff0000u), __uint_as_float(hi << 16), __uint_as_float(hi & 0xffff0000u)}; }
  const unsigned tb = OPART + q * 5120;
  f32x4 own[5];
#pragma unroll
  for (int n = 0; n < 5; ++n) own[n] = *(const LAS f32x4*)(tb + (n < 4 ? n : nnt - 1) * 1024 + lane * 16);
#pragma unroll
  for (int n = 0; n < 5; ++n) {
    const f32x4 a = (own[n] + px[0][n]) + (px[1][n] + px[2][n]);
    *(LAS u32x2*)(tb + li * 176 + g * 8 + n * 32) = u32x2{cvtpk(a[0], a[1]), cvtpk(a[2], a[3])};
  }
#pragma unroll
  for (unsigned i = 0; i < 3; ++i) {
    const unsigned ch = i * 64 + lane, row = ch / 10, cc = ch - row * 10;
    if (row < 16 && cc < 2 * nnt) {
      *(u32x4*)(outp + (size_t)(16 * q + row) * (2 * DM) + cc * 8) = *(const LAS u32x4*)(tb + row * 176 + cc * 16);
    }
  }
}

__global__ __launch_bounds__(768, 3) void k_fused(const u16* __restrict__ xA, const u16* __restrict__ kA, const u16* __restrict__ vA, const u16* __restrict__ wqA, const u16* __restrict__ woA,
                                                  const u16* __restrict__ xB, const u16* __restrict__ kB, const u16* __restrict__ vB, const u16* __restrict__ wqB, const u16* __restrict__ woB,
                                                  char* __restrict__ wsb, u16* __restrict__ out) {
  extern __shared__ __attribute__((aligned(16))) char smem[];
  const unsigned wid12 = rfl(threadIdx.x >> 6), half = wid12 >= 6, wid = wid12 - 6 * half, lb = half * GRP;
  const unsigned bx = blockIdx.x, pr_ = (bx & 6) * 4 + (bx >> 5), blk = (pr_ >> 2) | ((4 * (bx & 1) + ((bx >> 3) & 3)) << 3) | ((pr_ & 3) << 6), idx = blk >> 3, pair = (blk & 7) * 4 + (idx >> 3), m = idx & 7, br = pair >> 4, b = pair & 15;
  const unsigned hg0 = (blk + half) & 3, kq = m >> 1, g0 = ((blk ^ kq) & 1) ? (blk + 1) & 3 : blk & 3;
  char* op = wsb + WS_O + (size_t)pair * NH * ITEM_B;
  const u16* kb = (br ? kB : kA) + (size_t)b * NH * SK * DH; const u16* vb = (br ? vB : vA) + (size_t)b * NH * SK * DH;
  Hs hs;
  if (wid < 4) {
    att_wave<2>(op, kb, vb, 2 * wid, m, hg0, lb, hs, nullptr);
    const unsigned t = half ? 3 - ((wid + 2) & 3) : wid, nb = 2 * half + (t & 1);
    if (t == 0) oproj_consumer<5, 5>(wsb, nb, kq, blk);
    else if (t == 1) oproj_consumer<5, 4>(wsb, nb, kq, blk);
    else if (t == 2) oproj_consumer<4, 5>(wsb, nb, kq, blk);
    else oproj_consumer<4, 4>(wsb, nb, kq, blk);
  } else if (wid == 4) {
    if (!half) hs_entry(hs, wsb, blk);
    else {
      const __amdgpu_buffer_rsrc_t srdT = __builtin_amdgcn_make_buffer_rsrc((char*)((br ? xB : xA) + (size_t)b * SQ * DM) + m * 20736, (short)0, 20736, 0x00020000);
      __builtin_amdgcn_s_sleep(10);
#pragma unroll
      for (int p = 0; p < 3; ++p) { const unsigned n = p * 64 + lane_id(); dma4(vslot(3), (n < 162 ? n : 0) * 128, srdT); }
      {
        constexpr unsigned LN = (576 - 256) * 64 / 128;
        const __amdgpu_buffer_rsrc_t srdQ = __builtin_amdgcn_make_buffer_rsrc((char*)((blk & 1) ? wqB : wqA) + (size_t)256 * 8192 + (size_t)(blk >> 1) * LN * 128, (short)0, LN * 128, 0x00020000);
#pragma unroll
        for (unsigned p = 0; p < (LN + 63) / 64; ++p) { const unsigned n = p * 64 + lane_id(); dma4(vslot(3), (n < LN ? n : 0) * 128, srdQ); } }
    }
    att_wave<1>(op, kb, vb, 8, m, hg0, lb, hs, (br ? xB : xA) + (size_t)b * SQ * DM);
    if (half) {
      OLd ol; ol.init(op, br ? woB : woA, m, g0, 1, pair & 3);
      WAITV(0);
      ol.run(1);
    } else {
      WAITV(0);
      RBAR(); RBAR(); RBAR();
      if (lane_id() == 0) STA(hs.mine + 2, 1u);
#pragma unroll 1
      for (int j = 3; j < 8; ++j) RBAR();
      hs_post(hs.peer + 2, OBOX);
      RBAR(); RBAR();
      hs_wait_flag(hs, hs_take(OBOX), 2);
      RBAR(); RBAR(); RBAR();
      WAITV(0);
    }
    return;
  } else {
    OLd ol;
    loader(br ? wqB : wqA, (br ? xB : xA) + (size_t)b * SQ * DM, kb, vb, m, hg0, half, ol, op, br ? woB : woA, g0, pair & 3);
    ol.run(0);
    if (half) return;
  }
  oproj_finish(out + (size_t)b * SQ * (2 * DM) + br * DM + (m & 1) * 288 + 16 * ((9 * kq + 1) >> 1), wsb, wid < 4 ? wid + 4 * half : 8, kq, blk);
}

extern "C" void kernel_launch(void* const* d_in, const int* in_sizes, int n_in, void* d_out, int out_size, void* d_ws, size_t ws_size, hipStream_t stream) {
  (void)hipFuncSetAttribute((const void*)k_fused, hipFuncAttributeMaxDynamicSharedMemorySize, LDS_BYTES);
  k_fused<<<256, 768, LDS_BYTES, stream>>>((const u16*)d_in[0], (const u16*)d_in[1], (const u16*)d_in[2], (const u16*)d_in[3], (const u16*)d_in[4],
                                           (const u16*)d_in[5], (const u16*)d_in[6], (const u16*)d_in[7], (const u16*)d_in[8], (const u16*)d_in[9], (char*)d_ws, (u16*)d_out);
}
```

```cpp
#define STAMPS
#include <hip/hip_runtime.h>
#include <stdint.h>

typedef short    bf16x8 __attribute__((ext_vector_type(8)));
typedef short    s16x4  __attribute__((ext_vector_type(4)));
typedef float    f32x4  __attribute__((ext_vector_type(4)));
typedef float    f32x2  __attribute__((ext_vector_type(2)));
typedef __bf16   bf16x2 __attribute__((ext_vector_type(2)));
typedef unsigned u32x4  __attribute__((ext_vector_type(4)));
typedef unsigned u32x2  __attribute__((ext_vector_type(2)));
typedef unsigned short u16;
#define WARMPOL ""
#define NOKROT
#define OST_POL "sc1"
#define WNT false
#define XPOL ""
#define KVSTART 14
#define KVNT_ true
#define TROT(t) (t)
#define RPM_OLD 1
#define RPM_YOUNG 12
#define XAUX 16
#define TPI 0
#define TOFF 0
#define TPOL ""
#define PBF16
#define KS0 8
#define KS1 11
#define KS2 14
#define XWARM 10
#define WQW 256
#define DI __device__ __forceinline__
#define LAS __attribute__((address_space(3)))
#define MFMA16(a, b, c) __builtin_amdgcn_mfma_f32_16x16x32_bf16(a, b, c, 0, 0, 0)
#define SBAR() __builtin_amdgcn_sched_barrier(0)
#define PIN(x) asm volatile("" : "+v"(x))
#define MFMA16_V(acc, a, b) asm volatile("v_mfma_f32_16x16x32_bf16 %0, %1, %2, %0" : "+v"(acc) : "v"(a), "v"(b))
#define MFMA16_Z(d, a, b) asm volatile("v_mfma_f32_16x16x32_bf16 %0, %1, %2, 0" : "=&v"(d) : "v"(a), "v"(b))
#define MFMA16_C(d, a, b, cc) asm volatile("v_mfma_f32_16x16x32_bf16 %0, %1, %2, %3" : "=&v"(d) : "v"(a), "v"(b), "v"(cc))

constexpr int NB = 16, SQ = 144, DM = 576, NH = 32, SK = 512, DH = 128, HD = NH * DH;
constexpr int ITEM_B = 9 * 4 * 1024;
constexpr size_t WS_O = 0;
constexpr size_t WS_X = (size_t)1024 * ITEM_B, WS_F = WS_X + (size_t)256 * 4 * 46080;
constexpr float QSCALE = 0.08838834764831845f * 1.4426950408889634f;

DI unsigned rfl(unsigned x) { return __builtin_amdgcn_readfirstlane(x); }
DI unsigned lane_id() { unsigned l; asm volatile("v_mbcnt_lo_u32_b32 %0, -1, 0\n\tv_mbcnt_hi_u32_b32 %0, -1, %0" : "=v"(l)); return l; }
DI unsigned cvtpk(float lo, float hi) { f32x2 v = {lo, hi}; bf16x2 b = __builtin_convertvector(v, bf16x2); return __builtin_bit_cast(unsigned, b); }
DI bf16x8 lds128(unsigned a) { return *(const LAS bf16x8*)a; }
DI s16x4 ldstr(unsigned a) { return __builtin_bit_cast(s16x4, __builtin_amdgcn_ds_read_tr16_b64_v4i16((LAS s16x4*)a)); }
DI bf16x8 cat8(s16x4 lo, s16x4 hi) { return __builtin_shufflevector(lo, hi, 0, 1, 2, 3, 4, 5, 6, 7); }
DI void dma4(unsigned m0v, unsigned voff, __amdgpu_buffer_rsrc_t srd) {
  asm volatile("s_mov_b32 m0, %0\n\ts_nop 3\n\tbuffer_load_dword %1, %2, 0 offen " WARMPOL " lds" :: "s"(m0v), "v"(voff), "s"(srd) : "memory");
}
#define TILE8_BODY(POL) \
  "s_nop 3\n\ts_mov_b32 m0, %1\n\ts_mov_b32 %0, %2\n\t" \
  "buffer_load_dwordx4 %3, %5, %0 offen " POL " lds\n\ts_add_u32 m0, m0, 896\n\ts_nop 0\n\tbuffer_load_dwordx4 %3, %5, %0 offen offset:128 " POL " lds\n\ts_add_u32 m0, m0, 1152\n\ts_add_u32 %0, %0, %6\n\t" \
  "buffer_load_dwordx4 %4, %5, %0 offen " POL " lds\n\ts_add_u32 m0, m0, 896\n\ts_nop 0\n\tbuffer_load_dwordx4 %4, %5, %0 offen offset:128 " POL " lds\n\ts_add_u32 m0, m0, 1152\n\ts_add_u32 %0, %0, %6\n\t" \
  "buffer_load_dwordx4 %3, %5, %0 offen " POL " lds\n\ts_add_u32 m0, m0, 896\n\ts_nop 0\n\tbuffer_load_dwordx4 %3, %5, %0 offen offset:128 " POL " lds\n\ts_add_u32 m0, m0, 1152\n\ts_add_u32 %0, %0, %6\n\t" \
  "buffer_load_dwordx4 %4, %5, %0 offen " POL " lds\n\ts_add_u32 m0, m0, 896\n\ts_nop 0\n\tbuffer_load_dwordx4 %4, %5, %0 offen offset:128 " POL " lds"
template <bool NT, int D2> DI void dma_tile8(unsigned lo, unsigned so, unsigned va, unsigned vb, __amdgpu_buffer_rsrc_t srd) {
  unsigned t;
  if constexpr (NT) asm volatile(TILE8_BODY("nt") : "=&s"(t) : "s"(lo), "s"(so), "v"(va), "v"(vb), "s"(srd), "i"(D2) : "memory", "scc");
  else              asm volatile(TILE8_BODY("")   : "=&s"(t) : "s"(lo), "s"(so), "v"(va), "v"(vb), "s"(srd), "i"(D2) : "memory", "scc");
}
template <int D2> DI void dma_tile9(unsigned lo, unsigned so, unsigned va, __amdgpu_buffer_rsrc_t srd) {
  unsigned t;
  asm volatile("s_nop 3\n\ts_mov_b32 m0, %1\n\ts_mov_b32 %0, %2\n\t.rept 9\n\t"
               "buffer_load_dwordx4 %3, %4, %0 offen " XPOL " lds\n\ts_add_u32 m0, m0, 2048\n\ts_add_u32 %0, %0, %5\n\t.endr"
               : "=&s"(t) : "s"(lo), "s"(so), "v"(va), "s"(srd), "i"(D2) : "memory", "scc");
}
#define WAITV(n) asm volatile("s_waitcnt vmcnt(" #n ")" ::: "memory")
#define BAR() do { asm volatile("" ::: "memory"); __builtin_amdgcn_s_waitcnt(0xC07F); asm volatile("s_barrier" ::: "memory"); } while (0)
#define RBAR() asm volatile("s_barrier" ::: "memory")
constexpr unsigned GRP = 65536, WRING = 3 * 8192, XSLOT = 18432, XB = 126976, XA = XB + XSLOT, LDS_BYTES = 163840;
constexpr unsigned kslot(int i) { return 8192 * ((i & 3) == 0 ? 0 : (i & 3) == 1 ? 1 : (i & 3) == 2 ? 5 : 3); }
constexpr unsigned vslot(int i) { return 8192 * ((i & 3) == 0 ? 2 : (i & 3) == 1 ? 6 : (i & 3) == 2 ? 4 : 7); }
static_assert(XA + XSLOT == LDS_BYTES, "LDS map");

#define STAMP(k) do { if (NQ == 1 && lane_id() == 0) { const unsigned sb_ = blockIdx.x + (lb ? 256 : 0); unsigned long long t_ = __builtin_amdgcn_s_memrealtime(); g_stamps[sb_ * 32 + (k)] = t_; g_cyc[sb_ * 32 + (k)] = __builtin_amdgcn_s_memtime(); } } while (0)
__device__ unsigned long long g_stamps[512 * 32];
__device__ unsigned long long g_cyc[512 * 32];
#define STAMPW(k) do { unsigned long long t_ = __builtin_amdgcn_s_memrealtime(), c_ = __builtin_amdgcn_s_memtime(); stw[(k) - 26] = t_; stc[(k) - 26] = c_; if ((k) == 28 && lane == 0) for (int q_ = 0; q_ < 3; ++q_) { g_stamps[blockIdx.x * 32 + 26 + q_] = stw[q_]; g_cyc[blockIdx.x * 32 + 26 + q_] = stc[q_]; } } while (0)
#define STAMP2(k) do { if (nb == 1 && NQT == 4 && NNT == 4 && lane_id() == 0) { unsigned long long t_ = __builtin_amdgcn_s_memrealtime(); g_stamps[blockIdx.x * 32 + (k)] = t_; g_cyc[blockIdx.x * 32 + (k)] = __builtin_amdgcn_s_memtime(); } } while (0)

DI float red_g_max(float m) {
  auto r = __builtin_amdgcn_permlane32_swap(__float_as_uint(m), __float_as_uint(m), false, false);
  m = fmaxf(__uint_as_float(r[0]), __uint_as_float(r[1]));
  auto s = __builtin_amdgcn_permlane16_swap(__float_as_uint(m), __float_as_uint(m), false, false);
  return fmaxf(__uint_as_float(s[0]), __uint_as_float(s[1]));
}
DI float red_g_sum(float m) {
  auto r = __builtin_amdgcn_permlane32_swap(__float_as_uint(m), __float_as_uint(m), false, false);
  m = __uint_as_float(r[0]) + __uint_as_float(r[1]);
  auto s = __builtin_amdgcn_permlane16_swap(__float_as_uint(m), __float_as_uint(m), false, false);
  return __uint_as_float(s[0]) + __uint_as_float(s[1]);
}

template <int NQ> struct AttC {
  f32x4 O[NQ][8]; bf16x8 Q[NQ][4]; f32x4 S[2][NQ][2]; f32x4 negm[NQ]; float l[NQ];
  unsigned kb, vb0, vb1;
};
template <int NQ> struct Tab;
template <> struct Tab<2> {
  static constexpr int NPRE = 6;
  static constexpr int pre[6] = {100, 101, 102, 103, 104, 105};
  static constexpr int gx[16][2] = {{106, 0}, {107, 0}, {200, 201}, {110, 0}, {202, 203}, {111, 0}, {112, 0}, {113, 0}, {114, 0}, {210, 211}, {115, 0}, {116, 0}, {117, 0}, {300, 310}, {212, 213}, {301, 311}};
  static constexpr int gy[16] = {302, 312, 303, 313, 304, 314, 305, 315, 306, 316, 307, 317, 0, 0, 0, 0};
  static constexpr int all[24] = {100, 101, 102, 103, 104, 105, 106, 107, 110, 200, 201, 202, 203, 111, 112, 113, 114, 115, 116, 117, 210, 211, 212, 213};
  static constexpr int ady[16] = {300, 310, 301, 311, 302, 312, 303, 313, 304, 314, 305, 315, 306, 316, 307, 317};
};
template <> struct Tab<1> {
  static constexpr int NPRE = 4;
  static constexpr int pre[4] = {100, 101, 102, 103};
  static constexpr int gx[8][2] = {{104, 0}, {105, 0}, {200, 201}, {106, 0}, {107, 0}, {300, 301}, {202, 203}, {302, 303}};
  static constexpr int gy[8] = {304, 305, 306, 307, 0, 0, 0, 0};
  static constexpr int all[12] = {100, 101, 102, 103, 104, 105, 106, 107, 200, 201, 202, 203};
  static constexpr int ady[8] = {300, 301, 302, 303, 304, 305, 306, 307};
};
template <int NQ, int P, bool QK> struct Step {
  static constexpr int X = P & 1;
  static constexpr unsigned ks_ = kslot(P + 1), vs_ = vslot(P);
  bf16x8 fr[4]; float e[NQ][8]; unsigned pw[NQ][4]; u32x4 Pw[NQ];
  template <int OP> DI void op(AttC<NQ>& c) {
    constexpr int ty = OP / 100, qt = (OP / 10) % 10, j = OP % 10;
    if constexpr (ty == 1) { e[qt][j] = c.S[X][qt][j >> 2][j & 3]; asm volatile("v_exp_f32 %0, %0" : "+v"(e[qt][j])); }
    if constexpr (ty == 2) asm volatile("v_cvt_pk_bf16_f32 %0, %1, %2" : "=v"(pw[qt][j]) : "v"(e[qt][2 * j]), "v"(e[qt][2 * j + 1]));
    if constexpr (ty == 3) asm volatile("v_add_f32 %0, %0, %1" : "+v"(c.l[qt]) : "v"(e[qt][j]));
  }
  template <int R> DI void rd(AttC<NQ>& c) {
    if constexpr (R < 8) fr[R & 3] = lds128(c.kb + ks_ + 4096 * (R & 1) + 512 * (R >> 1));
    else if constexpr (R < 16) {
      constexpr int dt = R - 8;
      const unsigned vb = ((dt & 1) ? c.vb1 : c.vb0) + vs_ + 512 * (dt >> 1);
      fr[R & 3] = cat8(ldstr(vb), ldstr(vb + 4096));
    }
    SBAR();
  }
  template <int I> DI void gx(AttC<NQ>& c) {
    constexpr int K = I / NQ, qt = I % NQ, s = K >> 1, kt = K & 1;
    if constexpr (s == 0) MFMA16_C(c.S[X ^ 1][qt][kt], fr[K & 3], c.Q[qt][s], c.negm[qt]); else MFMA16_V(c.S[X ^ 1][qt][kt], fr[K & 3], c.Q[qt][s]);
    if constexpr (qt == NQ - 1) { SBAR(); rd<K + 4>(c); }
    op<Tab<NQ>::gx[I][0]>(c); op<Tab<NQ>::gx[I][1]>(c);
    if constexpr (I + 1 < 8 * NQ) gx<I + 1>(c);
  }
  template <int I> DI void gy(AttC<NQ>& c) {
    constexpr int Dt = I / NQ, qt = I % NQ;
    MFMA16_V(c.O[qt][Dt], fr[Dt & 3], Pw[qt]);
    if constexpr (qt == NQ - 1) { SBAR(); rd<Dt + 12>(c); }
    op<(QK ? Tab<NQ>::gy[I] : Tab<NQ>::ady[I])>(c);
    if constexpr (I + 1 < 8 * NQ) gy<I + 1>(c);
  }
  template <int I> DI void pre(AttC<NQ>& c) {
    op<(QK ? Tab<NQ>::pre[I < Tab<NQ>::NPRE ? I : 0] : Tab<NQ>::all[I])>(c);
    if constexpr (I + 1 < (QK ? Tab<NQ>::NPRE : 12 * NQ)) pre<I + 1>(c);
  }
  DI void run(AttC<NQ>& c) {
    if constexpr (QK) {
      rd<0>(c); rd<1>(c); rd<2>(c); rd<3>(c);
      pre<0>(c);
      gx<0>(c);
    } else {
      rd<8>(c); rd<9>(c); rd<10>(c); rd<11>(c);
      pre<0>(c);
      asm volatile("s_nop 1");
    }
#pragma unroll
    for (int qt = 0; qt < NQ; ++qt) Pw[qt] = u32x4{pw[qt][0], pw[qt][1], pw[qt][2], pw[qt][3]};
    gy<0>(c);
  }
};

template <int NQ, int P, bool QK> DI void att_step(AttC<NQ>& c) { BAR(); SBAR();
  Step<NQ, P, QK> s; s.run(c); SBAR(); }

template <int NQ> DI void att_prologue(AttC<NQ>& c) {
#pragma unroll
  for (int s = 0; s < 4; ++s)
#pragma unroll
    for (int kt = 0; kt < 2; ++kt) {
      const bf16x8 kf = lds128(c.kb + 4096 * kt + 512 * s);
#pragma unroll
      for (int qt = 0; qt < NQ; ++qt) c.S[0][qt][kt] = MFMA16(kf, c.Q[qt][s], s == 0 ? (f32x4{0, 0, 0, 0}) : c.S[0][qt][kt]);
    }
#pragma unroll
  for (int qt = 0; qt < NQ; ++qt) {
    float mx = c.S[0][qt][0][0];
#pragma unroll
    for (int j = 1; j < 8; ++j) mx = fmaxf(mx, c.S[0][qt][j >> 2][j & 3]);
    mx = red_g_max(mx);
    c.negm[qt] = f32x4{-mx, -mx, -mx, -mx};
    c.S[0][qt][0] += c.negm[qt]; c.S[0][qt][1] += c.negm[qt];
    c.l[qt] = 0.f;
#pragma unroll
    for (int dt = 0; dt < 8; ++dt) c.O[qt][dt] = f32x4{0, 0, 0, 0};
  }
}
template <int NQ> DI void att_redo_true_max(AttC<NQ>& c, const u16* __restrict__ kh, const u16* __restrict__ vh) {
  const unsigned lane = lane_id(), li = lane & 15, g = lane >> 4;
  const u16* kp = kh + li * DH + g * 8;
  float mx[NQ];
#pragma unroll
  for (int qt = 0; qt < NQ; ++qt) mx[qt] = -__builtin_inff();
#pragma unroll 1
  for (int kt = 0; kt < SK / 16; ++kt) {
    f32x4 s4[NQ];
#pragma unroll
    for (int qt = 0; qt < NQ; ++qt) s4[qt] = f32x4{0, 0, 0, 0};
#pragma unroll
    for (int s = 0; s < 4; ++s) {
      const bf16x8 kf = *(const bf16x8*)(kp + kt * 16 * DH + s * 32);
#pragma unroll
      for (int qt = 0; qt < NQ; ++qt) s4[qt] = MFMA16(kf, c.Q[qt][s], s4[qt]);
    }
#pragma unroll
    for (int qt = 0; qt < NQ; ++qt) mx[qt] = fmaxf(fmaxf(fmaxf(mx[qt], s4[qt][0]), fmaxf(s4[qt][1], s4[qt][2])), s4[qt][3]);
  }
#pragma unroll
  for (int qt = 0; qt < NQ; ++qt) {
    mx[qt] = red_g_max(mx[qt]); c.l[qt] = 0.f;
#pragma unroll
    for (int dt = 0; dt < 8; ++dt) c.O[qt][dt] = f32x4{0, 0, 0, 0};
  }
#pragma unroll 1
  for (int st = 0; st < SK / 32; ++st) {
    u32x4 pw[NQ];
#pragma unroll
    for (int kt = 0; kt < 2; ++kt) {
      f32x4 s4[NQ];
#pragma unroll
      for (int qt = 0; qt < NQ; ++qt) s4[qt] = f32x4{-mx[qt], -mx[qt], -mx[qt], -mx[qt]};
#pragma unroll
      for (int s = 0; s < 4; ++s) {
        const bf16x8 kf = *(const bf16x8*)(kp + (2 * st + kt) * 16 * DH + s * 32);
#pragma unroll
        for (int qt = 0; qt < NQ; ++qt) s4[qt] = MFMA16(kf, c.Q[qt][s], s4[qt]);
      }
#pragma unroll
      for (int qt = 0; qt < NQ; ++qt) {
        float e[4];
#pragma unroll
        for (int j = 0; j < 4; ++j) { e[j] = __builtin_amdgcn_exp2f(s4[qt][j]); c.l[qt] += e[j]; }
        pw[qt][2 * kt] = cvtpk(e[0], e[1]); pw[qt][2 * kt + 1] = cvtpk(e[2], e[3]);
      }
    }
    const u16* vp = vh + (32 * st + 4 * g) * DH + li;
#pragma unroll
    for (int dt = 0; dt < 8; ++dt) {
      bf16x8 vf;
#pragma unroll
      for (int j = 0; j < 8; ++j) vf[j] = (short)vp[(16 * (j >> 2) + (j & 3)) * DH + 16 * dt];
#pragma unroll
      for (int qt = 0; qt < NQ; ++qt) c.O[qt][dt] = MFMA16(vf, __builtin_bit_cast(bf16x8, pw[qt]), c.O[qt][dt]);
      SBAR();
    }
  }
#pragma unroll
  for (int qt = 0; qt < NQ; ++qt) c.l[qt] = red_g_sum(c.l[qt]);
}
template <int NQ> DI void att_epilogue(AttC<NQ>& c, char* ob, const u16* kh, const u16* vh) {
  bool bad = false;
#pragma unroll
  for (int qt = 0; qt < NQ; ++qt) { c.l[qt] = red_g_sum(c.l[qt]); bad |= !(c.l[qt] < 16777216.0f); }
  if (__builtin_expect(__any(bad), 0)) att_redo_true_max<NQ>(c, kh, vh);
#pragma unroll
  for (int qt = 0; qt < NQ; ++qt) {
    const float inv = __builtin_amdgcn_rcpf(c.l[qt]);
#pragma unroll
    for (int s = 0; s < 4; ++s) {
      const f32x4 a = c.O[qt][2 * s] * inv, bb = c.O[qt][2 * s + 1] * inv;
      u32x4 w = {cvtpk(a[0], a[1]), cvtpk(a[2], a[3]), cvtpk(bb[0], bb[1]), cvtpk(bb[2], bb[3])};
      asm volatile("global_store_dwordx4 %0, %1, off " OST_POL "\n\ts_nop 1" :: "v"(ob + (qt * 4 + s) * 1024), "v"(w) : "memory");
    }
  }
}

DI bf16x8 q_frag(const f32x4& t0, const f32x4& t1) {
  const f32x4 u0 = t0 * QSCALE, u1 = t1 * QSCALE;
  unsigned a0 = cvtpk(u0[0], u0[1]), a1 = cvtpk(u0[2], u0[3]), b0 = cvtpk(u1[0], u1[1]), b1 = cvtpk(u1[2], u1[3]);
  auto r0 = __builtin_amdgcn_permlane32_swap(a0, b0, false, false);
  auto r1 = __builtin_amdgcn_permlane32_swap(a1, b1, false, false);
  auto s0 = __builtin_amdgcn_permlane16_swap(r0[0], r0[1], false, false);
  auto s1 = __builtin_amdgcn_permlane16_swap(r1[0], r1[1], false, false);
  u32x4 w = {s0[0], s1[0], s0[1], s1[1]};
  return __builtin_bit_cast(bf16x8, w);
}
template <int NR, int WS, int XO, bool Z = false>
DI void q_unit(f32x4 (&qa)[NR][4], const bf16x8 (&wc)[4], bf16x8 (&wn)[4], bf16x8 (&xc)[NR], const bf16x8& lc, bf16x8 (&xn)[NR], bf16x8& ln, unsigned w0, unsigned w1, const unsigned (&xa)[3]) {
#define XADDR(row) (xa[(row) >> 1] + ((row) & 1) * 2048 + XO)
#pragma unroll
  for (int q = 0; q < NR; ++q) {
#pragma unroll
    for (int d = 0; d < 4; ++d) {
      if constexpr (Z) MFMA16_Z(qa[q][d], wc[d], q < NR - 1 ? xc[q] : lc); else MFMA16_V(qa[q][d], wc[d], q < NR - 1 ? xc[q] : lc);
      SBAR();
      const int i = 4 * q + d;
      if (WS >= 0 && i < 8 && !(i & 1)) { const int u = i >> 1; const unsigned a = ((u & 1) ? w1 : w0) + 8192 * WS + 512 * (u >> 1); wn[u] = cat8(ldstr(a), ldstr(a + 256)); }
      if (XO >= 0 && (i & 1) && (i >> 1) < NR) xn[i >> 1] = lds128(XADDR(i >> 1));
      if (XO >= 0 && i == 10) ln = lds128(XADDR(NR - 1) + 512);
      SBAR();
    }
    if (XO >= 0 && q < NR - 1) { xc[q] = lds128(XADDR(q) + 512); SBAR(); }
  }
#undef XADDR
}

#define LDA(p) __hip_atomic_load((p), __ATOMIC_RELAXED, __HIP_MEMORY_SCOPE_AGENT)
#define STA(p, v) __hip_atomic_store((p), (v), __ATOMIC_RELAXED, __HIP_MEMORY_SCOPE_AGENT)
constexpr int SPIN_CAP = 1 << 21;
struct Hs { unsigned* mine; unsigned* peer; unsigned me, x, e; bool ok; };
DI void hs_post(const unsigned* p, unsigned box) { asm volatile("s_mov_b32 m0, %0\n\ts_nop 3\n\tglobal_load_lds_dword %1, off sc1" :: "s"(box), "v"(p) : "memory"); }
DI unsigned hs_take(unsigned box) { WAITV(0); return *(volatile LAS unsigned*)(box + 4 * lane_id()); }
DI void hs_entry(Hs& h, char* wsb, unsigned blk) {
  const unsigned lane = lane_id();
  h.me = (blk >> 3) & 7;
  h.mine = (unsigned*)(wsb + WS_F) + blk * 64;
  h.peer = (unsigned*)(wsb + WS_F) + ((blk & ~56u) + 8 * (lane & 7)) * 64;
  if (lane >= 1 && lane <= 3) STA(h.mine + lane, 0u);
  const unsigned y0 = LDA(h.peer + 32 + h.me);
  unsigned x = rfl(y0) + 1;
  while (__any(y0 == x)) ++x;
  h.x = x; h.e = 0; h.ok = false;
  WAITV(0);
  if (lane == 0) STA(h.mine, x);
}
DI void hs_wait_flag(const Hs& h, unsigned v, int word) {
  for (int spin = 0; !__all(v == 1u) && spin < SPIN_CAP; ++spin) { __builtin_amdgcn_s_sleep(2); v = LDA(h.peer + word); }
  __builtin_amdgcn_fence(__ATOMIC_ACQUIRE, "agent");
}
constexpr unsigned HBOX = 161792;
DI void hs_hook(Hs& h, int k) {
  const unsigned lane = lane_id();
  if (k == 0) hs_post(h.peer, HBOX);
  else if (k == 1) { h.e = hs_take(HBOX); if (lane < 8) STA(h.mine + 32 + lane, h.e); }
  else if (k == 4) hs_post(h.peer + 32 + h.me, HBOX);
  else if (k == 5) { h.ok = __all(hs_take(HBOX) == h.x); hs_post(h.peer, HBOX); }
  else if (k == 6) { const unsigned e = hs_take(HBOX); if (lane < 8 && e != h.e) STA(h.mine + 32 + lane, e); hs_post(h.peer + 1, HBOX); }
  else if (k == 7) {
    unsigned f = hs_take(HBOX);
    if (!h.ok) {
      for (int spin = 0; spin < SPIN_CAP; ++spin) {
        const unsigned e = LDA(h.peer);
        if (lane < 8) STA(h.mine + 32 + lane, e);
        if (__all(LDA(h.peer + 32 + h.me) == h.x)) break;
        __builtin_amdgcn_s_sleep(2);
      }
      WAITV(0);
      const unsigned e = LDA(h.peer);
      if (lane < 8) STA(h.mine + 32 + lane, e);
      f = 0;
    }
    hs_wait_flag(h, f, 1);
  }
}

constexpr unsigned EX = 122880;
template <int NR> DI void q_phase(bf16x8 (&own)[2][2], unsigned wid, unsigned lb) {
  asm volatile("; q phase %0" :: "i"(NR) : "memory");
  const unsigned lane = lane_id(), li = lane & 15, g = lane >> 4, pos = lane & 3, tq = li >> 2;
  unsigned w0 = lb + WRING + 1024 * (wid & 1) + 2048 * g + 64 * tq + 32 * (g & 1) + 8 * pos, w1 = w0 ^ 32u;
  const unsigned xl = XB + 1024 * (li >> 3) + 64 * (li & 7) + 16 * (g ^ (2 * (li >> 3)));
  unsigned xa[3] = {xl + wid * 4096, xl + (wid ^ 1) * 4096, xl + 16384};
  bf16x8 w[4], v[4], xA[NR], xB[NR], l0, l1;
  f32x4 qa[NR][4];
  PIN(w0); PIN(w1); PIN(xa[0]); PIN(xa[1]); if (NR == 5) PIN(xa[2]);
  BAR();
#pragma unroll
  for (int u = 0; u < 4; ++u) { const unsigned a = ((u & 1) ? w1 : w0) + 512 * (u >> 1); w[u] = cat8(ldstr(a), ldstr(a + 256)); }
#pragma unroll
  for (int q = 0; q < NR; ++q) { const unsigned a = xa[q >> 1] + (q & 1) * 2048 + XSLOT; xB[q] = lds128(a); if (q < NR - 1) xA[q] = lds128(a + 512); else l0 = lds128(a + 512); }
  BAR(); q_unit<NR, 1, -1, true>(qa, w, v, xB, xB[NR - 1], xA, l1, w0, w1, xa);
#pragma unroll 1
  for (int n = 0; n < 4; ++n) {
    WAITV(0);
    BAR(); q_unit<NR, 2, 0>(qa, v, w, xA, l0, xB, l1, w0, w1, xa);
    BAR(); q_unit<NR, 3, -1>(qa, w, v, xB, xB[NR - 1], xA, l0, w0, w1, xa);
    BAR(); q_unit<NR, 0, XSLOT>(qa, v, w, xA, l1, xB, l0, w0, w1, xa);
    BAR(); q_unit<NR, 1, -1>(qa, w, v, xB, xB[NR - 1], xA, l1, w0, w1, xa);
  }
  BAR(); q_unit<NR, -1, -1>(qa, v, w, xA, l0, xB, l1, w0, w1, xa);
  asm volatile("s_nop 7" ::: "memory"); SBAR();
  unsigned ea = EX + (lb ? 0 : 20480) + lane * 16; PIN(ea);
#pragma unroll
  for (int s = 0; s < 2; ++s) {
    *(LAS bf16x8*)(ea + wid * 4096 + s * 1024) = q_frag(qa[2][2 * s], qa[2][2 * s + 1]);
    *(LAS bf16x8*)(ea + wid * 4096 + 2048 + s * 1024) = q_frag(qa[3][2 * s], qa[3][2 * s + 1]);
    if constexpr (NR == 5) *(LAS bf16x8*)(ea + 16384 + wid * 2048 + s * 1024) = q_frag(qa[4][2 * s], qa[4][2 * s + 1]);
  }
#pragma unroll
  for (int q = 0; q < 2; ++q)
#pragma unroll
    for (int s = 0; s < 2; ++s) own[q][s] = q_frag(qa[q][2 * s], qa[q][2 * s + 1]);
}

template <int NQ> DI void att_wave(char* ows_pair, const u16* kbase, const u16* vbase, unsigned qt0, unsigned m, unsigned hg0, unsigned lb, Hs& hs, const u16* x) {
  asm volatile("; attention wave NQ=%0" :: "i"(NQ) : "memory");
  STAMP(20);
#pragma unroll 1
  for (int r = 0; r < 2; ++r) {
    AttC<NQ> c;
    STAMP(4 * r);
    if constexpr (NQ == 2) {
      bf16x8 own[2][2];
      if (qt0 < 4) q_phase<5>(own, qt0 >> 1, lb); else q_phase<4>(own, qt0 >> 1, lb);
      unsigned ea = EX + (lb ? 0 : 20480) + (qt0 ^ 2) * 2048 + lane_id() * 16; PIN(ea);
      BAR();
      const bool hi = qt0 & 2;
#pragma unroll
      for (int q = 0; q < 2; ++q)
#pragma unroll
        for (int s = 0; s < 2; ++s) { const bf16x8 rc = lds128(ea + q * 2048 + s * 1024); c.Q[q][s] = hi ? rc : own[q][s]; c.Q[q][2 + s] = hi ? own[q][s] : rc; }
    } else {
      const __amdgpu_buffer_rsrc_t srdX = __builtin_amdgcn_make_buffer_rsrc((void*)x, (short)0, SQ * DM * 2, 0x00020000);
      const unsigned lane = lane_id(), half = lb != 0, vx = ((((lane >> 2) & 7) + 8 * half) * (DM * 2) + (lane >> 5) * 64 + (lane & 3) * 16) ^ (32u * half);
      BAR(); if (r == 0) STAMP(21);
#pragma unroll 1
      for (int n = 1; n < 19; n += 2) {
        BAR();
        if (n < 14) { dma_tile9<16 * DM * 2>(((n & 2) ? XB : XA) + half * 1024, ((n + 3) >> 1) * 128, vx, srdX); WAITV(9); } else WAITV(0);
        BAR(); if (r == 0 && n == 5) STAMP(22); if (r == 0 && n == 13) STAMP(23);
        if (n == 1 && lb == 0 && r == 1 && lane == 0) STA(hs.mine + 1, 1u);
      }
      BAR();
      unsigned ea = EX + (lb ? 0 : 20480) + 16384 + lane_id() * 16; PIN(ea);
#pragma unroll
      for (int s = 0; s < 4; ++s) c.Q[0][s] = lds128(ea + s * 1024);
      __builtin_amdgcn_s_waitcnt(0xC07F);
    }
    { const unsigned lane = lane_id(), li = lane & 15, g = lane >> 4;
      c.kb = lb + 2048 * (li >> 3) + 64 * (li & 7) + 16 * (g ^ (2 * (li >> 3)));
      c.vb0 = lb + 2048 * (g >> 1) + 64 * (4 * (g & 1) + (li >> 2)) + 32 * (g & 1) + 8 * (lane & 3); c.vb1 = c.vb0 ^ 32u;
      PIN(c.kb); PIN(c.vb0); PIN(c.vb1); }
    STAMP(4 * r + 1);
    if (NQ == 1 && lb == 0) hs_hook(hs, 4 * r);
    att_prologue<NQ>(c);
    SBAR();
#pragma unroll 1
    for (int ii = 0; ii < 3; ++ii) { att_step<NQ, 0, true>(c); att_step<NQ, 1, true>(c); att_step<NQ, 2, true>(c); att_step<NQ, 3, true>(c);
      if (NQ == 1 && lb == 0) hs_hook(hs, 4 * r + 1 + ii); }
    att_step<NQ, 0, true>(c); att_step<NQ, 1, true>(c);
    if (NQ == 1) WAITV(0);
    att_step<NQ, 2, true>(c); att_step<NQ, 3, false>(c);
    STAMP(4 * r + 2);
    asm volatile("s_nop 7\n\ts_nop 1" ::: "memory"); SBAR();
    const unsigned h = 8 * ((hg0 + 2 * r) & 3) + m;
    att_epilogue<NQ>(c, ows_pair + (size_t)h * ITEM_B + qt0 * 4096 + lane_id() * 16, kbase + (size_t)h * SK * DH, vbase + (size_t)h * SK * DH);
    STAMP(4 * r + 3);
  }
}

DI void wait_newer(int n) {
  if (n >= 32) { if (n >= 40) WAITV(40); else WAITV(32); }
  else if (n >= 16) { if (n >= 24) WAITV(24); else WAITV(16); }
  else if (n >= 8) WAITV(8); else WAITV(0);
}
DI unsigned oslot_o(unsigned s) { return s == 0 ? 131072 : GRP - 9216 + 9216 * s; }
DI unsigned oslot_w(unsigned s) { return s == 0 ? 140288 : 18432 * (s - 1); }
constexpr unsigned OPART = 93184;
constexpr unsigned OBOX = 55296;
struct OLd {
  __amdgpu_buffer_rsrc_t srd; unsigned vo[9], h0, isw, rot;
  DI void init(const char* op, const u16* wo, unsigned m, unsigned g0, unsigned isw_, unsigned rot_) {
    const unsigned lane = lane_id(); h0 = 8 * g0 + (m & 4); isw = isw_; rot = rot_;
    srd = isw ? __builtin_amdgcn_make_buffer_rsrc((void*)wo, (short)0, HD * DM * 2, 0x00020000) : __builtin_amdgcn_make_buffer_rsrc((void*)op, (short)0, NH * ITEM_B, 0x00020000);
#pragma unroll
    for (int p = 0; p < 9; ++p) { const unsigned n = 64 * p + lane, row = n / 36, ch = n - 36 * row;
      vo[p] = isw ? row * (DM * 2) + ((ch ^ ((row >> 1) & 2)) * 16) + (m & 1) * 576 : p * 4096 + lane * 16; }
  }
#define OPIECE(k) "buffer_load_dwordx4 %" #k ", %1, %2 offen lds\n\ts_add_u32 m0, m0, 1024\n\ts_nop 0\n\t"
  DI void issue(int j, unsigned part) const {
    const unsigned h = ((h0 + (j & 16)) & 31) + (((j >> 2) + rot) & 3), s = j & 3;
    const unsigned so = isw ? ((h * 4 + s) * 32 + 16 * part) * (DM * 2) : h * ITEM_B + s * 1024, lo = isw ? oslot_w(j & 3) + part * 9216 : oslot_o(j & 3);
    asm volatile("s_mov_b32 m0, %0\n\ts_nop 3\n\t" OPIECE(3) OPIECE(4) OPIECE(5) OPIECE(6) OPIECE(7) OPIECE(8) OPIECE(9) OPIECE(10) OPIECE(11)
                 :: "s"(lo), "s"(srd), "s"(so), "v"(vo[0]), "v"(vo[1]), "v"(vo[2]), "v"(vo[3]), "v"(vo[4]), "v"(vo[5]), "v"(vo[6]), "v"(vo[7]), "v"(vo[8]) : "memory", "scc");
  }
  DI void run(unsigned part) const {
#pragma unroll 1
    for (int j = 0; j < 32; ++j) {
      if (j == 0 || j == 30) WAITV(9); else if (j < 30) WAITV(18); else WAITV(0);
      RBAR();
      if (j == 0) issue(2, part);
      if (j + 3 < 32) issue(j + 3, part);
    }
  }
};

DI void loader(const u16* wq, const u16* x, const u16* kbase, const u16* vbase, unsigned m, unsigned hg0, unsigned half, OLd& ol, const char* op, const u16* wo, unsigned g0, unsigned rot) {
  const unsigned lane = lane_id(), row7 = (lane >> 2) & 7, sh = lane >> 5, pos = lane & 3, lb = half * GRP;
  const __amdgpu_buffer_rsrc_t srdW = __builtin_amdgcn_make_buffer_rsrc((void*)wq, (short)0, DM * HD * 2, 0x00020000);
  const __amdgpu_buffer_rsrc_t srdX = __builtin_amdgcn_make_buffer_rsrc((void*)x, (short)0, SQ * DM * 2, 0x00020000);
  const __amdgpu_buffer_rsrc_t srdK = __builtin_amdgcn_make_buffer_rsrc((void*)kbase, (short)0, NH * SK * DH * 2, 0x00020000);
  const __amdgpu_buffer_rsrc_t srdV = __builtin_amdgcn_make_buffer_rsrc((void*)vbase, (short)0, NH * SK * DH * 2, 0x00020000);
  const unsigned vw0 = row7 * (HD * 2) + sh * 64 + pos * 16, vw1 = vw0 ^ 32u;
  const unsigned vx = (row7 * (DM * 2) + sh * 64 + pos * 16) ^ (32u * half);
  const unsigned vk0 = row7 * 256 + sh * 64 + pos * 16, vk1 = vk0 ^ 32u, vv = row7 * 256 + sh * 64 + ((pos ^ (2 * (row7 >> 2))) * 16);
  int cnt = 0;
  auto issue_w = [&](unsigned h, int s) {
    dma_tile8<WNT, 8 * HD * 2>(lb + WRING + (s & 3) * 8192, s * 32 * (HD * 2) + h * (DH * 2), vw0, vw1, srdW);
    return cnt += 8;
  };
  auto issue_x = [&](int e) {
    dma_tile9<16 * DM * 2>(((e & 1) ? XB : XA) + half * 1024, e * 128 + half * 8 * (DM * 2), vx, srdX);
    return cnt += 9;
  };
  auto issue_k = [&](unsigned h, int t) {
    const unsigned q = t & 3, lo = q == 0 ? kslot(0) : q == 1 ? kslot(1) : q == 2 ? kslot(2) : kslot(3);
    dma_tile8<KVNT_, 2048>(lb + lo, h * (SK * DH * 2) + TROT(t) * 8192, vk0, vk1, srdK);
    return cnt += 8;
  };
  auto issue_v = [&](unsigned h, int t) {
    const unsigned q = t & 3, lo = q == 0 ? vslot(0) : q == 1 ? vslot(1) : q == 2 ? vslot(2) : vslot(3);
    dma_tile8<KVNT_, 2048>(lb + lo, h * (SK * DH * 2) + TROT(t) * 8192, vv, vv, srdV);
    return cnt += 8;
  };
  unsigned h = 8 * hg0 + m;
  unsigned tnext = 0;
  int kx = 0;
  int n0, n1, n2, n3;
  issue_w(h, 0); n0 = issue_x(0); n1 = issue_w(h, 1); issue_w(h, 2);
  ol.init(op, wo, m, g0, half ^ 1, rot);
#pragma unroll 1
  for (int r = 0; r < 2; ++r) {
    const unsigned hn = 8 * ((hg0 + 2) & 3) + m;
    wait_newer(cnt - n0); RBAR();
    n2 = issue_x(1);
    if (r == 0) issue_w(h, 3);
    n3 = cnt;
#pragma unroll 1
    for (int G = 1; G < 15; ++G) {
      wait_newer(cnt - n1); RBAR();
      n1 = n2; n2 = n3;
      issue_w(h, G + 3);
      n3 = cnt;
      if (r == 0) { if (G == KS0) kx = issue_k(h, 0); if (G == KS1) issue_k(h, 1); if (G == KS2) issue_v(h, 0); }
    }
    int a0 = cnt, a1, a2;
    wait_newer(cnt - n1); RBAR();
    issue_k(h, 2);
    wait_newer(cnt - n2); RBAR();
    a1 = issue_v(h, 1);
    wait_newer(cnt - n3); RBAR();
    issue_k(h, 3);
    wait_newer(cnt - kx); RBAR();
    a2 = issue_v(h, 2);
    RBAR();
#pragma unroll 1
    for (int i = 0; i < 16; ++i) {
      wait_newer(cnt - a0);
      RBAR();
      a0 = a1; a1 = a2;
      if (i < 12) issue_k(h, i + 4);
      if (i < 13) issue_v(h, i + 3);
      a2 = cnt;
      if (r == 0) {
        if (i == 12) issue_k(hn, 0);
        if (i == 13) { issue_k(hn, 1); issue_v(hn, 0); }
        if (i == 14) { issue_x(0); issue_w(hn, 2); issue_w(hn, 3); }
        if (i == 15) { n0 = issue_w(hn, 0); n1 = issue_w(hn, 1); }
      } else {
        if (i == 13 && ol.isw) { ol.issue(0, 0); ol.issue(0, 1); ol.issue(1, 0); ol.issue(1, 1); cnt += 36; }
        if (i == 14 && !ol.isw) { ol.issue(0, 0); ol.issue(1, 0); cnt += 18; }
      }
    }
    h = hn;
  }
}

template <int NQT, int NNT> struct OSet { bf16x8 w[NNT], ol; };
template <int NQT, int NNT, bool RD, bool Z> DI void o_unit(f32x4 (&acc)[NQT][NNT], bf16x8 (&of)[NQT - 1], const OSet<NQT, NNT>& cs, OSet<NQT, NNT>& ns, unsigned oa, unsigned wa0, unsigned wa1) {
#pragma unroll
  for (int q = 0; q < NQT; ++q) {
#pragma unroll
    for (int n = 0; n < NNT; ++n) {
      if constexpr (Z) MFMA16_Z(acc[q][n], cs.w[n], q < NQT - 1 ? of[q < NQT - 1 ? q : 0] : cs.ol); else MFMA16_V(acc[q][n], cs.w[n], q < NQT - 1 ? of[q < NQT - 1 ? q : 0] : cs.ol);
      SBAR();
      if (RD && q == 0) { const unsigned a = ((n & 1) ? wa1 : wa0) + 64 * (n >> 1); ns.w[n] = cat8(ldstr(a), ldstr(a + 9216)); }
      if (RD && q == 1 && n == 1) ns.ol = lds128(oa + (NQT - 1) * 1024);
      SBAR();
    }
    if (RD && q < NQT - 1) { of[q] = lds128(oa + q * 1024); SBAR(); }
  }
}
template <int NQT, int NNT> DI void oproj_consumer(char* __restrict__ wsb, unsigned nb, unsigned kq, unsigned blk) {
  asm volatile("; oproj consumer %0 x %1" :: "i"(NQT), "i"(NNT) : "memory");
  constexpr unsigned qh = NQT == 4, q0 = 5 * qh;
  f32x4 acc[NQT][NNT];
  bf16x8 of[NQT - 1]; OSet<NQT, NNT> sa, sb;
  unsigned oa, wa0, wa1;
  { const unsigned lane = lane_id(), li = lane & 15, g = lane >> 4, n0 = (9 * nb + 1) >> 1;
    const unsigned rb = oslot_w(0) + 576 * (4 * g + (li >> 2)) + 8 * (lane & 3);
    oa = oslot_o(0) + q0 * 1024 + lane * 16;
    wa0 = rb + 64 * (n0 >> 1) + 32 * ((n0 ^ g) & 1); wa1 = rb + 64 * ((n0 + 1) >> 1) + 32 * ((n0 ^ g ^ 1) & 1); }
#define NEXTO(j) do { const unsigned d_ = oslot_w(((j) + 1) & 3) - oslot_w((j) & 3); oa += oslot_o(((j) + 1) & 3) - oslot_o((j) & 3); wa0 += d_; wa1 += d_; PIN(oa); PIN(wa0); PIN(wa1); } while (0)
  PIN(oa); PIN(wa0); PIN(wa1);
  STAMP2(24);
  BAR();
  STAMP2(25);
#pragma unroll
  for (int n = 0; n < NNT; ++n) { const unsigned a = ((n & 1) ? wa1 : wa0) + 64 * (n >> 1); sa.w[n] = cat8(ldstr(a), ldstr(a + 9216)); }
#pragma unroll
  for (int q = 0; q < NQT - 1; ++q) of[q] = lds128(oa + q * 1024);
  sa.ol = lds128(oa + (NQT - 1) * 1024);
  NEXTO(0);
  BAR(); o_unit<NQT, NNT, true, true>(acc, of, sa, sb, oa, wa0, wa1); NEXTO(1);
  WAITV(0);
#pragma unroll 1
  for (int j = 2; j < 32; j += 2) {
    BAR(); o_unit<NQT, NNT, true, false>(acc, of, sb, sa, oa, wa0, wa1); NEXTO(j);
    BAR(); o_unit<NQT, NNT, true, false>(acc, of, sa, sb, oa, wa0, wa1); NEXTO(j + 1);
  }
  o_unit<NQT, NNT, false, false>(acc, of, sb, sa, 0, 0, 0);
  asm volatile("s_nop 7\n\ts_nop 1" ::: "memory"); SBAR();
  STAMP2(26);
  const unsigned lane = lane_id();
  if (nb == kq) {
    unsigned ra = OPART + q0 * 5120 + lane * 16; PIN(ra);
#pragma unroll
    for (int q = 0; q < NQT; ++q)
#pragma unroll
      for (int n = 0; n < NNT; ++n) *(LAS f32x4*)(ra + (q * 5 + n) * 1024) = acc[q][n];
  } else {
    const __amdgpu_buffer_rsrc_t srdS = __builtin_amdgcn_make_buffer_rsrc((void*)(wsb + WS_X + (size_t)(blk * 4 + nb) * 46080), (short)0, 46080, 0x00020000);
#pragma unroll
    for (int q = 0; q < NQT; ++q)
#pragma unroll
      for (int u = 0; u < (NNT + 1) / 2; ++u) {
        const f32x4 a = acc[q][2 * u], c2 = acc[q][2 * u + 1 < NNT ? 2 * u + 1 : 2 * u];
        __builtin_amdgcn_raw_buffer_store_b128(u32x4{cvtpk(a[0], a[1]), cvtpk(a[2], a[3]), cvtpk(c2[0], c2[1]), cvtpk(c2[2], c2[3])}, srdS, lane * 16, ((q0 + q) * 3 + u) * 1024, XAUX);
      }
    WAITV(0);
  }
}
DI void oproj_finish(u16* __restrict__ outp, char* __restrict__ wsb, unsigned q, unsigned kq, unsigned blk) {
  const unsigned lane = lane_id(), li = lane & 15, g = lane >> 4, nnt = 5 - (kq & 1);
  BAR();
  constexpr int NQT = 4, NNT = 4; const unsigned nb = q == 3 ? 1 : 0;
  STAMP2(27);
  unsigned* fl = (unsigned*)(wsb + WS_F);
  if (q == 8 && lane == 0) STA(fl + blk * 64 + 3, 1u);
  { const unsigned* pf = fl + ((blk & ~48u) + 16 * (lane & 3)) * 64 + 3;
    for (int spin = 0; spin < SPIN_CAP && !__all(LDA(pf) == 1u); ++spin) __builtin_amdgcn_s_sleep(1);
    __builtin_amdgcn_fence(__ATOMIC_ACQUIRE, "wavefront"); }
  STAMP2(28);
  f32x4 px[3][5];
  u32x4 pw[3][3];
#pragma unroll
  for (int p = 0; p < 3; ++p) {
    const unsigned pb = (blk & ~48u) + 16 * ((kq + 1 + p) & 3);
    const __amdgpu_buffer_rsrc_t srdP = __builtin_amdgcn_make_buffer_rsrc((void*)(wsb + WS_X + (size_t)(pb * 4 + kq) * 46080), (short)0, 46080, 0x00020000);
#pragma unroll
    for (int u = 0; u < 3; ++u) pw[p][u] = __builtin_amdgcn_raw_buffer_load_b128(srdP, lane * 16, (q * 3 + (u < 2 ? u : nnt - 3)) * 1024, 16);
  }
  SBAR();
#pragma unroll
  for (int p = 0; p < 3; ++p)
#pragma unroll
    for (int n = 0; n < 5; ++n) { const unsigned lo = pw[p][n >> 1][2 * (n & 1)], hi = pw[p][n >> 1][2 * (n & 1) + 1];
      px[p][n] = f32x4{__uint_as_float(lo << 16), __uint_as_float(lo & 0xffff0000u), __uint_as_float(hi << 16), __uint_as_float(hi & 0xffff0000u)}; }
  const unsigned tb = OPART + q * 5120;
  f32x4 own[5];
#pragma unroll
  for (int n = 0; n < 5; ++n) own[n] = *(const LAS f32x4*)(tb + (n < 4 ? n : nnt - 1) * 1024 + lane * 16);
#pragma unroll
  for (int n = 0; n < 5; ++n) {
    const f32x4 a = (own[n] + px[0][n]) + (px[1][n] + px[2][n]);
    *(LAS u32x2*)(tb + li * 176 + g * 8 + n * 32) = u32x2{cvtpk(a[0], a[1]), cvtpk(a[2], a[3])};
  }
#pragma unroll
  for (unsigned i = 0; i < 3; ++i) {
    const unsigned ch = i * 64 + lane, row = ch / 10, cc = ch - row * 10;
    if (row < 16 && cc < 2 * nnt) {
      *(u32x4*)(outp + (size_t)(16 * q + row) * (2 * DM) + cc * 8) = *(const LAS u32x4*)(tb + row * 176 + cc * 16);
    }
  }
}

__global__ __launch_bounds__(768, 3) void k_fused(const u16* __restrict__ xA, const u16* __restrict__ kA, const u16* __restrict__ vA, const u16* __restrict__ wqA, const u16* __restrict__ woA,
                                                  const u16* __restrict__ xB, const u16* __restrict__ kB, const u16* __restrict__ vB, const u16* __restrict__ wqB, const u16* __restrict__ woB,
                                                  char* __restrict__ wsb, u16* __restrict__ out) {
  extern __shared__ __attribute__((aligned(16))) char smem[];
  const unsigned wid12 = rfl(threadIdx.x >> 6), half = wid12 >= 6, wid = wid12 - 6 * half, lb = half * GRP;
  const unsigned blk = blockIdx.x;
  const unsigned idx = blk >> 3, pair = (blk & 7) * 4 + (idx >> 3), m = idx & 7, br = pair >> 4, b = pair & 15;
  const unsigned hg0 = (blk + half) & 3, kq = m >> 1, g0 = ((blk ^ kq) & 1) ? (blk + 1) & 3 : blk & 3;
  char* op = wsb + WS_O + (size_t)pair * NH * ITEM_B;
  const u16* kb = (br ? kB : kA) + (size_t)b * NH * SK * DH; const u16* vb = (br ? vB : vA) + (size_t)b * NH * SK * DH;
  Hs hs;
  if (wid < 4) {
    att_wave<2>(op, kb, vb, 2 * wid, m, hg0, lb, hs, nullptr);
    const unsigned t = half ? 3 - ((wid + 2) & 3) : wid, nb = 2 * half + (t & 1);
    if (t == 0) oproj_consumer<5, 5>(wsb, nb, kq, blk);
    else if (t == 1) oproj_consumer<5, 4>(wsb, nb, kq, blk);
    else if (t == 2) oproj_consumer<4, 5>(wsb, nb, kq, blk);
    else oproj_consumer<4, 4>(wsb, nb, kq, blk);
  } else if (wid == 4) {
    if (!half) hs_entry(hs, wsb, blk);
    else {
      const __amdgpu_buffer_rsrc_t srdT = __builtin_amdgcn_make_buffer_rsrc((char*)((br ? xB : xA) + (size_t)b * SQ * DM) + m * 20736, (short)0, 20736, 0x00020000);
      __builtin_amdgcn_s_sleep(XWARM);
#pragma unroll
      for (int p = 0; p < 3; ++p) { const unsigned n = p * 64 + lane_id(); dma4(vslot(3), (n < 162 ? n : 0) * 128, srdT); }
      {
        constexpr unsigned LN = (576 - WQW) * 64 / 128;
        const __amdgpu_buffer_rsrc_t srdQ = __builtin_amdgcn_make_buffer_rsrc((char*)((blk & 1) ? wqB : wqA) + (size_t)WQW * 8192 + (size_t)(blk >> 1) * LN * 128, (short)0, LN * 128, 0x00020000);
#pragma unroll
        for (unsigned p = 0; p < (LN + 63) / 64; ++p) { const unsigned n = p * 64 + lane_id(); dma4(vslot(3), (n < LN ? n : 0) * 128, srdQ); } }
    }
    att_wave<1>(op, kb, vb, 8, m, hg0, lb, hs, (br ? xB : xA) + (size_t)b * SQ * DM);
    if (half) {
      OLd ol; ol.init(op, br ? woB : woA, m, g0, 1, pair & 3);
      WAITV(0);
      ol.run(1);
    } else {
      WAITV(0);
      RBAR(); RBAR(); RBAR();
      if (lane_id() == 0) STA(hs.mine + 2, 1u);
#pragma unroll 1
      for (int j = 3; j < 8; ++j) RBAR();
      hs_post(hs.peer + 2, OBOX);
      RBAR(); RBAR();
      hs_wait_flag(hs, hs_take(OBOX), 2);
      RBAR(); RBAR(); RBAR();
      WAITV(0);
    }
    return;
  } else {
    OLd ol;
    loader(br ? wqB : wqA, (br ? xB : xA) + (size_t)b * SQ * DM, kb, vb, m, hg0, half, ol, op, br ? woB : woA, g0, pair & 3);
    ol.run(0);
    if (half) return;
  }
  oproj_finish(out + (size_t)b * SQ * (2 * DM) + br * DM + (m & 1) * 288 + 16 * ((9 * kq + 1) >> 1), wsb, wid < 4 ? wid + 4 * half : 8, kq, blk);
  { constexpr int NQT = 4, NNT = 4; const unsigned nb = wid12 == 3 ? 1 : 0; STAMP2(29); }
}

__global__ void k_rep() {
  unsigned long long t0 = ~0ull; for (int i = 0; i < 512 * 32; ++i) if (g_stamps[i] && g_stamps[i] < t0) t0 = g_stamps[i];
  const int ks[10] = {21, 1, 2, 3, 5, 6, 7, 25, 26, 29};
  for (int q = 0; q < 10; ++q) { double s = 0, mx = 0; for (int i = 0; i < 256; ++i) { double d = (g_stamps[i * 32 + ks[q]] - t0) / 100.0; s += d; if (d > mx) mx = d; } printf("s%d avg %.1f max %.1f\n", ks[q], s / 256, mx); }
}
extern "C" void kernel_launch(void* const* d_in, const int* in_sizes, int n_in, void* d_out, int out_size, void* d_ws, size_t ws_size, hipStream_t stream) {
  (void)hipFuncSetAttribute((const void*)k_fused, hipFuncAttributeMaxDynamicSharedMemorySize, LDS_BYTES);
  k_fused<<<256, 768, LDS_BYTES, stream>>>((const u16*)d_in[0], (const u16*)d_in[1], (const u16*)d_in[2], (const u16*)d_in[3], (const u16*)d_in[4],
                                           (const u16*)d_in[5], (const u16*)d_in[6], (const u16*)d_in[7], (const u16*)d_in[8], (const u16*)d_in[9], (char*)d_ws, (u16*)d_out);
  k_rep<<<1, 1, 0, stream>>>();
}
```

```cpp
#include <hip/hip_runtime.h>
#include <stdint.h>

typedef short    bf16x8 __attribute__((ext_vector_type(8)));
typedef short    s16x4  __attribute__((ext_vector_type(4)));
typedef float    f32x4  __attribute__((ext_vector_type(4)));
typedef float    f32x2  __attribute__((ext_vector_type(2)));
typedef __bf16   bf16x2 __attribute__((ext_vector_type(2)));
typedef unsigned u32x4  __attribute__((ext_vector_type(4)));
typedef unsigned u32x2  __attribute__((ext_vector_type(2)));
typedef unsigned short u16;
#define WARMPOL ""
#define NOKROT
#define OST_POL "sc1"
#define WNT false
#define XPOL ""
#define KVSTART 14
#define KVNT_ true
#define TROT(t) (t)
#define RPM_OLD 1
#define RPM_YOUNG 12
#define XAUX 16
#define TPI 0
#define TOFF 0
#define TPOL ""
#define PBF16
#define KS0 8
#define KS1 11
#define KS2 14
#define XWARM 10
#define WQW 256
#define DI __device__ __forceinline__
#define LAS __attribute__((address_space(3)))
#define MFMA16(a, b, c) __builtin_amdgcn_mfma_f32_16x16x32_bf16(a, b, c, 0, 0, 0)
#define SBAR() __builtin_amdgcn_sched_barrier(0)
#define PIN(x) asm volatile("" : "+v"(x))
#define MFMA16_V(acc, a, b) asm volatile("v_mfma_f32_16x16x32_bf16 %0, %1, %2, %0" : "+v"(acc) : "v"(a), "v"(b))
#define MFMA16_Z(d, a, b) asm volatile("v_mfma_f32_16x16x32_bf16 %0, %1, %2, 0" : "=&v"(d) : "v"(a), "v"(b))
#define MFMA16_C(d, a, b, cc) asm volatile("v_mfma_f32_16x16x32_bf16 %0, %1, %2, %3" : "=&v"(d) : "v"(a), "v"(b), "v"(cc))

constexpr int NB = 16, SQ = 144, DM = 576, NH = 32, SK = 512, DH = 128, HD = NH * DH;
constexpr int ITEM_B = 9 * 4 * 1024;
constexpr size_t WS_O = 0;
constexpr size_t WS_X = (size_t)1024 * ITEM_B, WS_F = WS_X + (size_t)256 * 4 * 46080;
constexpr float QSCALE = 0.08838834764831845f * 1.4426950408889634f;

DI unsigned rfl(unsigned x) { return __builtin_amdgcn_readfirstlane(x); }
DI unsigned lane_id() { unsigned l; asm volatile("v_mbcnt_lo_u32_b32 %0, -1, 0\n\tv_mbcnt_hi_u32_b32 %0, -1, %0" : "=v"(l)); return l; }
DI unsigned cvtpk(float lo, float hi) { f32x2 v = {lo, hi}; bf16x2 b = __builtin_convertvector(v, bf16x2); return __builtin_bit_cast(unsigned, b); }
DI bf16x8 lds128(unsigned a) { return *(const LAS bf16x8*)a; }
DI s16x4 ldstr(unsigned a) { return __builtin_bit_cast(s16x4, __builtin_amdgcn_ds_read_tr16_b64_v4i16((LAS s16x4*)a)); }
DI bf16x8 cat8(s16x4 lo, s16x4 hi) { return __builtin_shufflevector(lo, hi, 0, 1, 2, 3, 4, 5, 6, 7); }
DI void dma4(unsigned m0v, unsigned voff, __amdgpu_buffer_rsrc_t srd) {
  asm volatile("s_mov_b32 m0, %0\n\ts_nop 3\n\tbuffer_load_dword %1, %2, 0 offen " WARMPOL " lds" :: "s"(m0v), "v"(voff), "s"(srd) : "memory");
}
#define TILE8_BODY(POL) \
  "s_nop 3\n\ts_mov_b32 m0, %1\n\ts_mov_b32 %0, %2\n\t" \
  "buffer_load_dwordx4 %3, %5, %0 offen " POL " lds\n\ts_add_u32 m0, m0, 896\n\ts_nop 0\n\tbuffer_load_dwordx4 %3, %5, %0 offen offset:128 " POL " lds\n\ts_add_u32 m0, m0, 1152\n\ts_add_u32 %0, %0, %6\n\t" \
  "buffer_load_dwordx4 %4, %5, %0 offen " POL " lds\n\ts_add_u32 m0, m0, 896\n\ts_nop 0\n\tbuffer_load_dwordx4 %4, %5, %0 offen offset:128 " POL " lds\n\ts_add_u32 m0, m0, 1152\n\ts_add_u32 %0, %0, %6\n\t" \
  "buffer_load_dwordx4 %3, %5, %0 offen " POL " lds\n\ts_add_u32 m0, m0, 896\n\ts_nop 0\n\tbuffer_load_dwordx4 %3, %5, %0 offen offset:128 " POL " lds\n\ts_add_u32 m0, m0, 1152\n\ts_add_u32 %0, %0, %6\n\t" \
  "buffer_load_dwordx4 %4, %5, %0 offen " POL " lds\n\ts_add_u32 m0, m0, 896\n\ts_nop 0\n\tbuffer_load_dwordx4 %4, %5, %0 offen offset:128 " POL " lds"
template <bool NT, int D2> DI void dma_tile8(unsigned lo, unsigned so, unsigned va, unsigned vb, __amdgpu_buffer_rsrc_t srd) {
  unsigned t;
  if constexpr (NT) asm volatile(TILE8_BODY("nt") : "=&s"(t) : "s"(lo), "s"(so), "v"(va), "v"(vb), "s"(srd), "i"(D2) : "memory", "scc");
  else              asm volatile(TILE8_BODY("")   : "=&s"(t) : "s"(lo), "s"(so), "v"(va), "v"(vb), "s"(srd), "i"(D2) : "memory", "scc");
}
template <int D2> DI void dma_tile9(unsigned lo, unsigned so, unsigned va, __amdgpu_buffer_rsrc_t srd) {
  unsigned t;
  asm volatile("s_nop 3\n\ts_mov_b32 m0, %1\n\ts_mov_b32 %0, %2\n\t.rept 9\n\t"
               "buffer_load_dwordx4 %3, %4, %0 offen " XPOL " lds\n\ts_add_u32 m0, m0, 2048\n\ts_add_u32 %0, %0, %5\n\t.endr"
               : "=&s"(t) : "s"(lo), "s"(so), "v"(va), "s"(srd), "i"(D2) : "memory", "scc");
}
#define WAITV(n) asm volatile("s_waitcnt vmcnt(" #n ")" ::: "memory")
#define BAR() do { asm volatile("" ::: "memory"); __builtin_amdgcn_s_waitcnt(0xC07F); asm volatile("s_barrier" ::: "memory"); } while (0)
#define RBAR() asm volatile("s_barrier" ::: "memory")
constexpr unsigned GRP = 65536, WRING = 3 * 8192, XSLOT = 18432, XB = 126976, XA = XB + XSLOT, LDS_BYTES = 163840;
constexpr unsigned kslot(int i) { return 8192 * ((i & 3) == 0 ? 0 : (i & 3) == 1 ? 1 : (i & 3) == 2 ? 5 : 3); }
constexpr unsigned vslot(int i) { return 8192 * ((i & 3) == 0 ? 2 : (i & 3) == 1 ? 6 : (i & 3) == 2 ? 4 : 7); }
static_assert(XA + XSLOT == LDS_BYTES, "LDS map");

#define STAMP2(k)
#define STAMP(k)
#define STAMPW(k)

DI float red_g_max(float m) {
  auto r = __builtin_amdgcn_permlane32_swap(__float_as_uint(m), __float_as_uint(m), false, false);
  m = fmaxf(__uint_as_float(r[0]), __uint_as_float(r[1]));
  auto s = __builtin_amdgcn_permlane16_swap(__float_as_uint(m), __float_as_uint(m), false, false);
  return fmaxf(__uint_as_float(s[0]), __uint_as_float(s[1]));
}
DI float red_g_sum(float m) {
  auto r = __builtin_amdgcn_permlane32_swap(__float_as_uint(m), __float_as_uint(m), false, false);
  m = __uint_as_float(r[0]) + __uint_as_float(r[1]);
  auto s = __builtin_amdgcn_permlane16_swap(__float_as_uint(m), __float_as_uint(m), false, false);
  return __uint_as_float(s[0]) + __uint_as_float(s[1]);
}

template <int NQ> struct AttC {
  f32x4 O[NQ][8]; bf16x8 Q[NQ][4]; f32x4 S[2][NQ][2]; f32x4 negm[NQ]; float l[NQ];
  unsigned kb, vb0, vb1;
};
template <int NQ> struct Tab;
template <> struct Tab<2> {
  static constexpr int NPRE = 6;
  static constexpr int pre[6] = {100, 101, 102, 103, 104, 105};
  static constexpr int gx[16][2] = {{106, 0}, {107, 0}, {200, 201}, {110, 0}, {202, 203}, {111, 0}, {112, 0}, {113, 0}, {114, 0}, {210, 211}, {115, 0}, {116, 0}, {117, 0}, {300, 310}, {212, 213}, {301, 311}};
  static constexpr int gy[16] = {302, 312, 303, 313, 304, 314, 305, 315, 306, 316, 307, 317, 0, 0, 0, 0};
  static constexpr int all[24] = {100, 101, 102, 103, 104, 105, 106, 107, 110, 200, 201, 202, 203, 111, 112, 113, 114, 115, 116, 117, 210, 211, 212, 213};
  static constexpr int ady[16] = {300, 310, 301, 311, 302, 312, 303, 313, 304, 314, 305, 315, 306, 316, 307, 317};
};
template <> struct Tab<1> {
  static constexpr int NPRE = 4;
  static constexpr int pre[4] = {100, 101, 102, 103};
  static constexpr int gx[8][2] = {{104, 0}, {105, 0}, {200, 201}, {106, 0}, {107, 0}, {300, 301}, {202, 203}, {302, 303}};
  static constexpr int gy[8] = {304, 305, 306, 307, 0, 0, 0, 0};
  static constexpr int all[12] = {100, 101, 102, 103, 104, 105, 106, 107, 200, 201, 202, 203};
  static constexpr int ady[8] = {300, 301, 302, 303, 304, 305, 306, 307};
};
template <int NQ, int P, bool QK> struct Step {
  static constexpr int X = P & 1;
  static constexpr unsigned ks_ = kslot(P + 1), vs_ = vslot(P);
  bf16x8 fr[4]; float e[NQ][8]; unsigned pw[NQ][4]; u32x4 Pw[NQ];
  template <int OP> DI void op(AttC<NQ>& c) {
    constexpr int ty = OP / 100, qt = (OP / 10) % 10, j = OP % 10;
    if constexpr (ty == 1) { e[qt][j] = c.S[X][qt][j >> 2][j & 3]; asm volatile("v_exp_f32 %0, %0" : "+v"(e[qt][j])); }
    if constexpr (ty == 2) asm volatile("v_cvt_pk_bf16_f32 %0, %1, %2" : "=v"(pw[qt][j]) : "v"(e[qt][2 * j]), "v"(e[qt][2 * j + 1]));
    if constexpr (ty == 3) asm volatile("v_add_f32 %0, %0, %1" : "+v"(c.l[qt]) : "v"(e[qt][j]));
  }
  template <int R> DI void rd(AttC<NQ>& c) {
    if constexpr (R < 8) fr[R & 3] = lds128(c.kb + ks_ + 4096 * (R & 1) + 512 * (R >> 1));
    else if constexpr (R < 16) {
      constexpr int dt = R - 8;
      const unsigned vb = ((dt & 1) ? c.vb1 : c.vb0) + vs_ + 512 * (dt >> 1);
      fr[R & 3] = cat8(ldstr(vb), ldstr(vb + 4096));
    }
    SBAR();
  }
  template <int I> DI void gx(AttC<NQ>& c) {
    constexpr int K = I / NQ, qt = I % NQ, s = K >> 1, kt = K & 1;
    if constexpr (s == 0) MFMA16_C(c.S[X ^ 1][qt][kt], fr[K & 3], c.Q[qt][s], c.negm[qt]); else MFMA16_V(c.S[X ^ 1][qt][kt], fr[K & 3], c.Q[qt][s]);
    if constexpr (qt == NQ - 1) { SBAR(); rd<K + 4>(c); }
    op<Tab<NQ>::gx[I][0]>(c); op<Tab<NQ>::gx[I][1]>(c);
    if constexpr (I + 1 < 8 * NQ) gx<I + 1>(c);
  }
  template <int I> DI void gy(AttC<NQ>& c) {
    constexpr int Dt = I / NQ, qt = I % NQ;
    MFMA16_V(c.O[qt][Dt], fr[Dt & 3], Pw[qt]);
    if constexpr (qt == NQ - 1) { SBAR(); rd<Dt + 12>(c); }
    op<(QK ? Tab<NQ>::gy[I] : Tab<NQ>::ady[I])>(c);
    if constexpr (I + 1 < 8 * NQ) gy<I + 1>(c);
  }
  template <int I> DI void pre(AttC<NQ>& c) {
    op<(QK ? Tab<NQ>::pre[I < Tab<NQ>::NPRE ? I : 0] : Tab<NQ>::all[I])>(c);
    if constexpr (I + 1 < (QK ? Tab<NQ>::NPRE : 12 * NQ)) pre<I + 1>(c);
  }
  DI void run(AttC<NQ>& c) {
    if constexpr (QK) {
      rd<0>(c); rd<1>(c); rd<2>(c); rd<3>(c);
      pre<0>(c);
      gx<0>(c);
    } else {
      rd<8>(c); rd<9>(c); rd<10>(c); rd<11>(c);
      pre<0>(c);
      asm volatile("s_nop 1");
    }
#pragma unroll
    for (int qt = 0; qt < NQ; ++qt) Pw[qt] = u32x4{pw[qt][0], pw[qt][1], pw[qt][2], pw[qt][3]};
    gy<0>(c);
  }
};

template <int NQ, int P, bool QK> DI void att_step(AttC<NQ>& c) { BAR(); SBAR();
  Step<NQ, P, QK> s; s.run(c); SBAR(); }

template <int NQ> DI void att_prologue(AttC<NQ>& c) {
#pragma unroll
  for (int s = 0; s < 4; ++s)
#pragma unroll
    for (int kt = 0; kt < 2; ++kt) {
      const bf16x8 kf = lds128(c.kb + 4096 * kt + 512 * s);
#pragma unroll
      for (int qt = 0; qt < NQ; ++qt) c.S[0][qt][kt] = MFMA16(kf, c.Q[qt][s], s == 0 ? (f32x4{0, 0, 0, 0}) : c.S[0][qt][kt]);
    }
#pragma unroll
  for (int qt = 0; qt < NQ; ++qt) {
    float mx = c.S[0][qt][0][0];
#pragma unroll
    for (int j = 1; j < 8; ++j) mx = fmaxf(mx, c.S[0][qt][j >> 2][j & 3]);
    mx = red_g_max(mx);
    c.negm[qt] = f32x4{-mx, -mx, -mx, -mx};
    c.S[0][qt][0] += c.negm[qt]; c.S[0][qt][1] += c.negm[qt];
    c.l[qt] = 0.f;
#pragma unroll
    for (int dt = 0; dt < 8; ++dt) c.O[qt][dt] = f32x4{0, 0, 0, 0};
  }
}
template <int NQ> DI void att_redo_true_max(AttC<NQ>& c, const u16* __restrict__ kh, const u16* __restrict__ vh) {
  const unsigned lane = lane_id(), li = lane & 15, g = lane >> 4;
  const u16* kp = kh + li * DH + g * 8;
  float mx[NQ];
#pragma unroll
  for (int qt = 0; qt < NQ; ++qt) mx[qt] = -__builtin_inff();
#pragma unroll 1
  for (int kt = 0; kt < SK / 16; ++kt) {
    f32x4 s4[NQ];
#pragma unroll
    for (int qt = 0; qt < NQ; ++qt) s4[qt] = f32x4{0, 0, 0, 0};
#pragma unroll
    for (int s = 0; s < 4; ++s) {
      const bf16x8 kf = *(const bf16x8*)(kp + kt * 16 * DH + s * 32);
#pragma unroll
      for (int qt = 0; qt < NQ; ++qt) s4[qt] = MFMA16(kf, c.Q[qt][s], s4[qt]);
    }
#pragma unroll
    for (int qt = 0; qt < NQ; ++qt) mx[qt] = fmaxf(fmaxf(fmaxf(mx[qt], s4[qt][0]), fmaxf(s4[qt][1], s4[qt][2])), s4[qt][3]);
  }
#pragma unroll
  for (int qt = 0; qt < NQ; ++qt) {
    mx[qt] = red_g_max(mx[qt]); c.l[qt] = 0.f;
#pragma unroll
    for (int dt = 0; dt < 8; ++dt) c.O[qt][dt] = f32x4{0, 0, 0, 0};
  }
#pragma unroll 1
  for (int st = 0; st < SK / 32; ++st) {
    u32x4 pw[NQ];
#pragma unroll
    for (int kt = 0; kt < 2; ++kt) {
      f32x4 s4[NQ];
#pragma unroll
      for (int qt = 0; qt < NQ; ++qt) s4[qt] = f32x4{-mx[qt], -mx[qt], -mx[qt], -mx[qt]};
#pragma unroll
      for (int s = 0; s < 4; ++s) {
        const bf16x8 kf = *(const bf16x8*)(kp + (2 * st + kt) * 16 * DH + s * 32);
#pragma unroll
        for (int qt = 0; qt < NQ; ++qt) s4[qt] = MFMA16(kf, c.Q[qt][s], s4[qt]);
      }
#pragma unroll
      for (int qt = 0; qt < NQ; ++qt) {
        float e[4];
#pragma unroll
        for (int j = 0; j < 4; ++j) { e[j] = __builtin_amdgcn_exp2f(s4[qt][j]); c.l[qt] += e[j]; }
        pw[qt][2 * kt] = cvtpk(e[0], e[1]); pw[qt][2 * kt + 1] = cvtpk(e[2], e[3]);
      }
    }
    const u16* vp = vh + (32 * st + 4 * g) * DH + li;
#pragma unroll
    for (int dt = 0; dt < 8; ++dt) {
      bf16x8 vf;
#pragma unroll
      for (int j = 0; j < 8; ++j) vf[j] = (short)vp[(16 * (j >> 2) + (j & 3)) * DH + 16 * dt];
#pragma unroll
      for (int qt = 0; qt < NQ; ++qt) c.O[qt][dt] = MFMA16(vf, __builtin_bit_cast(bf16x8, pw[qt]), c.O[qt][dt]);
      SBAR();
    }
  }
#pragma unroll
  for (int qt = 0; qt < NQ; ++qt) c.l[qt] = red_g_sum(c.l[qt]);
}
template <int NQ> DI void att_epilogue(AttC<NQ>& c, char* ob, const u16* kh, const u16* vh) {
  bool bad = false;
#pragma unroll
  for (int qt = 0; qt < NQ; ++qt) { c.l[qt] = red_g_sum(c.l[qt]); bad |= !(c.l[qt] < 16777216.0f); }
  if (__builtin_expect(__any(bad), 0)) att_redo_true_max<NQ>(c, kh, vh);
#pragma unroll
  for (int qt = 0; qt < NQ; ++qt) {
    const float inv = __builtin_amdgcn_rcpf(c.l[qt]);
#pragma unroll
    for (int s = 0; s < 4; ++s) {
      const f32x4 a = c.O[qt][2 * s] * inv, bb = c.O[qt][2 * s + 1] * inv;
      u32x4 w = {cvtpk(a[0], a[1]), cvtpk(a[2], a[3]), cvtpk(bb[0], bb[1]), cvtpk(bb[2], bb[3])};
      asm volatile("global_store_dwordx4 %0, %1, off " OST_POL "\n\ts_nop 1" :: "v"(ob + (qt * 4 + s) * 1024), "v"(w) : "memory");
    }
  }
}

DI bf16x8 q_frag(const f32x4& t0, const f32x4& t1) {
  const f32x4 u0 = t0 * QSCALE, u1 = t1 * QSCALE;
  unsigned a0 = cvtpk(u0[0], u0[1]), a1 = cvtpk(u0[2], u0[3]), b0 = cvtpk(u1[0], u1[1]), b1 = cvtpk(u1[2], u1[3]);
  auto r0 = __builtin_amdgcn_permlane32_swap(a0, b0, false, false);
  auto r1 = __builtin_amdgcn_permlane32_swap(a1, b1, false, false);
  auto s0 = __builtin_amdgcn_permlane16_swap(r0[0], r0[1], false, false);
  auto s1 = __builtin_amdgcn_permlane16_swap(r1[0], r1[1], false, false);
  u32x4 w = {s0[0], s1[0], s0[1], s1[1]};
  return __builtin_bit_cast(bf16x8, w);
}
template <int NR, int WS, int XO, bool Z = false>
DI void q_unit(f32x4 (&qa)[NR][4], const bf16x8 (&wc)[4], bf16x8 (&wn)[4], bf16x8 (&xc)[NR], const bf16x8& lc, bf16x8 (&xn)[NR], bf16x8& ln, unsigned w0, unsigned w1, const unsigned (&xa)[3]) {
#define XADDR(row) (xa[(row) >> 1] + ((row) & 1) * 2048 + XO)
#pragma unroll
  for (int q = 0; q < NR; ++q) {
#pragma unroll
    for (int d = 0; d < 4; ++d) {
      if constexpr (Z) MFMA16_Z(qa[q][d], wc[d], q < NR - 1 ? xc[q] : lc); else MFMA16_V(qa[q][d], wc[d], q < NR - 1 ? xc[q] : lc);
      SBAR();
      const int i = 4 * q + d;
      if (WS >= 0 && i < 8 && !(i & 1)) { const int u = i >> 1; const unsigned a = ((u & 1) ? w1 : w0) + 8192 * WS + 512 * (u >> 1); wn[u] = cat8(ldstr(a), ldstr(a + 256)); }
      if (XO >= 0 && (i & 1) && (i >> 1) < NR) xn[i >> 1] = lds128(XADDR(i >> 1));
      if (XO >= 0 && i == 10) ln = lds128(XADDR(NR - 1) + 512);
      SBAR();
    }
    if (XO >= 0 && q < NR - 1) { xc[q] = lds128(XADDR(q) + 512); SBAR(); }
  }
#undef XADDR
}

#define LDA(p) __hip_atomic_load((p), __ATOMIC_RELAXED, __HIP_MEMORY_SCOPE_AGENT)
#define STA(p, v) __hip_atomic_store((p), (v), __ATOMIC_RELAXED, __HIP_MEMORY_SCOPE_AGENT)
constexpr int SPIN_CAP = 1 << 21;
struct Hs { unsigned* mine; unsigned* peer; unsigned me, x, e; bool ok; };
DI void hs_post(const unsigned* p, unsigned box) { asm volatile("s_mov_b32 m0, %0\n\ts_nop 3\n\tglobal_load_lds_dword %1, off sc1" :: "s"(box), "v"(p) : "memory"); }
DI unsigned hs_take(unsigned box) { WAITV(0); return *(volatile LAS unsigned*)(box + 4 * lane_id()); }
DI void hs_entry(Hs& h, char* wsb, unsigned blk) {
  const unsigned lane = lane_id();
  h.me = (blk >> 3) & 7;
  h.mine = (unsigned*)(wsb + WS_F) + blk * 64;
  h.peer = (unsigned*)(wsb + WS_F) + ((blk & ~56u) + 8 * (lane & 7)) * 64;
  if (lane >= 1 && lane <= 3) STA(h.mine + lane, 0u);
  const unsigned y0 = LDA(h.peer + 32 + h.me);
  unsigned x = rfl(y0) + 1;
  while (__any(y0 == x)) ++x;
  h.x = x; h.e = 0; h.ok = false;
  WAITV(0);
  if (lane == 0) STA(h.mine, x);
}
DI void hs_wait_flag(const Hs& h, unsigned v, int word) {
  for (int spin = 0; !__all(v == 1u) && spin < SPIN_CAP; ++spin) { __builtin_amdgcn_s_sleep(2); v = LDA(h.peer + word); }
  __builtin_amdgcn_fence(__ATOMIC_ACQUIRE, "agent");
}
constexpr unsigned HBOX = 161792;
DI void hs_hook(Hs& h, int k) {
  const unsigned lane = lane_id();
  if (k == 0) hs_post(h.peer, HBOX);
  else if (k == 1) { h.e = hs_take(HBOX); if (lane < 8) STA(h.mine + 32 + lane, h.e); }
  else if (k == 4) hs_post(h.peer + 32 + h.me, HBOX);
  else if (k == 5) { h.ok = __all(hs_take(HBOX) == h.x); hs_post(h.peer, HBOX); }
  else if (k == 6) { const unsigned e = hs_take(HBOX); if (lane < 8 && e != h.e) STA(h.mine + 32 + lane, e); hs_post(h.peer + 1, HBOX); }
  else if (k == 7) {
    unsigned f = hs_take(HBOX);
    if (!h.ok) {
      for (int spin = 0; spin < SPIN_CAP; ++spin) {
        const unsigned e = LDA(h.peer);
        if (lane < 8) STA(h.mine + 32 + lane, e);
        if (__all(LDA(h.peer + 32 + h.me) == h.x)) break;
        __builtin_amdgcn_s_sleep(2);
      }
      WAITV(0);
      const unsigned e = LDA(h.peer);
      if (lane < 8) STA(h.mine + 32 + lane, e);
      f = 0;
    }
    hs_wait_flag(h, f, 1);
  }
}

constexpr unsigned EX = 122880;
template <int NR> DI void q_phase(bf16x8 (&own)[2][2], unsigned wid, unsigned lb) {
  asm volatile("; q phase %0" :: "i"(NR) : "memory");
  const unsigned lane = lane_id(), li = lane & 15, g = lane >> 4, pos = lane & 3, tq = li >> 2;
  unsigned w0 = lb + WRING + 1024 * (wid & 1) + 2048 * g + 64 * tq + 32 * (g & 1) + 8 * pos, w1 = w0 ^ 32u;
  const unsigned xl = XB + 1024 * (li >> 3) + 64 * (li & 7) + 16 * (g ^ (2 * (li >> 3)));
  unsigned xa[3] = {xl + wid * 4096, xl + (wid ^ 1) * 4096, xl + 16384};
  bf16x8 w[4], v[4], xA[NR], xB[NR], l0, l1;
  f32x4 qa[NR][4];
  PIN(w0); PIN(w1); PIN(xa[0]); PIN(xa[1]); if (NR == 5) PIN(xa[2]);
  BAR();
#pragma unroll
  for (int u = 0; u < 4; ++u) { const unsigned a = ((u & 1) ? w1 : w0) + 512 * (u >> 1); w[u] = cat8(ldstr(a), ldstr(a + 256)); }
#pragma unroll
  for (int q = 0; q < NR; ++q) { const unsigned a = xa[q >> 1] + (q & 1) * 2048 + XSLOT; xB[q] = lds128(a); if (q < NR - 1) xA[q] = lds128(a + 512); else l0 = lds128(a + 512); }
  BAR(); q_unit<NR, 1, -1, true>(qa, w, v, xB, xB[NR - 1], xA, l1, w0, w1, xa);
#pragma unroll 1
  for (int n = 0; n < 4; ++n) {
    WAITV(0);
    BAR(); q_unit<NR, 2, 0>(qa, v, w, xA, l0, xB, l1, w0, w1, xa);
    BAR(); q_unit<NR, 3, -1>(qa, w, v, xB, xB[NR - 1], xA, l0, w0, w1, xa);
    BAR(); q_unit<NR, 0, XSLOT>(qa, v, w, xA, l1, xB, l0, w0, w1, xa);
    BAR(); q_unit<NR, 1, -1>(qa, w, v, xB, xB[NR - 1], xA, l1, w0, w1, xa);
  }
  BAR(); q_unit<NR, -1, -1>(qa, v, w, xA, l0, xB, l1, w0, w1, xa);
  asm volatile("s_nop 7" ::: "memory"); SBAR();
  unsigned ea = EX + (lb ? 0 : 20480) + lane * 16; PIN(ea);
#pragma unroll
  for (int s = 0; s < 2; ++s) {
    *(LAS bf16x8*)(ea + wid * 4096 + s * 1024) = q_frag(qa[2][2 * s], qa[2][2 * s + 1]);
    *(LAS bf16x8*)(ea + wid * 4096 + 2048 + s * 1024) = q_frag(qa[3][2 * s], qa[3][2 * s + 1]);
    if constexpr (NR == 5) *(LAS bf16x8*)(ea + 16384 + wid * 2048 + s * 1024) = q_frag(qa[4][2 * s], qa[4][2 * s + 1]);
  }
#pragma unroll
  for (int q = 0; q < 2; ++q)
#pragma unroll
    for (int s = 0; s < 2; ++s) own[q][s] = q_frag(qa[q][2 * s], qa[q][2 * s + 1]);
}

template <int NQ> DI void att_wave(char* ows_pair, const u16* kbase, const u16* vbase, unsigned qt0, unsigned m, unsigned hg0, unsigned lb, Hs& hs, const u16* x) {
  asm volatile("; attention wave NQ=%0" :: "i"(NQ) : "memory");
  STAMP(20);
#pragma unroll 1
  for (int r = 0; r < 2; ++r) {
    AttC<NQ> c;
    STAMP(4 * r);
    if constexpr (NQ == 2) {
      bf16x8 own[2][2];
      if (qt0 < 4) q_phase<5>(own, qt0 >> 1, lb); else q_phase<4>(own, qt0 >> 1, lb);
      unsigned ea = EX + (lb ? 0 : 20480) + (qt0 ^ 2) * 2048 + lane_id() * 16; PIN(ea);
      BAR();
      const bool hi = qt0 & 2;
#pragma unroll
      for (int q = 0; q < 2; ++q)
#pragma unroll
        for (int s = 0; s < 2; ++s) { const bf16x8 rc = lds128(ea + q * 2048 + s * 1024); c.Q[q][s] = hi ? rc : own[q][s]; c.Q[q][2 + s] = hi ? own[q][s] : rc; }
    } else {
      const __amdgpu_buffer_rsrc_t srdX = __builtin_amdgcn_make_buffer_rsrc((void*)x, (short)0, SQ * DM * 2, 0x00020000);
      const unsigned lane = lane_id(), half = lb != 0, vx = ((((lane >> 2) & 7) + 8 * half) * (DM * 2) + (lane >> 5) * 64 + (lane & 3) * 16) ^ (32u * half);
      BAR(); if (r == 0) STAMP(21);
#pragma unroll 1
      for (int n = 1; n < 19; n += 2) {
        BAR();
        if (n < 14) { dma_tile9<16 * DM * 2>(((n & 2) ? XB : XA) + half * 1024, ((n + 3) >> 1) * 128, vx, srdX); WAITV(9); } else WAITV(0);
        BAR(); if (r == 0 && n == 5) STAMP(22); if (r == 0 && n == 13) STAMP(23);
        if (n == 1 && lb == 0 && r == 1 && lane == 0) STA(hs.mine + 1, 1u);
      }
      BAR();
      unsigned ea = EX + (lb ? 0 : 20480) + 16384 + lane_id() * 16; PIN(ea);
#pragma unroll
      for (int s = 0; s < 4; ++s) c.Q[0][s] = lds128(ea + s * 1024);
      __builtin_amdgcn_s_waitcnt(0xC07F);
    }
    { const unsigned lane = lane_id(), li = lane & 15, g = lane >> 4;
      c.kb = lb + 2048 * (li >> 3) + 64 * (li & 7) + 16 * (g ^ (2 * (li >> 3)));
      c.vb0 = lb + 2048 * (g >> 1) + 64 * (4 * (g & 1) + (li >> 2)) + 32 * (g & 1) + 8 * (lane & 3); c.vb1 = c.vb0 ^ 32u;
      PIN(c.kb); PIN(c.vb0); PIN(c.vb1); }
    STAMP(4 * r + 1);
    if (NQ == 1 && lb == 0) hs_hook(hs, 4 * r);
    att_prologue<NQ>(c);
    SBAR();
#pragma unroll 1
    for (int ii = 0; ii < 3; ++ii) { att_step<NQ, 0, true>(c); att_step<NQ, 1, true>(c); att_step<NQ, 2, true>(c); att_step<NQ, 3, true>(c);
      if (NQ == 1 && lb == 0) hs_hook(hs, 4 * r + 1 + ii); }
    att_step<NQ, 0, true>(c); att_step<NQ, 1, true>(c);
    if (NQ == 1) WAITV(0);
    att_step<NQ, 2, true>(c); att_step<NQ, 3, false>(c);
    STAMP(4 * r + 2);
    asm volatile("s_nop 7\n\ts_nop 1" ::: "memory"); SBAR();
    const unsigned h = 8 * ((hg0 + 2 * r) & 3) + m;
    att_epilogue<NQ>(c, ows_pair + (size_t)h * ITEM_B + qt0 * 4096 + lane_id() * 16, kbase + (size_t)h * SK * DH, vbase + (size_t)h * SK * DH);
    STAMP(4 * r + 3);
  }
}

DI void wait_newer(int n) {
  if (n >= 32) { if (n >= 40) WAITV(40); else WAITV(32); }
  else if (n >= 16) { if (n >= 24) WAITV(24); else WAITV(16); }
  else if (n >= 8) WAITV(8); else WAITV(0);
}
DI unsigned oslot_o(unsigned s) { return s == 0 ? 131072 : GRP - 9216 + 9216 * s; }
DI unsigned oslot_w(unsigned s) { return s == 0 ? 140288 : 18432 * (s - 1); }
constexpr unsigned OPART = 93184;
constexpr unsigned OBOX = 55296;
struct OLd {
  __amdgpu_buffer_rsrc_t srd; unsigned vo[9], h0, isw, rot;
  DI void init(const char* op, const u16* wo, unsigned m, unsigned g0, unsigned isw_, unsigned rot_) {
    const unsigned lane = lane_id(); h0 = 8 * g0 + (m & 4); isw = isw_; rot = rot_;
    srd = isw ? __builtin_amdgcn_make_buffer_rsrc((void*)wo, (short)0, HD * DM * 2, 0x00020000) : __builtin_amdgcn_make_buffer_rsrc((void*)op, (short)0, NH * ITEM_B, 0x00020000);
#pragma unroll
    for (int p = 0; p < 9; ++p) { const unsigned n = 64 * p + lane, row = n / 36, ch = n - 36 * row;
      vo[p] = isw ? row * (DM * 2) + ((ch ^ ((row >> 1) & 2)) * 16) + (m & 1) * 576 : p * 4096 + lane * 16; }
  }
#define OPIECE(k) "buffer_load_dwordx4 %" #k ", %1, %2 offen lds\n\ts_add_u32 m0, m0, 1024\n\ts_nop 0\n\t"
  DI void issue(int j, unsigned part) const {
    const unsigned h = ((h0 + (j & 16)) & 31) + (((j >> 2) + rot) & 3), s = j & 3;
    const unsigned so = isw ? ((h * 4 + s) * 32 + 16 * part) * (DM * 2) : h * ITEM_B + s * 1024, lo = isw ? oslot_w(j & 3) + part * 9216 : oslot_o(j & 3);
    asm volatile("s_mov_b32 m0, %0\n\ts_nop 3\n\t" OPIECE(3) OPIECE(4) OPIECE(5) OPIECE(6) OPIECE(7) OPIECE(8) OPIECE(9) OPIECE(10) OPIECE(11)
                 :: "s"(lo), "s"(srd), "s"(so), "v"(vo[0]), "v"(vo[1]), "v"(vo[2]), "v"(vo[3]), "v"(vo[4]), "v"(vo[5]), "v"(vo[6]), "v"(vo[7]), "v"(vo[8]) : "memory", "scc");
  }
  DI void run(unsigned part) const {
#pragma unroll 1
    for (int j = 0; j < 32; ++j) {
      if (j == 0 || j == 30) WAITV(9); else if (j < 30) WAITV(18); else WAITV(0);
      RBAR();
      if (j == 0) issue(2, part);
      if (j + 3 < 32) issue(j + 3, part);
    }
  }
};

DI void loader(const u16* wq, const u16* x, const u16* kbase, const u16* vbase, unsigned m, unsigned hg0, unsigned half, OLd& ol, const char* op, const u16* wo, unsigned g0, unsigned rot) {
  const unsigned lane = lane_id(), row7 = (lane >> 2) & 7, sh = lane >> 5, pos = lane & 3, lb = half * GRP;
  const __amdgpu_buffer_rsrc_t srdW = __builtin_amdgcn_make_buffer_rsrc((void*)wq, (short)0, DM * HD * 2, 0x00020000);
  const __amdgpu_buffer_rsrc_t srdX = __builtin_amdgcn_make_buffer_rsrc((void*)x, (short)0, SQ * DM * 2, 0x00020000);
  const __amdgpu_buffer_rsrc_t srdK = __builtin_amdgcn_make_buffer_rsrc((void*)kbase, (short)0, NH * SK * DH * 2, 0x00020000);
  const __amdgpu_buffer_rsrc_t srdV = __builtin_amdgcn_make_buffer_rsrc((void*)vbase, (short)0, NH * SK * DH * 2, 0x00020000);
  const unsigned vw0 = row7 * (HD * 2) + sh * 64 + pos * 16, vw1 = vw0 ^ 32u;
  const unsigned vx = (row7 * (DM * 2) + sh * 64 + pos * 16) ^ (32u * half);
  const unsigned vk0 = row7 * 256 + sh * 64 + pos * 16, vk1 = vk0 ^ 32u, vv = row7 * 256 + sh * 64 + ((pos ^ (2 * (row7 >> 2))) * 16);
  int cnt = 0;
  auto issue_w = [&](unsigned h, int s) {
    dma_tile8<WNT, 8 * HD * 2>(lb + WRING + (s & 3) * 8192, s * 32 * (HD * 2) + h * (DH * 2), vw0, vw1, srdW);
    return cnt += 8;
  };
  auto issue_x = [&](int e) {
    dma_tile9<16 * DM * 2>(((e & 1) ? XB : XA) + half * 1024, e * 128 + half * 8 * (DM * 2), vx, srdX);
    return cnt += 9;
  };
  auto issue_k = [&](unsigned h, int t) {
    const unsigned q = t & 3, lo = q == 0 ? kslot(0) : q == 1 ? kslot(1) : q == 2 ? kslot(2) : kslot(3);
    dma_tile8<KVNT_, 2048>(lb + lo, h * (SK * DH * 2) + TROT(t) * 8192, vk0, vk1, srdK);
    return cnt += 8;
  };
  auto issue_v = [&](unsigned h, int t) {
    const unsigned q = t & 3, lo = q == 0 ? vslot(0) : q == 1 ? vslot(1) : q == 2 ? vslot(2) : vslot(3);
    dma_tile8<KVNT_, 2048>(lb + lo, h * (SK * DH * 2) + TROT(t) * 8192, vv, vv, srdV);
    return cnt += 8;
  };
  unsigned h = 8 * hg0 + m;
  unsigned tnext = 0;
  int kx = 0;
  int n0, n1, n2, n3;
  issue_w(h, 0); n0 = issue_x(0); n1 = issue_w(h, 1); issue_w(h, 2);
  ol.init(op, wo, m, g0, half ^ 1, rot);
#pragma unroll 1
  for (int r = 0; r < 2; ++r) {
    const unsigned hn = 8 * ((hg0 + 2) & 3) + m;
    wait_newer(cnt - n0); RBAR();
    n2 = issue_x(1);
    if (r == 0) issue_w(h, 3);
    n3 = cnt;
#pragma unroll 1
    for (int G = 1; G < 15; ++G) {
      wait_newer(cnt - n1); RBAR();
      n1 = n2; n2 = n3;
      issue_w(h, G + 3);
      n3 = cnt;
      if (r == 0) { if (G == KS0) kx = issue_k(h, 0); if (G == KS1) issue_k(h, 1); if (G == KS2) issue_v(h, 0); }
    }
    int a0 = cnt, a1, a2;
    wait_newer(cnt - n1); RBAR();
    issue_k(h, 2);
    wait_newer(cnt - n2); RBAR();
    a1 = issue_v(h, 1);
    wait_newer(cnt - n3); RBAR();
    issue_k(h, 3);
    wait_newer(cnt - kx); RBAR();
    a2 = issue_v(h, 2);
    RBAR();
#pragma unroll 1
    for (int i = 0; i < 16; ++i) {
      wait_newer(cnt - a0);
      RBAR();
      a0 = a1; a1 = a2;
      if (i < 12) issue_k(h, i + 4);
      if (i < 13) issue_v(h, i + 3);
      a2 = cnt;
      if (r == 0) {
        if (i == 12) issue_k(hn, 0);
        if (i == 13) { issue_k(hn, 1); issue_v(hn, 0); }
        if (i == 14) { issue_x(0); issue_w(hn, 2); issue_w(hn, 3); }
        if (i == 15) { n0 = issue_w(hn, 0); n1 = issue_w(hn, 1); }
      } else {
        if (i == 13 && ol.isw) { ol.issue(0, 0); ol.issue(0, 1); ol.issue(1, 0); ol.issue(1, 1); cnt += 36; }
        if (i == 14 && !ol.isw) { ol.issue(0, 0); ol.issue(1, 0); cnt += 18; }
      }
    }
    h = hn;
  }
}

template <int NQT, int NNT> struct OSet { bf16x8 w[NNT], ol; };
template <int NQT, int NNT, bool RD, bool Z> DI void o_unit(f32x4 (&acc)[NQT][NNT], bf16x8 (&of)[NQT - 1], const OSet<NQT, NNT>& cs, OSet<NQT, NNT>& ns, unsigned oa, unsigned wa0, unsigned wa1) {
#pragma unroll
  for (int q = 0; q < NQT; ++q) {
#pragma unroll
    for (int n = 0; n < NNT; ++n) {
      if constexpr (Z) MFMA16_Z(acc[q][n], cs.w[n], q < NQT - 1 ? of[q < NQT - 1 ? q : 0] : cs.ol); else MFMA16_V(acc[q][n], cs.w[n], q < NQT - 1 ? of[q < NQT - 1 ? q : 0] : cs.ol);
      SBAR();
      if (RD && q == 0) { const unsigned a = ((n & 1) ? wa1 : wa0) + 64 * (n >> 1); ns.w[n] = cat8(ldstr(a), ldstr(a + 9216)); }
      if (RD && q == 1 && n == 1) ns.ol = lds128(oa + (NQT - 1) * 1024);
      SBAR();
    }
    if (RD && q < NQT - 1) { of[q] = lds128(oa + q * 1024); SBAR(); }
  }
}
template <int NQT, int NNT> DI void oproj_consumer(char* __restrict__ wsb, unsigned nb, unsigned kq, unsigned blk) {
  asm volatile("; oproj consumer %0 x %1" :: "i"(NQT), "i"(NNT) : "memory");
  constexpr unsigned qh = NQT == 4, q0 = 5 * qh;
  f32x4 acc[NQT][NNT];
  bf16x8 of[NQT - 1]; OSet<NQT, NNT> sa, sb;
  unsigned oa, wa0, wa1;
  { const unsigned lane = lane_id(), li = lane & 15, g = lane >> 4, n0 = (9 * nb + 1) >> 1;
    const unsigned rb = oslot_w(0) + 576 * (4 * g + (li >> 2)) + 8 * (lane & 3);
    oa = oslot_o(0) + q0 * 1024 + lane * 16;
    wa0 = rb + 64 * (n0 >> 1) + 32 * ((n0 ^ g) & 1); wa1 = rb + 64 * ((n0 + 1) >> 1) + 32 * ((n0 ^ g ^ 1) & 1); }
#define NEXTO(j) do { const unsigned d_ = oslot_w(((j) + 1) & 3) - oslot_w((j) & 3); oa += oslot_o(((j) + 1) & 3) - oslot_o((j) & 3); wa0 += d_; wa1 += d_; PIN(oa); PIN(wa0); PIN(wa1); } while (0)
  PIN(oa); PIN(wa0); PIN(wa1);
  STAMP2(24);
  BAR();
  STAMP2(25);
#pragma unroll
  for (int n = 0; n < NNT; ++n) { const unsigned a = ((n & 1) ? wa1 : wa0) + 64 * (n >> 1); sa.w[n] = cat8(ldstr(a), ldstr(a + 9216)); }
#pragma unroll
  for (int q = 0; q < NQT - 1; ++q) of[q] = lds128(oa + q * 1024);
  sa.ol = lds128(oa + (NQT - 1) * 1024);
  NEXTO(0);
  BAR(); o_unit<NQT, NNT, true, true>(acc, of, sa, sb, oa, wa0, wa1); NEXTO(1);
  WAITV(0);
#pragma unroll 1
  for (int j = 2; j < 32; j += 2) {
    BAR(); o_unit<NQT, NNT, true, false>(acc, of, sb, sa, oa, wa0, wa1); NEXTO(j);
    BAR(); o_unit<NQT, NNT, true, false>(acc, of, sa, sb, oa, wa0, wa1); NEXTO(j + 1);
  }
  o_unit<NQT, NNT, false, false>(acc, of, sb, sa, 0, 0, 0);
  asm volatile("s_nop 7\n\ts_nop 1" ::: "memory"); SBAR();
  STAMP2(26);
  const unsigned lane = lane_id();
  if (nb == kq) {
    unsigned ra = OPART + q0 * 5120 + lane * 16; PIN(ra);
#pragma unroll
    for (int q = 0; q < NQT; ++q)
#pragma unroll
      for (int n = 0; n < NNT; ++n) *(LAS f32x4*)(ra + (q * 5 + n) * 1024) = acc[q][n];
  } else {
    const __amdgpu_buffer_rsrc_t srdS = __builtin_amdgcn_make_buffer_rsrc((void*)(wsb + WS_X + (size_t)(blk * 4 + nb) * 46080), (short)0, 46080, 0x00020000);
#pragma unroll
    for (int q = 0; q < NQT; ++q)
#pragma unroll
      for (int u = 0; u < (NNT + 1) / 2; ++u) {
        const f32x4 a = acc[q][2 * u], c2 = acc[q][2 * u + 1 < NNT ? 2 * u + 1 : 2 * u];
        __builtin_amdgcn_raw_buffer_store_b128(u32x4{cvtpk(a[0], a[1]), cvtpk(a[2], a[3]), cvtpk(c2[0], c2[1]), cvtpk(c2[2], c2[3])}, srdS, lane * 16, ((q0 + q) * 3 + u) * 1024, XAUX);
      }
    WAITV(0);
  }
}
DI void oproj_finish(u16* __restrict__ outp, char* __restrict__ wsb, unsigned q, unsigned kq, unsigned blk) {
  const unsigned lane = lane_id(), li = lane & 15, g = lane >> 4, nnt = 5 - (kq & 1);
  BAR();
  STAMP2(27);
  unsigned* fl = (unsigned*)(wsb + WS_F);
  if (q == 8 && lane == 0) STA(fl + blk * 64 + 3, 1u);
  { const unsigned* pf = fl + ((blk & ~48u) + 16 * (lane & 3)) * 64 + 3;
    for (int spin = 0; spin < SPIN_CAP && !__all(LDA(pf) == 1u); ++spin) __builtin_amdgcn_s_sleep(1);
    __builtin_amdgcn_fence(__ATOMIC_ACQUIRE, "wavefront"); }
  STAMP2(28);
  f32x4 px[3][5];
  u32x4 pw[3][3];
#pragma unroll
  for (int p = 0; p < 3; ++p) {
    const unsigned pb = (blk & ~48u) + 16 * ((kq + 1 + p) & 3);
    const __amdgpu_buffer_rsrc_t srdP = __builtin_amdgcn_make_buffer_rsrc((void*)(wsb + WS_X + (size_t)(pb * 4 + kq) * 46080), (short)0, 46080, 0x00020000);
#pragma unroll
    for (int u = 0; u < 3; ++u) pw[p][u] = __builtin_amdgcn_raw_buffer_load_b128(srdP, lane * 16, (q * 3 + (u < 2 ? u : nnt - 3)) * 1024, 16);
  }
  SBAR();
#pragma unroll
  for (int p = 0; p < 3; ++p)
#pragma unroll
    for (int n = 0; n < 5; ++n) { const unsigned lo = pw[p][n >> 1][2 * (n & 1)], hi = pw[p][n >> 1][2 * (n & 1) + 1];
      px[p][n] = f32x4{__uint_as_float(lo << 16), __uint_as_float(lo & 0xffff0000u), __uint_as_float(hi << 16), __uint_as_float(hi & 0xffff0000u)}; }
  const unsigned tb = OPART + q * 5120;
  f32x4 own[5];
#pragma unroll
  for (int n = 0; n < 5; ++n) own[n] = *(const LAS f32x4*)(tb + (n < 4 ? n : nnt - 1) * 1024 + lane * 16);
#pragma unroll
  for (int n = 0; n < 5; ++n) {
    const f32x4 a = (own[n] + px[0][n]) + (px[1][n] + px[2][n]);
    *(LAS u32x2*)(tb + li * 176 + g * 8 + n * 32) = u32x2{cvtpk(a[0], a[1]), cvtpk(a[2], a[3])};
  }
#pragma unroll
  for (unsigned i = 0; i < 3; ++i) {
    const unsigned ch = i * 64 + lane, row = ch / 10, cc = ch - row * 10;
    if (row < 16 && cc < 2 * nnt) {
      *(u32x4*)(outp + (size_t)(16 * q + row) * (2 * DM) + cc * 8) = *(const LAS u32x4*)(tb + row * 176 + cc * 16);
    }
  }
}

__global__ __launch_bounds__(768, 3) void k_fused(const u16* __restrict__ xA, const u16* __restrict__ kA, const u16* __restrict__ vA, const u16* __restrict__ wqA, const u16* __restrict__ woA,
                                                  const u16* __restrict__ xB, const u16* __restrict__ kB, const u16* __restrict__ vB, const u16* __restrict__ wqB, const u16* __restrict__ woB,
                                                  char* __restrict__ wsb, u16* __restrict__ out) {
  extern __shared__ __attribute__((aligned(16))) char smem[];
  const unsigned wid12 = rfl(threadIdx.x >> 6), half = wid12 >= 6, wid = wid12 - 6 * half, lb = half * GRP;
  const unsigned blk = blockIdx.x;
  const unsigned idx = blk >> 3, pair = (blk & 7) * 4 + (idx >> 3), m = idx & 7, br = pair >> 4, b = pair & 15;
  const unsigned hg0 = (blk + half) & 3, kq = m >> 1, g0 = ((blk ^ kq) & 1) ? (blk + 1) & 3 : blk & 3;
  char* op = wsb + WS_O + (size_t)pair * NH * ITEM_B;
  const u16* kb = (br ? kB : kA) + (size_t)b * NH * SK * DH; const u16* vb = (br ? vB : vA) + (size_t)b * NH * SK * DH;
  Hs hs;
  if (wid < 4) {
    att_wave<2>(op, kb, vb, 2 * wid, m, hg0, lb, hs, nullptr);
    const unsigned t = half ? 3 - ((wid + 2) & 3) : wid, nb = 2 * half + (t & 1);
    if (t == 0) oproj_consumer<5, 5>(wsb, nb, kq, blk);
    else if (t == 1) oproj_consumer<5, 4>(wsb, nb, kq, blk);
    else if (t == 2) oproj_consumer<4, 5>(wsb, nb, kq, blk);
    else oproj_consumer<4, 4>(wsb, nb, kq, blk);
  } else if (wid == 4) {
    if (!half) hs_entry(hs, wsb, blk);
    else {
      const __amdgpu_buffer_rsrc_t srdT = __builtin_amdgcn_make_buffer_rsrc((char*)((br ? xB : xA) + (size_t)b * SQ * DM) + m * 20736, (short)0, 20736, 0x00020000);
      __builtin_amdgcn_s_sleep(XWARM);
#pragma unroll
      for (int p = 0; p < 3; ++p) { const unsigned n = p * 64 + lane_id(); dma4(vslot(3), (n < 162 ? n : 0) * 128, srdT); }
      {
        constexpr unsigned LN = (576 - WQW) * 64 / 128;
        const __amdgpu_buffer_rsrc_t srdQ = __builtin_amdgcn_make_buffer_rsrc((char*)((blk & 1) ? wqB : wqA) + (size_t)WQW * 8192 + (size_t)(blk >> 1) * LN * 128, (short)0, LN * 128, 0x00020000);
#pragma unroll
        for (unsigned p = 0; p < (LN + 63) / 64; ++p) { const unsigned n = p * 64 + lane_id(); dma4(vslot(3), (n < LN ? n : 0) * 128, srdQ); } }
    }
    att_wave<1>(op, kb, vb, 8, m, hg0, lb, hs, (br ? xB : xA) + (size_t)b * SQ * DM);
    if (half) {
      OLd ol; ol.init(op, br ? woB : woA, m, g0, 1, pair & 3);
      WAITV(0);
      ol.run(1);
    } else {
      WAITV(0);
      RBAR(); RBAR(); RBAR();
      if (lane_id() == 0) STA(hs.mine + 2, 1u);
#pragma unroll 1
      for (int j = 3; j < 8; ++j) RBAR();
      hs_post(hs.peer + 2, OBOX);
      RBAR(); RBAR();
      hs_wait_flag(hs, hs_take(OBOX), 2);
      RBAR(); RBAR(); RBAR();
      WAITV(0);
    }
    return;
  } else {
    OLd ol;
    loader(br ? wqB : wqA, (br ? xB : xA) + (size_t)b * SQ * DM, kb, vb, m, hg0, half, ol, op, br ? woB : woA, g0, pair & 3);
    ol.run(0);
    if (half) return;
  }
  oproj_finish(out + (size_t)b * SQ * (2 * DM) + br * DM + (m & 1) * 288 + 16 * ((9 * kq + 1) >> 1), wsb, wid < 4 ? wid + 4 * half : 8, kq, blk);
}

extern "C" void kernel_launch(void* const* d_in, const int* in_sizes, int n_in, void* d_out, int out_size, void* d_ws, size_t ws_size, hipStream_t stream) {
  (void)hipFuncSetAttribute((const void*)k_fused, hipFuncAttributeMaxDynamicSharedMemorySize, LDS_BYTES);
  k_fused<<<256, 768, LDS_BYTES, stream>>>((const u16*)d_in[0], (const u16*)d_in[1], (const u16*)d_in[2], (const u16*)d_in[3], (const u16*)d_in[4],
                                           (const u16*)d_in[5], (const u16*)d_in[6], (const u16*)d_in[7], (const u16*)d_in[8], (const u16*)d_in[9], (char*)d_ws, (u16*)d_out);
}
```

```cpp
#include <hip/hip_runtime.h>
#include <stdint.h>

typedef short    bf16x8 __attribute__((ext_vector_type(8)));
typedef short    s16x4  __attribute__((ext_vector_type(4)));
typedef float    f32x4  __attribute__((ext_vector_type(4)));
typedef float    f32x2  __attribute__((ext_vector_type(2)));
typedef __bf16   bf16x2 __attribute__((ext_vector_type(2)));
typedef unsigned u32x4  __attribute__((ext_vector_type(4)));
typedef unsigned u32x2  __attribute__((ext_vector_type(2)));
typedef unsigned short u16;
#define DI __device__ __forceinline__
#define LAS __attribute__((address_space(3)))
#define MFMA16(a, b, c) __builtin_amdgcn_mfma_f32_16x16x32_bf16(a, b, c, 0, 0, 0)
#define SBAR() __builtin_amdgcn_sched_barrier(0)
#define PIN(x) asm volatile("" : "+v"(x))
#define MFMA16_V(acc, a, b) asm volatile("v_mfma_f32_16x16x32_bf16 %0, %1, %2, %0" : "+v"(acc) : "v"(a), "v"(b))
#define MFMA16_Z(d, a, b) asm volatile("v_mfma_f32_16x16x32_bf16 %0, %1, %2, 0" : "=&v"(d) : "v"(a), "v"(b))
#define MFMA16_C(d, a, b, cc) asm volatile("v_mfma_f32_16x16x32_bf16 %0, %1, %2, %3" : "=&v"(d) : "v"(a), "v"(b), "v"(cc))

constexpr int NB = 16, SQ = 144, DM = 576, NH = 32, SK = 512, DH = 128, HD = NH * DH;
constexpr int ITEM_B = 9 * 4 * 1024;
constexpr size_t WS_O = 0;
constexpr size_t WS_X = (size_t)1024 * ITEM_B, WS_F = WS_X + (size_t)256 * 4 * 46080;
constexpr float QSCALE = 0.08838834764831845f * 1.4426950408889634f;

DI unsigned rfl(unsigned x) { return __builtin_amdgcn_readfirstlane(x); }
DI unsigned lane_id() { unsigned l; asm volatile("v_mbcnt_lo_u32_b32 %0, -1, 0\n\tv_mbcnt_hi_u32_b32 %0, -1, %0" : "=v"(l)); return l; }
DI unsigned cvtpk(float lo, float hi) { f32x2 v = {lo, hi}; bf16x2 b = __builtin_convertvector(v, bf16x2); return __builtin_bit_cast(unsigned, b); }
DI bf16x8 lds128(unsigned a) { return *(const LAS bf16x8*)a; }
DI s16x4 ldstr(unsigned a) { return __builtin_bit_cast(s16x4, __builtin_amdgcn_ds_read_tr16_b64_v4i16((LAS s16x4*)a)); }
DI bf16x8 cat8(s16x4 lo, s16x4 hi) { return __builtin_shufflevector(lo, hi, 0, 1, 2, 3, 4, 5, 6, 7); }
DI void dma4(unsigned m0v, unsigned voff, __amdgpu_buffer_rsrc_t srd) {
  asm volatile("s_mov_b32 m0, %0\n\ts_nop 3\n\tbuffer_load_dword %1, %2, 0 offen lds" :: "s"(m0v), "v"(voff), "s"(srd) : "memory");
}
#define TILE8_BODY(POL) \
  "s_nop 3\n\ts_mov_b32 m0, %1\n\ts_mov_b32 %0, %2\n\t" \
  "buffer_load_dwordx4 %3, %5, %0 offen " POL " lds\n\ts_add_u32 m0, m0, 896\n\ts_nop 0\n\tbuffer_load_dwordx4 %3, %5, %0 offen offset:128 " POL " lds\n\ts_add_u32 m0, m0, 1152\n\ts_add_u32 %0, %0, %6\n\t" \
  "buffer_load_dwordx4 %4, %5, %0 offen " POL " lds\n\ts_add_u32 m0, m0, 896\n\ts_nop 0\n\tbuffer_load_dwordx4 %4, %5, %0 offen offset:128 " POL " lds\n\ts_add_u32 m0, m0, 1152\n\ts_add_u32 %0, %0, %6\n\t" \
  "buffer_load_dwordx4 %3, %5, %0 offen " POL " lds\n\ts_add_u32 m0, m0, 896\n\ts_nop 0\n\tbuffer_load_dwordx4 %3, %5, %0 offen offset:128 " POL " lds\n\ts_add_u32 m0, m0, 1152\n\ts_add_u32 %0, %0, %6\n\t" \
  "buffer_load_dwordx4 %4, %5, %0 offen " POL " lds\n\ts_add_u32 m0, m0, 896\n\ts_nop 0\n\tbuffer_load_dwordx4 %4, %5, %0 offen offset:128 " POL " lds"
template <bool NT, int D2> DI void dma_tile8(unsigned lo, unsigned so, unsigned va, unsigned vb, __amdgpu_buffer_rsrc_t srd) {
  unsigned t;
  if constexpr (NT) asm volatile(TILE8_BODY("nt") : "=&s"(t) : "s"(lo), "s"(so), "v"(va), "v"(vb), "s"(srd), "i"(D2) : "memory", "scc");
  else              asm volatile(TILE8_BODY("")   : "=&s"(t) : "s"(lo), "s"(so), "v"(va), "v"(vb), "s"(srd), "i"(D2) : "memory", "scc");
}
template <int D2> DI void dma_tile9(unsigned lo, unsigned so, unsigned va, __amdgpu_buffer_rsrc_t srd) {
  unsigned t;
  asm volatile("s_nop 3\n\ts_mov_b32 m0, %1\n\ts_mov_b32 %0, %2\n\t.rept 9\n\t"
               "buffer_load_dwordx4 %3, %4, %0 offen lds\n\ts_add_u32 m0, m0, 2048\n\ts_add_u32 %0, %0, %5\n\t.endr"
               : "=&s"(t) : "s"(lo), "s"(so), "v"(va), "s"(srd), "i"(D2) : "memory", "scc");
}
#define WAITV(n) asm volatile("s_waitcnt vmcnt(" #n ")" ::: "memory")
#define BAR() do { asm volatile("" ::: "memory"); __builtin_amdgcn_s_waitcnt(0xC07F); asm volatile("s_barrier" ::: "memory"); } while (0)
#define RBAR() asm volatile("s_barrier" ::: "memory")
constexpr unsigned GRP = 65536, WRING = 3 * 8192, XSLOT = 18432, XB = 126976, XA = XB + XSLOT, LDS_BYTES = 163840;
constexpr unsigned kslot(int i) { return 8192 * ((i & 3) == 0 ? 0 : (i & 3) == 1 ? 1 : (i & 3) == 2 ? 5 : 3); }
constexpr unsigned vslot(int i) { return 8192 * ((i & 3) == 0 ? 2 : (i & 3) == 1 ? 6 : (i & 3) == 2 ? 4 : 7); }
static_assert(XA + XSLOT == LDS_BYTES, "LDS map");


DI float red_g_max(float m) {
  auto r = __builtin_amdgcn_permlane32_swap(__float_as_uint(m), __float_as_uint(m), false, false);
  m = fmaxf(__uint_as_float(r[0]), __uint_as_float(r[1]));
  auto s = __builtin_amdgcn_permlane16_swap(__float_as_uint(m), __float_as_uint(m), false, false);
  return fmaxf(__uint_as_float(s[0]), __uint_as_float(s[1]));
}
DI float red_g_sum(float m) {
  auto r = __builtin_amdgcn_permlane32_swap(__float_as_uint(m), __float_as_uint(m), false, false);
  m = __uint_as_float(r[0]) + __uint_as_float(r[1]);
  auto s = __builtin_amdgcn_permlane16_swap(__float_as_uint(m), __float_as_uint(m), false, false);
  return __uint_as_float(s[0]) + __uint_as_float(s[1]);
}

template <int NQ> struct AttC {
  f32x4 O[NQ][8]; bf16x8 Q[NQ][4]; f32x4 S[2][NQ][2]; f32x4 negm[NQ]; float l[NQ];
  unsigned kb, vb0, vb1;
};
template <int NQ> struct Tab;
template <> struct Tab<2> {
  static constexpr int NPRE = 6;
  static constexpr int pre[6] = {100, 101, 102, 103, 104, 105};
  static constexpr int gx[16][2] = {{106, 0}, {107, 0}, {200, 201}, {110, 0}, {202, 203}, {111, 0}, {112, 0}, {113, 0}, {114, 0}, {210, 211}, {115, 0}, {116, 0}, {117, 0}, {300, 310}, {212, 213}, {301, 311}};
  static constexpr int gy[16] = {302, 312, 303, 313, 304, 314, 305, 315, 306, 316, 307, 317, 0, 0, 0, 0};
  static constexpr int all[24] = {100, 101, 102, 103, 104, 105, 106, 107, 110, 200, 201, 202, 203, 111, 112, 113, 114, 115, 116, 117, 210, 211, 212, 213};
  static constexpr int ady[16] = {300, 310, 301, 311, 302, 312, 303, 313, 304, 314, 305, 315, 306, 316, 307, 317};
};
template <> struct Tab<1> {
  static constexpr int NPRE = 4;
  static constexpr int pre[4] = {100, 101, 102, 103};
  static constexpr int gx[8][2] = {{104, 0}, {105, 0}, {200, 201}, {106, 0}, {107, 0}, {300, 301}, {202, 203}, {302, 303}};
  static constexpr int gy[8] = {304, 305, 306, 307, 0, 0, 0, 0};
  static constexpr int all[12] = {100, 101, 102, 103, 104, 105, 106, 107, 200, 201, 202, 203};
  static constexpr int ady[8] = {300, 301, 302, 303, 304, 305, 306, 307};
};
template <int NQ, int P, bool QK> struct Step {
  static constexpr int X = P & 1;
  static constexpr unsigned ks_ = kslot(P + 1), vs_ = vslot(P);
  bf16x8 fr[4]; float e[NQ][8]; unsigned pw[NQ][4]; u32x4 Pw[NQ];
  template <int OP> DI void op(AttC<NQ>& c) {
    constexpr int ty = OP / 100, qt = (OP / 10) % 10, j = OP % 10;
    if constexpr (ty == 1) { e[qt][j] = c.S[X][qt][j >> 2][j & 3]; asm volatile("v_exp_f32 %0, %0" : "+v"(e[qt][j])); }
    if constexpr (ty == 2) asm volatile("v_cvt_pk_bf16_f32 %0, %1, %2" : "=v"(pw[qt][j]) : "v"(e[qt][2 * j]), "v"(e[qt][2 * j + 1]));
    if constexpr (ty == 3) asm volatile("v_add_f32 %0, %0, %1" : "+v"(c.l[qt]) : "v"(e[qt][j]));
  }
  template <int R> DI void rd(AttC<NQ>& c) {
    if constexpr (R < 8) fr[R & 3] = lds128(c.kb + ks_ + 4096 * (R & 1) + 512 * (R >> 1));
    else if constexpr (R < 16) {
      constexpr int dt = R - 8;
      const unsigned vb = ((dt & 1) ? c.vb1 : c.vb0) + vs_ + 512 * (dt >> 1);
      fr[R & 3] = cat8(ldstr(vb), ldstr(vb + 4096));
    }
    SBAR();
  }
  template <int I> DI void gx(AttC<NQ>& c) {
    constexpr int K = I / NQ, qt = I % NQ, s = K >> 1, kt = K & 1;
    if constexpr (s == 0) MFMA16_C(c.S[X ^ 1][qt][kt], fr[K & 3], c.Q[qt][s], c.negm[qt]); else MFMA16_V(c.S[X ^ 1][qt][kt], fr[K & 3], c.Q[qt][s]);
    if constexpr (qt == NQ - 1) { SBAR(); rd<K + 4>(c); }
    op<Tab<NQ>::gx[I][0]>(c); op<Tab<NQ>::gx[I][1]>(c);
    if constexpr (I + 1 < 8 * NQ) gx<I + 1>(c);
  }
  template <int I> DI void gy(AttC<NQ>& c) {
    constexpr int Dt = I / NQ, qt = I % NQ;
    MFMA16_V(c.O[qt][Dt], fr[Dt & 3], Pw[qt]);
    if constexpr (qt == NQ - 1) { SBAR(); rd<Dt + 12>(c); }
    op<(QK ? Tab<NQ>::gy[I] : Tab<NQ>::ady[I])>(c);
    if constexpr (I + 1 < 8 * NQ) gy<I + 1>(c);
  }
  template <int I> DI void pre(AttC<NQ>& c) {
    op<(QK ? Tab<NQ>::pre[I < Tab<NQ>::NPRE ? I : 0] : Tab<NQ>::all[I])>(c);
    if constexpr (I + 1 < (QK ? Tab<NQ>::NPRE : 12 * NQ)) pre<I + 1>(c);
  }
  DI void run(AttC<NQ>& c) {
    if constexpr (QK) {
      rd<0>(c); rd<1>(c); rd<2>(c); rd<3>(c);
      pre<0>(c);
      gx<0>(c);
    } else {
      rd<8>(c); rd<9>(c); rd<10>(c); rd<11>(c);
      pre<0>(c);
      asm volatile("s_nop 1");
    }
#pragma unroll
    for (int qt = 0; qt < NQ; ++qt) Pw[qt] = u32x4{pw[qt][0], pw[qt][1], pw[qt][2], pw[qt][3]};
    gy<0>(c);
  }
};

template <int NQ, int P, bool QK> DI void att_step(AttC<NQ>& c) { BAR(); SBAR();
  Step<NQ, P, QK> s; s.run(c); SBAR(); }

template <int NQ> DI void att_prologue(AttC<NQ>& c) {
#pragma unroll
  for (int s = 0; s < 4; ++s)
#pragma unroll
    for (int kt = 0; kt < 2; ++kt) {
      const bf16x8 kf = lds128(c.kb + 4096 * kt + 512 * s);
#pragma unroll
      for (int qt = 0; qt < NQ; ++qt) c.S[0][qt][kt] = MFMA16(kf, c.Q[qt][s], s == 0 ? (f32x4{0, 0, 0, 0}) : c.S[0][qt][kt]);
    }
#pragma unroll
  for (int qt = 0; qt < NQ; ++qt) {
    float mx = c.S[0][qt][0][0];
#pragma unroll
    for (int j = 1; j < 8; ++j) mx = fmaxf(mx, c.S[0][qt][j >> 2][j & 3]);
    mx = red_g_max(mx);
    c.negm[qt] = f32x4{-mx, -mx, -mx, -mx};
    c.S[0][qt][0] += c.negm[qt]; c.S[0][qt][1] += c.negm[qt];
    c.l[qt] = 0.f;
#pragma unroll
    for (int dt = 0; dt < 8; ++dt) c.O[qt][dt] = f32x4{0, 0, 0, 0};
  }
}
template <int NQ> DI void att_redo_true_max(AttC<NQ>& c, const u16* __restrict__ kh, const u16* __restrict__ vh) {
  const unsigned lane = lane_id(), li = lane & 15, g = lane >> 4;
  const u16* kp = kh + li * DH + g * 8;
  float mx[NQ];
#pragma unroll
  for (int qt = 0; qt < NQ; ++qt) mx[qt] = -__builtin_inff();
#pragma unroll 1
  for (int kt = 0; kt < SK / 16; ++kt) {
    f32x4 s4[NQ];
#pragma unroll
    for (int qt = 0; qt < NQ; ++qt) s4[qt] = f32x4{0, 0, 0, 0};
#pragma unroll
    for (int s = 0; s < 4; ++s) {
      const bf16x8 kf = *(const bf16x8*)(kp + kt * 16 * DH + s * 32);
#pragma unroll
      for (int qt = 0; qt < NQ; ++qt) s4[qt] = MFMA16(kf, c.Q[qt][s], s4[qt]);
    }
#pragma unroll
    for (int qt = 0; qt < NQ; ++qt) mx[qt] = fmaxf(fmaxf(fmaxf(mx[qt], s4[qt][0]), fmaxf(s4[qt][1], s4[qt][2])), s4[qt][3]);
  }
#pragma unroll
  for (int qt = 0; qt < NQ; ++qt) {
    mx[qt] = red_g_max(mx[qt]); c.l[qt] = 0.f;
#pragma unroll
    for (int dt = 0; dt < 8; ++dt) c.O[qt][dt] = f32x4{0, 0, 0, 0};
  }
#pragma unroll 1
  for (int st = 0; st < SK / 32; ++st) {
    u32x4 pw[NQ];
#pragma unroll
    for (int kt = 0; kt < 2; ++kt) {
      f32x4 s4[NQ];
#pragma unroll
      for (int qt = 0; qt < NQ; ++qt) s4[qt] = f32x4{-mx[qt], -mx[qt], -mx[qt], -mx[qt]};
#pragma unroll
      for (int s = 0; s < 4; ++s) {
        const bf16x8 kf = *(const bf16x8*)(kp + (2 * st + kt) * 16 * DH + s * 32);
#pragma unroll
        for (int qt = 0; qt < NQ; ++qt) s4[qt] = MFMA16(kf, c.Q[qt][s], s4[qt]);
      }
#pragma unroll
      for (int qt = 0; qt < NQ; ++qt) {
        float e[4];
#pragma unroll
        for (int j = 0; j < 4; ++j) { e[j] = __builtin_amdgcn_exp2f(s4[qt][j]); c.l[qt] += e[j]; }
        pw[qt][2 * kt] = cvtpk(e[0], e[1]); pw[qt][2 * kt + 1] = cvtpk(e[2], e[3]);
      }
    }
    const u16* vp = vh + (32 * st + 4 * g) * DH + li;
#pragma unroll
    for (int dt = 0; dt < 8; ++dt) {
      bf16x8 vf;
#pragma unroll
      for (int j = 0; j < 8; ++j) vf[j] = (short)vp[(16 * (j >> 2) + (j & 3)) * DH + 16 * dt];
#pragma unroll
      for (int qt = 0; qt < NQ; ++qt) c.O[qt][dt] = MFMA16(vf, __builtin_bit_cast(bf16x8, pw[qt]), c.O[qt][dt]);
      SBAR();
    }
  }
#pragma unroll
  for (int qt = 0; qt < NQ; ++qt) c.l[qt] = red_g_sum(c.l[qt]);
}
template <int NQ> DI void att_epilogue(AttC<NQ>& c, char* ob, const u16* kh, const u16* vh) {
  bool bad = false;
#pragma unroll
  for (int qt = 0; qt < NQ; ++qt) { c.l[qt] = red_g_sum(c.l[qt]); bad |= !(c.l[qt] < 16777216.0f); }
  if (__builtin_expect(__any(bad), 0)) att_redo_true_max<NQ>(c, kh, vh);
#pragma unroll
  for (int qt = 0; qt < NQ; ++qt) {
    const float inv = __builtin_amdgcn_rcpf(c.l[qt]);
#pragma unroll
    for (int s = 0; s < 4; ++s) {
      const f32x4 a = c.O[qt][2 * s] * inv, bb = c.O[qt][2 * s + 1] * inv;
      u32x4 w = {cvtpk(a[0], a[1]), cvtpk(a[2], a[3]), cvtpk(bb[0], bb[1]), cvtpk(bb[2], bb[3])};
      asm volatile("global_store_dwordx4 %0, %1, off sc1\n\ts_nop 1" :: "v"(ob + (qt * 4 + s) * 1024), "v"(w) : "memory");
    }
  }
}

DI bf16x8 q_frag(const f32x4& t0, const f32x4& t1) {
  const f32x4 u0 = t0 * QSCALE, u1 = t1 * QSCALE;
  unsigned a0 = cvtpk(u0[0], u0[1]), a1 = cvtpk(u0[2], u0[3]), b0 = cvtpk(u1[0], u1[1]), b1 = cvtpk(u1[2], u1[3]);
  auto r0 = __builtin_amdgcn_permlane32_swap(a0, b0, false, false);
  auto r1 = __builtin_amdgcn_permlane32_swap(a1, b1, false, false);
  auto s0 = __builtin_amdgcn_permlane16_swap(r0[0], r0[1], false, false);
  auto s1 = __builtin_amdgcn_permlane16_swap(r1[0], r1[1], false, false);
  u32x4 w = {s0[0], s1[0], s0[1], s1[1]};
  return __builtin_bit_cast(bf16x8, w);
}
template <int NR, int WS, int XO, bool Z = false>
DI void q_unit(f32x4 (&qa)[NR][4], const bf16x8 (&wc)[4], bf16x8 (&wn)[4], bf16x8 (&xc)[NR], const bf16x8& lc, bf16x8 (&xn)[NR], bf16x8& ln, unsigned w0, unsigned w1, const unsigned (&xa)[3]) {
#define XADDR(row) (xa[(row) >> 1] + ((row) & 1) * 2048 + XO)
#pragma unroll
  for (int q = 0; q < NR; ++q) {
#pragma unroll
    for (int d = 0; d < 4; ++d) {
      if constexpr (Z) MFMA16_Z(qa[q][d], wc[d], q < NR - 1 ? xc[q] : lc); else MFMA16_V(qa[q][d], wc[d], q < NR - 1 ? xc[q] : lc);
      SBAR();
      const int i = 4 * q + d;
      if (WS >= 0 && i < 8 && !(i & 1)) { const int u = i >> 1; const unsigned a = ((u & 1) ? w1 : w0) + 8192 * WS + 512 * (u >> 1); wn[u] = cat8(ldstr(a), ldstr(a + 256)); }
      if (XO >= 0 && (i & 1) && (i >> 1) < NR) xn[i >> 1] = lds128(XADDR(i >> 1));
      if (XO >= 0 && i == 10) ln = lds128(XADDR(NR - 1) + 512);
      SBAR();
    }
    if (XO >= 0 && q < NR - 1) { xc[q] = lds128(XADDR(q) + 512); SBAR(); }
  }
#undef XADDR
}

#define LDA(p) __hip_atomic_load((p), __ATOMIC_RELAXED, __HIP_MEMORY_SCOPE_AGENT)
#define STA(p, v) __hip_atomic_store((p), (v), __ATOMIC_RELAXED, __HIP_MEMORY_SCOPE_AGENT)
constexpr int SPIN_CAP = 1 << 21;
struct Hs { unsigned* mine; unsigned* peer; unsigned me, x, e; bool ok; };
DI void hs_post(const unsigned* p, unsigned box) { asm volatile("s_mov_b32 m0, %0\n\ts_nop 3\n\tglobal_load_lds_dword %1, off sc1" :: "s"(box), "v"(p) : "memory"); }
DI unsigned hs_take(unsigned box) { WAITV(0); return *(volatile LAS unsigned*)(box + 4 * lane_id()); }
DI void hs_entry(Hs& h, char* wsb, unsigned blk) {
  const unsigned lane = lane_id();
  h.me = (blk >> 3) & 7;
  h.mine = (unsigned*)(wsb + WS_F) + blk * 64;
  h.peer = (unsigned*)(wsb + WS_F) + ((blk & ~56u) + 8 * (lane & 7)) * 64;
  if (lane >= 1 && lane <= 3) STA(h.mine + lane, 0u);
  const unsigned y0 = LDA(h.peer + 32 + h.me);
  unsigned x = rfl(y0) + 1;
  while (__any(y0 == x)) ++x;
  h.x = x; h.e = 0; h.ok = false;
  WAITV(0);
  if (lane == 0) STA(h.mine, x);
}
DI void hs_wait_flag(const Hs& h, unsigned v, int word) {
  for (int spin = 0; !__all(v == 1u) && spin < SPIN_CAP; ++spin) { __builtin_amdgcn_s_sleep(2); v = LDA(h.peer + word); }
  __builtin_amdgcn_fence(__ATOMIC_ACQUIRE, "agent");
}
constexpr unsigned HBOX = 161792;
DI void hs_hook(Hs& h, int k) {
  const unsigned lane = lane_id();
  if (k == 0) hs_post(h.peer, HBOX);
  else if (k == 1) { h.e = hs_take(HBOX); if (lane < 8) STA(h.mine + 32 + lane, h.e); }
  else if (k == 4) hs_post(h.peer + 32 + h.me, HBOX);
  else if (k == 5) { h.ok = __all(hs_take(HBOX) == h.x); hs_post(h.peer, HBOX); }
  else if (k == 6) { const unsigned e = hs_take(HBOX); if (lane < 8 && e != h.e) STA(h.mine + 32 + lane, e); hs_post(h.peer + 1, HBOX); }
  else if (k == 7) {
    unsigned f = hs_take(HBOX);
    if (!h.ok) {
      for (int spin = 0; spin < SPIN_CAP; ++spin) {
        const unsigned e = LDA(h.peer);
        if (lane < 8) STA(h.mine + 32 + lane, e);
        if (__all(LDA(h.peer + 32 + h.me) == h.x)) break;
        __builtin_amdgcn_s_sleep(2);
      }
      WAITV(0);
      const unsigned e = LDA(h.peer);
      if (lane < 8) STA(h.mine + 32 + lane, e);
      f = 0;
    }
    hs_wait_flag(h, f, 1);
  }
}

constexpr unsigned EX = 122880;
template <int NR> DI void q_phase(bf16x8 (&own)[2][2], unsigned wid, unsigned lb) {
  asm volatile("; q phase %0" :: "i"(NR) : "memory");
  const unsigned lane = lane_id(), li = lane & 15, g = lane >> 4, pos = lane & 3, tq = li >> 2;
  unsigned w0 = lb + WRING + 1024 * (wid & 1) + 2048 * g + 64 * tq + 32 * (g & 1) + 8 * pos, w1 = w0 ^ 32u;
  const unsigned xl = XB + 1024 * (li >> 3) + 64 * (li & 7) + 16 * (g ^ (2 * (li >> 3)));
  unsigned xa[3] = {xl + wid * 4096, xl + (wid ^ 1) * 4096, xl + 16384};
  bf16x8 w[4], v[4], xA[NR], xB[NR], l0, l1;
  f32x4 qa[NR][4];
  PIN(w0); PIN(w1); PIN(xa[0]); PIN(xa[1]); if (NR == 5) PIN(xa[2]);
  BAR();
#pragma unroll
  for (int u = 0; u < 4; ++u) { const unsigned a = ((u & 1) ? w1 : w0) + 512 * (u >> 1); w[u] = cat8(ldstr(a), ldstr(a + 256)); }
#pragma unroll
  for (int q = 0; q < NR; ++q) { const unsigned a = xa[q >> 1] + (q & 1) * 2048 + XSLOT; xB[q] = lds128(a); if (q < NR - 1) xA[q] = lds128(a + 512); else l0 = lds128(a + 512); }
  BAR(); q_unit<NR, 1, -1, true>(qa, w, v, xB, xB[NR - 1], xA, l1, w0, w1, xa);
#pragma unroll 1
  for (int n = 0; n < 4; ++n) {
    WAITV(0);
    BAR(); q_unit<NR, 2, 0>(qa, v, w, xA, l0, xB, l1, w0, w1, xa);
    BAR(); q_unit<NR, 3, -1>(qa, w, v, xB, xB[NR - 1], xA, l0, w0, w1, xa);
    BAR(); q_unit<NR, 0, XSLOT>(qa, v, w, xA, l1, xB, l0, w0, w1, xa);
    BAR(); q_unit<NR, 1, -1>(qa, w, v, xB, xB[NR - 1], xA, l1, w0, w1, xa);
  }
  BAR(); q_unit<NR, -1, -1>(qa, v, w, xA, l0, xB, l1, w0, w1, xa);
  asm volatile("s_nop 7" ::: "memory"); SBAR();
  unsigned ea = EX + (lb ? 0 : 20480) + lane * 16; PIN(ea);
#pragma unroll
  for (int s = 0; s < 2; ++s) {
    *(LAS bf16x8*)(ea + wid * 4096 + s * 1024) = q_frag(qa[2][2 * s], qa[2][2 * s + 1]);
    *(LAS bf16x8*)(ea + wid * 4096 + 2048 + s * 1024) = q_frag(qa[3][2 * s], qa[3][2 * s + 1]);
    if constexpr (NR == 5) *(LAS bf16x8*)(ea + 16384 + wid * 2048 + s * 1024) = q_frag(qa[4][2 * s], qa[4][2 * s + 1]);
  }
#pragma unroll
  for (int q = 0; q < 2; ++q)
#pragma unroll
    for (int s = 0; s < 2; ++s) own[q][s] = q_frag(qa[q][2 * s], qa[q][2 * s + 1]);
}

template <int NQ> DI void att_wave(char* ows_pair, const u16* kbase, const u16* vbase, unsigned qt0, unsigned m, unsigned hg0, unsigned lb, Hs& hs, const u16* x) {
  asm volatile("; attention wave NQ=%0" :: "i"(NQ) : "memory");
#pragma unroll 1
  for (int r = 0; r < 2; ++r) {
    AttC<NQ> c;
    if constexpr (NQ == 2) {
      bf16x8 own[2][2];
      if (qt0 < 4) q_phase<5>(own, qt0 >> 1, lb); else q_phase<4>(own, qt0 >> 1, lb);
      unsigned ea = EX + (lb ? 0 : 20480) + (qt0 ^ 2) * 2048 + lane_id() * 16; PIN(ea);
      BAR();
      const bool hi = qt0 & 2;
#pragma unroll
      for (int q = 0; q < 2; ++q)
#pragma unroll
        for (int s = 0; s < 2; ++s) { const bf16x8 rc = lds128(ea + q * 2048 + s * 1024); c.Q[q][s] = hi ? rc : own[q][s]; c.Q[q][2 + s] = hi ? own[q][s] : rc; }
    } else {
      const __amdgpu_buffer_rsrc_t srdX = __builtin_amdgcn_make_buffer_rsrc((void*)x, (short)0, SQ * DM * 2, 0x00020000);
      const unsigned lane = lane_id(), half = lb != 0, vx = ((((lane >> 2) & 7) + 8 * half) * (DM * 2) + (lane >> 5) * 64 + (lane & 3) * 16) ^ (32u * half);
      BAR();
#pragma unroll 1
      for (int n = 1; n < 19; n += 2) {
        BAR();
        if (n < 14) { dma_tile9<16 * DM * 2>(((n & 2) ? XB : XA) + half * 1024, ((n + 3) >> 1) * 128, vx, srdX); WAITV(9); } else WAITV(0);
        BAR();         if (n == 1 && lb == 0 && r == 1 && lane == 0) STA(hs.mine + 1, 1u);
      }
      BAR();
      unsigned ea = EX + (lb ? 0 : 20480) + 16384 + lane_id() * 16; PIN(ea);
#pragma unroll
      for (int s = 0; s < 4; ++s) c.Q[0][s] = lds128(ea + s * 1024);
      __builtin_amdgcn_s_waitcnt(0xC07F);
    }
    { const unsigned lane = lane_id(), li = lane & 15, g = lane >> 4;
      c.kb = lb + 2048 * (li >> 3) + 64 * (li & 7) + 16 * (g ^ (2 * (li >> 3)));
      c.vb0 = lb + 2048 * (g >> 1) + 64 * (4 * (g & 1) + (li >> 2)) + 32 * (g & 1) + 8 * (lane & 3); c.vb1 = c.vb0 ^ 32u;
      PIN(c.kb); PIN(c.vb0); PIN(c.vb1); }
    if (NQ == 1 && lb == 0) hs_hook(hs, 4 * r);
    att_prologue<NQ>(c);
    SBAR();
#pragma unroll 1
    for (int ii = 0; ii < 3; ++ii) { att_step<NQ, 0, true>(c); att_step<NQ, 1, true>(c); att_step<NQ, 2, true>(c); att_step<NQ, 3, true>(c);
      if (NQ == 1 && lb == 0) hs_hook(hs, 4 * r + 1 + ii); }
    att_step<NQ, 0, true>(c); att_step<NQ, 1, true>(c);
    if (NQ == 1) WAITV(0);
    att_step<NQ, 2, true>(c); att_step<NQ, 3, false>(c);
    asm volatile("s_nop 7\n\ts_nop 1" ::: "memory"); SBAR();
    const unsigned h = 8 * ((hg0 + 2 * r) & 3) + m;
    att_epilogue<NQ>(c, ows_pair + (size_t)h * ITEM_B + qt0 * 4096 + lane_id() * 16, kbase + (size_t)h * SK * DH, vbase + (size_t)h * SK * DH);
  }
}

constexpr unsigned STEP_TICKS = 106;
DI void wait_newer(int n) {
  if (n >= 32) { if (n >= 40) WAITV(40); else WAITV(32); }
  else if (n >= 16) { if (n >= 24) WAITV(24); else WAITV(16); }
  else if (n >= 8) WAITV(8); else WAITV(0);
}
DI unsigned oslot_o(unsigned s) { return s == 0 ? 131072 : GRP - 9216 + 9216 * s; }
DI unsigned oslot_w(unsigned s) { return s == 0 ? 140288 : 18432 * (s - 1); }
constexpr unsigned OPART = 93184;
constexpr unsigned OBOX = 55296;
struct OLd {
  __amdgpu_buffer_rsrc_t srd; unsigned vo[9], h0, isw, rot;
  DI void init(const char* op, const u16* wo, unsigned m, unsigned g0, unsigned isw_, unsigned rot_) {
    const unsigned lane = lane_id(); h0 = 8 * g0 + (m & 4); isw = isw_; rot = rot_;
    srd = isw ? __builtin_amdgcn_make_buffer_rsrc((void*)wo, (short)0, HD * DM * 2, 0x00020000) : __builtin_amdgcn_make_buffer_rsrc((void*)op, (short)0, NH * ITEM_B, 0x00020000);
#pragma unroll
    for (int p = 0; p < 9; ++p) { const unsigned n = 64 * p + lane, row = n / 36, ch = n - 36 * row;
      vo[p] = isw ? row * (DM * 2) + ((ch ^ ((row >> 1) & 2)) * 16) + (m & 1) * 576 : p * 4096 + lane * 16; }
  }
#define OPIECE(k) "buffer_load_dwordx4 %" #k ", %1, %2 offen lds\n\ts_add_u32 m0, m0, 1024\n\ts_nop 0\n\t"
  DI void issue(int j, unsigned part) const {
    const unsigned h = ((h0 + (j & 16)) & 31) + (((j >> 2) + rot) & 3), s = j & 3;
    const unsigned so = isw ? ((h * 4 + s) * 32 + 16 * part) * (DM * 2) : h * ITEM_B + s * 1024, lo = isw ? oslot_w(j & 3) + part * 9216 : oslot_o(j & 3);
    asm volatile("s_mov_b32 m0, %0\n\ts_nop 3\n\t" OPIECE(3) OPIECE(4) OPIECE(5) OPIECE(6) OPIECE(7) OPIECE(8) OPIECE(9) OPIECE(10) OPIECE(11)
                 :: "s"(lo), "s"(srd), "s"(so), "v"(vo[0]), "v"(vo[1]), "v"(vo[2]), "v"(vo[3]), "v"(vo[4]), "v"(vo[5]), "v"(vo[6]), "v"(vo[7]), "v"(vo[8]) : "memory", "scc");
  }
  DI void run(unsigned part) const {
#pragma unroll 1
    for (int j = 0; j < 32; ++j) {
      if (j == 0 || j == 30) WAITV(9); else if (j < 30) WAITV(18); else WAITV(0);
      RBAR();
      if (j == 0) issue(2, part);
      if (j + 3 < 32) issue(j + 3, part);
    }
  }
};

DI void loader(const u16* wq, const u16* x, const u16* kbase, const u16* vbase, unsigned m, unsigned hg0, unsigned half, OLd& ol, const char* op, const u16* wo, unsigned g0, unsigned rot) {
  const unsigned lane = lane_id(), row7 = (lane >> 2) & 7, sh = lane >> 5, pos = lane & 3, lb = half * GRP;
  const __amdgpu_buffer_rsrc_t srdW = __builtin_amdgcn_make_buffer_rsrc((void*)wq, (short)0, DM * HD * 2, 0x00020000);
  const __amdgpu_buffer_rsrc_t srdX = __builtin_amdgcn_make_buffer_rsrc((void*)x, (short)0, SQ * DM * 2, 0x00020000);
  const __amdgpu_buffer_rsrc_t srdK = __builtin_amdgcn_make_buffer_rsrc((void*)kbase, (short)0, NH * SK * DH * 2, 0x00020000);
  const __amdgpu_buffer_rsrc_t srdV = __builtin_amdgcn_make_buffer_rsrc((void*)vbase, (short)0, NH * SK * DH * 2, 0x00020000);
  const unsigned vw0 = row7 * (HD * 2) + sh * 64 + pos * 16, vw1 = vw0 ^ 32u;
  const unsigned vx = (row7 * (DM * 2) + sh * 64 + pos * 16) ^ (32u * half);
  const unsigned vk0 = row7 * 256 + sh * 64 + pos * 16, vk1 = vk0 ^ 32u, vv = row7 * 256 + sh * 64 + ((pos ^ (2 * (row7 >> 2))) * 16);
  int cnt = 0;
  auto issue_w = [&](unsigned h, int s) {
    dma_tile8<false, 8 * HD * 2>(lb + WRING + (s & 3) * 8192, s * 32 * (HD * 2) + h * (DH * 2), vw0, vw1, srdW);
    return cnt += 8;
  };
  auto issue_x = [&](int e) {
    dma_tile9<16 * DM * 2>(((e & 1) ? XB : XA) + half * 1024, e * 128 + half * 8 * (DM * 2), vx, srdX);
    return cnt += 9;
  };
  auto issue_k = [&](unsigned h, int t) {
    const unsigned q = t & 3, lo = q == 0 ? kslot(0) : q == 1 ? kslot(1) : q == 2 ? kslot(2) : kslot(3);
    dma_tile8<true, 2048>(lb + lo, h * (SK * DH * 2) + t * 8192, vk0, vk1, srdK);
    return cnt += 8;
  };
  auto issue_v = [&](unsigned h, int t) {
    const unsigned q = t & 3, lo = q == 0 ? vslot(0) : q == 1 ? vslot(1) : q == 2 ? vslot(2) : vslot(3);
    dma_tile8<true, 2048>(lb + lo, h * (SK * DH * 2) + t * 8192, vv, vv, srdV);
    return cnt += 8;
  };
  unsigned h = 8 * hg0 + m;
  unsigned tnext = 0;
  int kx = 0;
  int n0, n1, n2, n3;
  issue_w(h, 0); n0 = issue_x(0); n1 = issue_w(h, 1); issue_w(h, 2);
  ol.init(op, wo, m, g0, half ^ 1, rot);
#pragma unroll 1
  for (int r = 0; r < 2; ++r) {
    const unsigned hn = 8 * ((hg0 + 2) & 3) + m;
    wait_newer(cnt - n0); RBAR();
    n2 = issue_x(1);
    if (r == 0) issue_w(h, 3);
    n3 = cnt;
#pragma unroll 1
    for (int G = 1; G < 15; ++G) {
      wait_newer(cnt - n1); RBAR();
      n1 = n2; n2 = n3;
      issue_w(h, G + 3);
      n3 = cnt;
      if (r == 0) { if (G == 8) kx = issue_k(h, 0); if (G == 11) issue_k(h, 1); if (G == 14) issue_v(h, 0); }
    }
    int a0 = cnt, a1, a2;
    wait_newer(cnt - n1); RBAR();
    issue_k(h, 2);
    wait_newer(cnt - n2); RBAR();
    a1 = issue_v(h, 1);
    wait_newer(cnt - n3); RBAR();
    issue_k(h, 3);
    wait_newer(cnt - kx); RBAR();
    a2 = issue_v(h, 2);
    RBAR();
#pragma unroll 1
    for (int i = 0; i < 16; ++i) {
      wait_newer(cnt - a0);
      {
        unsigned now = (unsigned)__builtin_amdgcn_s_memrealtime();
        while (i > 0 && i < 14 && (int)(tnext - now) > 0) { __builtin_amdgcn_s_sleep(1); now = (unsigned)__builtin_amdgcn_s_memrealtime(); }
        tnext = now + STEP_TICKS; }
      RBAR();
      a0 = a1; a1 = a2;
      if (i < 12) issue_k(h, i + 4);
      if (i < 13) issue_v(h, i + 3);
      a2 = cnt;
      if (r == 0) {
        if (i == 12) issue_k(hn, 0);
        if (i == 13) { issue_k(hn, 1); issue_v(hn, 0); }
        if (i == 14) { issue_x(0); issue_w(hn, 2); issue_w(hn, 3); }
        if (i == 15) { n0 = issue_w(hn, 0); n1 = issue_w(hn, 1); }
      } else {
        if (i == 13 && ol.isw) { ol.issue(0, 0); ol.issue(0, 1); ol.issue(1, 0); ol.issue(1, 1); cnt += 36; }
        if (i == 14 && !ol.isw) { ol.issue(0, 0); ol.issue(1, 0); cnt += 18; }
      }
    }
    h = hn;
  }
}

template <int NQT, int NNT> struct OSet { bf16x8 w[NNT], ol; };
template <int NQT, int NNT, bool RD, bool Z> DI void o_unit(f32x4 (&acc)[NQT][NNT], bf16x8 (&of)[NQT - 1], const OSet<NQT, NNT>& cs, OSet<NQT, NNT>& ns, unsigned oa, unsigned wa0, unsigned wa1) {
#pragma unroll
  for (int q = 0; q < NQT; ++q) {
#pragma unroll
    for (int n = 0; n < NNT; ++n) {
      if constexpr (Z) MFMA16_Z(acc[q][n], cs.w[n], q < NQT - 1 ? of[q < NQT - 1 ? q : 0] : cs.ol); else MFMA16_V(acc[q][n], cs.w[n], q < NQT - 1 ? of[q < NQT - 1 ? q : 0] : cs.ol);
      SBAR();
      if (RD && q == 0) { const unsigned a = ((n & 1) ? wa1 : wa0) + 64 * (n >> 1); ns.w[n] = cat8(ldstr(a), ldstr(a + 9216)); }
      if (RD && q == 1 && n == 1) ns.ol = lds128(oa + (NQT - 1) * 1024);
      SBAR();
    }
    if (RD && q < NQT - 1) { of[q] = lds128(oa + q * 1024); SBAR(); }
  }
}
template <int NQT, int NNT> DI void oproj_consumer(char* __restrict__ wsb, unsigned nb, unsigned kq, unsigned blk) {
  asm volatile("; oproj consumer %0 x %1" :: "i"(NQT), "i"(NNT) : "memory");
  constexpr unsigned qh = NQT == 4, q0 = 5 * qh;
  f32x4 acc[NQT][NNT];
  bf16x8 of[NQT - 1]; OSet<NQT, NNT> sa, sb;
  unsigned oa, wa0, wa1;
  { const unsigned lane = lane_id(), li = lane & 15, g = lane >> 4, n0 = (9 * nb + 1) >> 1;
    const unsigned rb = oslot_w(0) + 576 * (4 * g + (li >> 2)) + 8 * (lane & 3);
    oa = oslot_o(0) + q0 * 1024 + lane * 16;
    wa0 = rb + 64 * (n0 >> 1) + 32 * ((n0 ^ g) & 1); wa1 = rb + 64 * ((n0 + 1) >> 1) + 32 * ((n0 ^ g ^ 1) & 1); }
#define NEXTO(j) do { const unsigned d_ = oslot_w(((j) + 1) & 3) - oslot_w((j) & 3); oa += oslot_o(((j) + 1) & 3) - oslot_o((j) & 3); wa0 += d_; wa1 += d_; PIN(oa); PIN(wa0); PIN(wa1); } while (0)
  PIN(oa); PIN(wa0); PIN(wa1);
  BAR();
#pragma unroll
  for (int n = 0; n < NNT; ++n) { const unsigned a = ((n & 1) ? wa1 : wa0) + 64 * (n >> 1); sa.w[n] = cat8(ldstr(a), ldstr(a + 9216)); }
#pragma unroll
  for (int q = 0; q < NQT - 1; ++q) of[q] = lds128(oa + q * 1024);
  sa.ol = lds128(oa + (NQT - 1) * 1024);
  NEXTO(0);
  BAR(); o_unit<NQT, NNT, true, true>(acc, of, sa, sb, oa, wa0, wa1); NEXTO(1);
  WAITV(0);
#pragma unroll 1
  for (int j = 2; j < 32; j += 2) {
    BAR(); o_unit<NQT, NNT, true, false>(acc, of, sb, sa, oa, wa0, wa1); NEXTO(j);
    BAR(); o_unit<NQT, NNT, true, false>(acc, of, sa, sb, oa, wa0, wa1); NEXTO(j + 1);
  }
  o_unit<NQT, NNT, false, false>(acc, of, sb, sa, 0, 0, 0);
  asm volatile("s_nop 7\n\ts_nop 1" ::: "memory"); SBAR();
  const unsigned lane = lane_id();
  if (nb == kq) {
    unsigned ra = OPART + q0 * 5120 + lane * 16; PIN(ra);
#pragma unroll
    for (int q = 0; q < NQT; ++q)
#pragma unroll
      for (int n = 0; n < NNT; ++n) *(LAS f32x4*)(ra + (q * 5 + n) * 1024) = acc[q][n];
  } else {
    const __amdgpu_buffer_rsrc_t srdS = __builtin_amdgcn_make_buffer_rsrc((void*)(wsb + WS_X + (size_t)(blk * 4 + nb) * 46080), (short)0, 46080, 0x00020000);
#pragma unroll
    for (int q = 0; q < NQT; ++q)
#pragma unroll
      for (int u = 0; u < (NNT + 1) / 2; ++u) {
        const f32x4 a = acc[q][2 * u], c2 = acc[q][2 * u + 1 < NNT ? 2 * u + 1 : 2 * u];
        __builtin_amdgcn_raw_buffer_store_b128(u32x4{cvtpk(a[0], a[1]), cvtpk(a[2], a[3]), cvtpk(c2[0], c2[1]), cvtpk(c2[2], c2[3])}, srdS, lane * 16, ((q0 + q) * 3 + u) * 1024, 16);
      }
    WAITV(0);
  }
}
DI void oproj_finish(u16* __restrict__ outp, char* __restrict__ wsb, unsigned q, unsigned kq, unsigned blk) {
  const unsigned lane = lane_id(), li = lane & 15, g = lane >> 4, nnt = 5 - (kq & 1);
  BAR();
  unsigned* fl = (unsigned*)(wsb + WS_F);
  if (q == 8 && lane == 0) STA(fl + blk * 64 + 3, 1u);
  { const unsigned* pf = fl + ((blk & ~48u) + 16 * (lane & 3)) * 64 + 3;
    for (int spin = 0; spin < SPIN_CAP && !__all(LDA(pf) == 1u); ++spin) __builtin_amdgcn_s_sleep(1);
    __builtin_amdgcn_fence(__ATOMIC_ACQUIRE, "wavefront"); }
  f32x4 px[3][5];
  u32x4 pw[3][3];
#pragma unroll
  for (int p = 0; p < 3; ++p) {
    const unsigned pb = (blk & ~48u) + 16 * ((kq + 1 + p) & 3);
    const __amdgpu_buffer_rsrc_t srdP = __builtin_amdgcn_make_buffer_rsrc((void*)(wsb + WS_X + (size_t)(pb * 4 + kq) * 46080), (short)0, 46080, 0x00020000);
#pragma unroll
    for (int u = 0; u < 3; ++u) pw[p][u] = __builtin_amdgcn_raw_buffer_load_b128(srdP, lane * 16, (q * 3 + (u < 2 ? u : nnt - 3)) * 1024, 16);
  }
  SBAR();
#pragma unroll
  for (int p = 0; p < 3; ++p)
#pragma unroll
    for (int n = 0; n < 5; ++n) { const unsigned lo = pw[p][n >> 1][2 * (n & 1)], hi = pw[p][n >> 1][2 * (n & 1) + 1];
      px[p][n] = f32x4{__uint_as_float(lo << 16), __uint_as_float(lo & 0xffff0000u), __uint_as_float(hi << 16), __uint_as_float(hi & 0xffff0000u)}; }
  const unsigned tb = OPART + q * 5120;
  f32x4 own[5];
#pragma unroll
  for (int n = 0; n < 5; ++n) own[n] = *(const LAS f32x4*)(tb + (n < 4 ? n : nnt - 1) * 1024 + lane * 16);
#pragma unroll
  for (int n = 0; n < 5; ++n) {
    const f32x4 a = (own[n] + px[0][n]) + (px[1][n] + px[2][n]);
    *(LAS u32x2*)(tb + li * 176 + g * 8 + n * 32) = u32x2{cvtpk(a[0], a[1]), cvtpk(a[2], a[3])};
  }
#pragma unroll
  for (unsigned i = 0; i < 3; ++i) {
    const unsigned ch = i * 64 + lane, row = ch / 10, cc = ch - row * 10;
    if (row < 16 && cc < 2 * nnt) {
      *(u32x4*)(outp + (size_t)(16 * q + row) * (2 * DM) + cc * 8) = *(const LAS u32x4*)(tb + row * 176 + cc * 16);
    }
  }
}

__global__ __launch_bounds__(768, 3) void k_fused(const u16* __restrict__ xA, const u16* __restrict__ kA, const u16* __restrict__ vA, const u16* __restrict__ wqA, const u16* __restrict__ woA,
                                                  const u16* __restrict__ xB, const u16* __restrict__ kB, const u16* __restrict__ vB, const u16* __restrict__ wqB, const u16* __restrict__ woB,
                                                  char* __restrict__ wsb, u16* __restrict__ out) {
  extern __shared__ __attribute__((aligned(16))) char smem[];
  const unsigned wid12 = rfl(threadIdx.x >> 6), half = wid12 >= 6, wid = wid12 - 6 * half, lb = half * GRP;
  const unsigned blk = blockIdx.x;
  const unsigned idx = blk >> 3, pair = (blk & 7) * 4 + (idx >> 3), m = idx & 7, br = pair >> 4, b = pair & 15;
  const unsigned hg0 = (blk + half) & 3, kq = m >> 1, g0 = ((blk ^ kq) & 1) ? (blk + 1) & 3 : blk & 3;
  char* op = wsb + WS_O + (size_t)pair * NH * ITEM_B;
  const u16* kb = (br ? kB : kA) + (size_t)b * NH * SK * DH; const u16* vb = (br ? vB : vA) + (size_t)b * NH * SK * DH;
  Hs hs;
  if (wid < 4) {
    att_wave<2>(op, kb, vb, 2 * wid, m, hg0, lb, hs, nullptr);
    const unsigned t = half ? 3 - ((wid + 2) & 3) : wid, nb = 2 * half + (t & 1);
    if (t == 0) oproj_consumer<5, 5>(wsb, nb, kq, blk);
    else if (t == 1) oproj_consumer<5, 4>(wsb, nb, kq, blk);
    else if (t == 2) oproj_consumer<4, 5>(wsb, nb, kq, blk);
    else oproj_consumer<4, 4>(wsb, nb, kq, blk);
  } else if (wid == 4) {
    if (!half) hs_entry(hs, wsb, blk);
    else {
      const __amdgpu_buffer_rsrc_t srdT = __builtin_amdgcn_make_buffer_rsrc((char*)((br ? xB : xA) + (size_t)b * SQ * DM) + m * 20736, (short)0, 20736, 0x00020000);
      __builtin_amdgcn_s_sleep(10);
#pragma unroll
      for (int p = 0; p < 3; ++p) { const unsigned n = p * 64 + lane_id(); dma4(vslot(3), (n < 162 ? n : 0) * 128, srdT); }
      {
        constexpr unsigned LN = (576 - 256) * 64 / 128;
        const __amdgpu_buffer_rsrc_t srdQ = __builtin_amdgcn_make_buffer_rsrc((char*)((blk & 1) ? wqB : wqA) + (size_t)256 * 8192 + (size_t)(blk >> 1) * LN * 128, (short)0, LN * 128, 0x00020000);
#pragma unroll
        for (unsigned p = 0; p < (LN + 63) / 64; ++p) { const unsigned n = p * 64 + lane_id(); dma4(vslot(3), (n < LN ? n : 0) * 128, srdQ); } }
    }
    att_wave<1>(op, kb, vb, 8, m, hg0, lb, hs, (br ? xB : xA) + (size_t)b * SQ * DM);
    if (half) {
      OLd ol; ol.init(op, br ? woB : woA, m, g0, 1, pair & 3);
      WAITV(0);
      ol.run(1);
    } else {
      WAITV(0);
      RBAR(); RBAR(); RBAR();
      if (lane_id() == 0) STA(hs.mine + 2, 1u);
#pragma unroll 1
      for (int j = 3; j < 8; ++j) RBAR();
      hs_post(hs.peer + 2, OBOX);
      RBAR(); RBAR();
      hs_wait_flag(hs, hs_take(OBOX), 2);
      RBAR(); RBAR(); RBAR();
      WAITV(0);
    }
    return;
  } else {
    OLd ol;
    loader(br ? wqB : wqA, (br ? xB : xA) + (size_t)b * SQ * DM, kb, vb, m, hg0, half, ol, op, br ? woB : woA, g0, pair & 3);
    ol.run(0);
    if (half) return;
  }
  oproj_finish(out + (size_t)b * SQ * (2 * DM) + br * DM + (m & 1) * 288 + 16 * ((9 * kq + 1) >> 1), wsb, wid < 4 ? wid + 4 * half : 8, kq, blk);
}

extern "C" void kernel_launch(void* const* d_in, const int* in_sizes, int n_in, void* d_out, int out_size, void* d_ws, size_t ws_size, hipStream_t stream) {
  (void)hipFuncSetAttribute((const void*)k_fused, hipFuncAttributeMaxDynamicSharedMemorySize, LDS_BYTES);
  k_fused<<<256, 768, LDS_BYTES, stream>>>((const u16*)d_in[0], (const u16*)d_in[1], (const u16*)d_in[2], (const u16*)d_in[3], (const u16*)d_in[4],
                                           (const u16*)d_in[5], (const u16*)d_in[6], (const u16*)d_in[7], (const u16*)d_in[8], (const u16*)d_in[9], (char*)d_ws, (u16*)d_out);
}
```

```cpp
#define KS0 14
#define KS1 14
#define KS2 14
#include <hip/hip_runtime.h>
#include <stdint.h>

typedef short    bf16x8 __attribute__((ext_vector_type(8)));
typedef short    s16x4  __attribute__((ext_vector_type(4)));
typedef float    f32x4  __attribute__((ext_vector_type(4)));
typedef float    f32x2  __attribute__((ext_vector_type(2)));
typedef __bf16   bf16x2 __attribute__((ext_vector_type(2)));
typedef unsigned u32x4  __attribute__((ext_vector_type(4)));
typedef unsigned u32x2  __attribute__((ext_vector_type(2)));
typedef unsigned short u16;
#define WARMPOL ""
#define NOKROT
#define OST_POL "sc1"
#define WNT false
#define XPOL ""
#define KVSTART 14
#define KVNT_ true
#define TROT(t) (t)
#define RPM_OLD 1
#define RPM_YOUNG 12
#define XAUX 16
#define TPI 0
#define TOFF 0
#define TPOL ""
#define PBF16
#define XWARM 10
#define WQW 256
#define DI __device__ __forceinline__
#define LAS __attribute__((address_space(3)))
#define MFMA16(a, b, c) __builtin_amdgcn_mfma_f32_16x16x32_bf16(a, b, c, 0, 0, 0)
#define SBAR() __builtin_amdgcn_sched_barrier(0)
#define PIN(x) asm volatile("" : "+v"(x))
#define MFMA16_V(acc, a, b) asm volatile("v_mfma_f32_16x16x32_bf16 %0, %1, %2, %0" : "+v"(acc) : "v"(a), "v"(b))
#define MFMA16_Z(d, a, b) asm volatile("v_mfma_f32_16x16x32_bf16 %0, %1, %2, 0" : "=&v"(d) : "v"(a), "v"(b))
#define MFMA16_C(d, a, b, cc) asm volatile("v_mfma_f32_16x16x32_bf16 %0, %1, %2, %3" : "=&v"(d) : "v"(a), "v"(b), "v"(cc))

constexpr int NB = 16, SQ = 144, DM = 576, NH = 32, SK = 512, DH = 128, HD = NH * DH;
constexpr int ITEM_B = 9 * 4 * 1024;
constexpr size_t WS_O = 0;
constexpr size_t WS_X = (size_t)1024 * ITEM_B, WS_F = WS_X + (size_t)256 * 4 * 46080;
constexpr float QSCALE = 0.08838834764831845f * 1.4426950408889634f;

DI unsigned rfl(unsigned x) { return __builtin_amdgcn_readfirstlane(x); }
DI unsigned lane_id() { unsigned l; asm volatile("v_mbcnt_lo_u32_b32 %0, -1, 0\n\tv_mbcnt_hi_u32_b32 %0, -1, %0" : "=v"(l)); return l; }
DI unsigned cvtpk(float lo, float hi) { f32x2 v = {lo, hi}; bf16x2 b = __builtin_convertvector(v, bf16x2); return __builtin_bit_cast(unsigned, b); }
DI bf16x8 lds128(unsigned a) { return *(const LAS bf16x8*)a; }
DI s16x4 ldstr(unsigned a) { return __builtin_bit_cast(s16x4, __builtin_amdgcn_ds_read_tr16_b64_v4i16((LAS s16x4*)a)); }
DI bf16x8 cat8(s16x4 lo, s16x4 hi) { return __builtin_shufflevector(lo, hi, 0, 1, 2, 3, 4, 5, 6, 7); }
DI void dma4(unsigned m0v, unsigned voff, __amdgpu_buffer_rsrc_t srd) {
  asm volatile("s_mov_b32 m0, %0\n\ts_nop 3\n\tbuffer_load_dword %1, %2, 0 offen " WARMPOL " lds" :: "s"(m0v), "v"(voff), "s"(srd) : "memory");
}
#define TILE8_BODY(POL) \
  "s_nop 3\n\ts_mov_b32 m0, %1\n\ts_mov_b32 %0, %2\n\t" \
  "buffer_load_dwordx4 %3, %5, %0 offen " POL " lds\n\ts_add_u32 m0, m0, 896\n\ts_nop 0\n\tbuffer_load_dwordx4 %3, %5, %0 offen offset:128 " POL " lds\n\ts_add_u32 m0, m0, 1152\n\ts_add_u32 %0, %0, %6\n\t" \
  "buffer_load_dwordx4 %4, %5, %0 offen " POL " lds\n\ts_add_u32 m0, m0, 896\n\ts_nop 0\n\tbuffer_load_dwordx4 %4, %5, %0 offen offset:128 " POL " lds\n\ts_add_u32 m0, m0, 1152\n\ts_add_u32 %0, %0, %6\n\t" \
  "buffer_load_dwordx4 %3, %5, %0 offen " POL " lds\n\ts_add_u32 m0, m0, 896\n\ts_nop 0\n\tbuffer_load_dwordx4 %3, %5, %0 offen offset:128 " POL " lds\n\ts_add_u32 m0, m0, 1152\n\ts_add_u32 %0, %0, %6\n\t" \
  "buffer_load_dwordx4 %4, %5, %0 offen " POL " lds\n\ts_add_u32 m0, m0, 896\n\ts_nop 0\n\tbuffer_load_dwordx4 %4, %5, %0 offen offset:128 " POL " lds"
template <bool NT, int D2> DI void dma_tile8(unsigned lo, unsigned so, unsigned va, unsigned vb, __amdgpu_buffer_rsrc_t srd) {
  unsigned t;
  if constexpr (NT) asm volatile(TILE8_BODY("nt") : "=&s"(t) : "s"(lo), "s"(so), "v"(va), "v"(vb), "s"(srd), "i"(D2) : "memory", "scc");
  else              asm volatile(TILE8_BODY("")   : "=&s"(t) : "s"(lo), "s"(so), "v"(va), "v"(vb), "s"(srd), "i"(D2) : "memory", "scc");
}
template <int D2> DI void dma_tile9(unsigned lo, unsigned so, unsigned va, __amdgpu_buffer_rsrc_t srd) {
  unsigned t;
  asm volatile("s_nop 3\n\ts_mov_b32 m0, %1\n\ts_mov_b32 %0, %2\n\t.rept 9\n\t"
               "buffer_load_dwordx4 %3, %4, %0 offen " XPOL " lds\n\ts_add_u32 m0, m0, 2048\n\ts_add_u32 %0, %0, %5\n\t.endr"
               : "=&s"(t) : "s"(lo), "s"(so), "v"(va), "s"(srd), "i"(D2) : "memory", "scc");
}
#define WAITV(n) asm volatile("s_waitcnt vmcnt(" #n ")" ::: "memory")
#define BAR() do { asm volatile("" ::: "memory"); __builtin_amdgcn_s_waitcnt(0xC07F); asm volatile("s_barrier" ::: "memory"); } while (0)
#define RBAR() asm volatile("s_barrier" ::: "memory")
constexpr unsigned GRP = 65536, WRING = 3 * 8192, XSLOT = 18432, XB = 126976, XA = XB + XSLOT, LDS_BYTES = 163840;
constexpr unsigned kslot(int i) { return 8192 * ((i & 3) == 0 ? 0 : (i & 3) == 1 ? 1 : (i & 3) == 2 ? 5 : 3); }
constexpr unsigned vslot(int i) { return 8192 * ((i & 3) == 0 ? 2 : (i & 3) == 1 ? 6 : (i & 3) == 2 ? 4 : 7); }
static_assert(XA + XSLOT == LDS_BYTES, "LDS map");

#define STAMP2(k)
#define STAMP(k)
#define STAMPW(k)

DI float red_g_max(float m) {
  auto r = __builtin_amdgcn_permlane32_swap(__float_as_uint(m), __float_as_uint(m), false, false);
  m = fmaxf(__uint_as_float(r[0]), __uint_as_float(r[1]));
  auto s = __builtin_amdgcn_permlane16_swap(__float_as_uint(m), __float_as_uint(m), false, false);
  return fmaxf(__uint_as_float(s[0]), __uint_as_float(s[1]));
}
DI float red_g_sum(float m) {
  auto r = __builtin_amdgcn_permlane32_swap(__float_as_uint(m), __float_as_uint(m), false, false);
  m = __uint_as_float(r[0]) + __uint_as_float(r[1]);
  auto s = __builtin_amdgcn_permlane16_swap(__float_as_uint(m), __float_as_uint(m), false, false);
  return __uint_as_float(s[0]) + __uint_as_float(s[1]);
}

template <int NQ> struct AttC {
  f32x4 O[NQ][8]; bf16x8 Q[NQ][4]; f32x4 S[2][NQ][2]; f32x4 negm[NQ]; float l[NQ];
  unsigned kb, vb0, vb1;
};
template <int NQ> struct Tab;
template <> struct Tab<2> {
  static constexpr int NPRE = 6;
  static constexpr int pre[6] = {100, 101, 102, 103, 104, 105};
  static constexpr int gx[16][2] = {{106, 0}, {107, 0}, {200, 201}, {110, 0}, {202, 203}, {111, 0}, {112, 0}, {113, 0}, {114, 0}, {210, 211}, {115, 0}, {116, 0}, {117, 0}, {300, 310}, {212, 213}, {301, 311}};
  static constexpr int gy[16] = {302, 312, 303, 313, 304, 314, 305, 315, 306, 316, 307, 317, 0, 0, 0, 0};
  static constexpr int all[24] = {100, 101, 102, 103, 104, 105, 106, 107, 110, 200, 201, 202, 203, 111, 112, 113, 114, 115, 116, 117, 210, 211, 212, 213};
  static constexpr int ady[16] = {300, 310, 301, 311, 302, 312, 303, 313, 304, 314, 305, 315, 306, 316, 307, 317};
};
template <> struct Tab<1> {
  static constexpr int NPRE = 4;
  static constexpr int pre[4] = {100, 101, 102, 103};
  static constexpr int gx[8][2] = {{104, 0}, {105, 0}, {200, 201}, {106, 0}, {107, 0}, {300, 301}, {202, 203}, {302, 303}};
  static constexpr int gy[8] = {304, 305, 306, 307, 0, 0, 0, 0};
  static constexpr int all[12] = {100, 101, 102, 103, 104, 105, 106, 107, 200, 201, 202, 203};
  static constexpr int ady[8] = {300, 301, 302, 303, 304, 305, 306, 307};
};
template <int NQ, int P, bool QK> struct Step {
  static constexpr int X = P & 1;
  static constexpr unsigned ks_ = kslot(P + 1), vs_ = vslot(P);
  bf16x8 fr[4]; float e[NQ][8]; unsigned pw[NQ][4]; u32x4 Pw[NQ];
  template <int OP> DI void op(AttC<NQ>& c) {
    constexpr int ty = OP / 100, qt = (OP / 10) % 10, j = OP % 10;
    if constexpr (ty == 1) { e[qt][j] = c.S[X][qt][j >> 2][j & 3]; asm volatile("v_exp_f32 %0, %0" : "+v"(e[qt][j])); }
    if constexpr (ty == 2) asm volatile("v_cvt_pk_bf16_f32 %0, %1, %2" : "=v"(pw[qt][j]) : "v"(e[qt][2 * j]), "v"(e[qt][2 * j + 1]));
    if constexpr (ty == 3) asm volatile("v_add_f32 %0, %0, %1" : "+v"(c.l[qt]) : "v"(e[qt][j]));
  }
  template <int R> DI void rd(AttC<NQ>& c) {
    if constexpr (R < 8) fr[R & 3] = lds128(c.kb + ks_ + 4096 * (R & 1) + 512 * (R >> 1));
    else if constexpr (R < 16) {
      constexpr int dt = R - 8;
      const unsigned vb = ((dt & 1) ? c.vb1 : c.vb0) + vs_ + 512 * (dt >> 1);
      fr[R & 3] = cat8(ldstr(vb), ldstr(vb + 4096));
    }
    SBAR();
  }
  template <int I> DI void gx(AttC<NQ>& c) {
    constexpr int K = I / NQ, qt = I % NQ, s = K >> 1, kt = K & 1;
    if constexpr (s == 0) MFMA16_C(c.S[X ^ 1][qt][kt], fr[K & 3], c.Q[qt][s], c.negm[qt]); else MFMA16_V(c.S[X ^ 1][qt][kt], fr[K & 3], c.Q[qt][s]);
    if constexpr (qt == NQ - 1) { SBAR(); rd<K + 4>(c); }
    op<Tab<NQ>::gx[I][0]>(c); op<Tab<NQ>::gx[I][1]>(c);
    if constexpr (I + 1 < 8 * NQ) gx<I + 1>(c);
  }
  template <int I> DI void gy(AttC<NQ>& c) {
    constexpr int Dt = I / NQ, qt = I % NQ;
    MFMA16_V(c.O[qt][Dt], fr[Dt & 3], Pw[qt]);
    if constexpr (qt == NQ - 1) { SBAR(); rd<Dt + 12>(c); }
    op<(QK ? Tab<NQ>::gy[I] : Tab<NQ>::ady[I])>(c);
    if constexpr (I + 1 < 8 * NQ) gy<I + 1>(c);
  }
  template <int I> DI void pre(AttC<NQ>& c) {
    op<(QK ? Tab<NQ>::pre[I < Tab<NQ>::NPRE ? I : 0] : Tab<NQ>::all[I])>(c);
    if constexpr (I + 1 < (QK ? Tab<NQ>::NPRE : 12 * NQ)) pre<I + 1>(c);
  }
  DI void run(AttC<NQ>& c) {
    if constexpr (QK) {
      rd<0>(c); rd<1>(c); rd<2>(c); rd<3>(c);
      pre<0>(c);
      gx<0>(c);
    } else {
      rd<8>(c); rd<9>(c); rd<10>(c); rd<11>(c);
      pre<0>(c);
      asm volatile("s_nop 1");
    }
#pragma unroll
    for (int qt = 0; qt < NQ; ++qt) Pw[qt] = u32x4{pw[qt][0], pw[qt][1], pw[qt][2], pw[qt][3]};
    gy<0>(c);
  }
};

template <int NQ, int P, bool QK> DI void att_step(AttC<NQ>& c) { BAR(); SBAR();
  Step<NQ, P, QK> s; s.run(c); SBAR(); }

template <int NQ> DI void att_prologue(AttC<NQ>& c) {
#pragma unroll
  for (int s = 0; s < 4; ++s)
#pragma unroll
    for (int kt = 0; kt < 2; ++kt) {
      const bf16x8 kf = lds128(c.kb + 4096 * kt + 512 * s);
#pragma unroll
      for (int qt = 0; qt < NQ; ++qt) c.S[0][qt][kt] = MFMA16(kf, c.Q[qt][s], s == 0 ? (f32x4{0, 0, 0, 0}) : c.S[0][qt][kt]);
    }
#pragma unroll
  for (int qt = 0; qt < NQ; ++qt) {
    float mx = c.S[0][qt][0][0];
#pragma unroll
    for (int j = 1; j < 8; ++j) mx = fmaxf(mx, c.S[0][qt][j >> 2][j & 3]);
    mx = red_g_max(mx);
    c.negm[qt] = f32x4{-mx, -mx, -mx, -mx};
    c.S[0][qt][0] += c.negm[qt]; c.S[0][qt][1] += c.negm[qt];
    c.l[qt] = 0.f;
#pragma unroll
    for (int dt = 0; dt < 8; ++dt) c.O[qt][dt] = f32x4{0, 0, 0, 0};
  }
}
template <int NQ> DI void att_redo_true_max(AttC<NQ>& c, const u16* __restrict__ kh, const u16* __restrict__ vh) {
  const unsigned lane = lane_id(), li = lane & 15, g = lane >> 4;
  const u16* kp = kh + li * DH + g * 8;
  float mx[NQ];
#pragma unroll
  for (int qt = 0; qt < NQ; ++qt) mx[qt] = -__builtin_inff();
#pragma unroll 1
  for (int kt = 0; kt < SK / 16; ++kt) {
    f32x4 s4[NQ];
#pragma unroll
    for (int qt = 0; qt < NQ; ++qt) s4[qt] = f32x4{0, 0, 0, 0};
#pragma unroll
    for (int s = 0; s < 4; ++s) {
      const bf16x8 kf = *(const bf16x8*)(kp + kt * 16 * DH + s * 32);
#pragma unroll
      for (int qt = 0; qt < NQ; ++qt) s4[qt] = MFMA16(kf, c.Q[qt][s], s4[qt]);
    }
#pragma unroll
    for (int qt = 0; qt < NQ; ++qt) mx[qt] = fmaxf(fmaxf(fmaxf(mx[qt], s4[qt][0]), fmaxf(s4[qt][1], s4[qt][2])), s4[qt][3]);
  }
#pragma unroll
  for (int qt = 0; qt < NQ; ++qt) {
    mx[qt] = red_g_max(mx[qt]); c.l[qt] = 0.f;
#pragma unroll
    for (int dt = 0; dt < 8; ++dt) c.O[qt][dt] = f32x4{0, 0, 0, 0};
  }
#pragma unroll 1
  for (int st = 0; st < SK / 32; ++st) {
    u32x4 pw[NQ];
#pragma unroll
    for (int kt = 0; kt < 2; ++kt) {
      f32x4 s4[NQ];
#pragma unroll
      for (int qt = 0; qt < NQ; ++qt) s4[qt] = f32x4{-mx[qt], -mx[qt], -mx[qt], -mx[qt]};
#pragma unroll
      for (int s = 0; s < 4; ++s) {
        const bf16x8 kf = *(const bf16x8*)(kp + (2 * st + kt) * 16 * DH + s * 32);
#pragma unroll
        for (int qt = 0; qt < NQ; ++qt) s4[qt] = MFMA16(kf, c.Q[qt][s], s4[qt]);
      }
#pragma unroll
      for (int qt = 0; qt < NQ; ++qt) {
        float e[4];
#pragma unroll
        for (int j = 0; j < 4; ++j) { e[j] = __builtin_amdgcn_exp2f(s4[qt][j]); c.l[qt] += e[j]; }
        pw[qt][2 * kt] = cvtpk(e[0], e[1]); pw[qt][2 * kt + 1] = cvtpk(e[2], e[3]);
      }
    }
    const u16* vp = vh + (32 * st + 4 * g) * DH + li;
#pragma unroll
    for (int dt = 0; dt < 8; ++dt) {
      bf16x8 vf;
#pragma unroll
      for (int j = 0; j < 8; ++j) vf[j] = (short)vp[(16 * (j >> 2) + (j & 3)) * DH + 16 * dt];
#pragma unroll
      for (int qt = 0; qt < NQ; ++qt) c.O[qt][dt] = MFMA16(vf, __builtin_bit_cast(bf16x8, pw[qt]), c.O[qt][dt]);
      SBAR();
    }
  }
#pragma unroll
  for (int qt = 0; qt < NQ; ++qt) c.l[qt] = red_g_sum(c.l[qt]);
}
template <int NQ> DI void att_epilogue(AttC<NQ>& c, char* ob, const u16* kh, const u16* vh) {
  bool bad = false;
#pragma unroll
  for (int qt = 0; qt < NQ; ++qt) { c.l[qt] = red_g_sum(c.l[qt]); bad |= !(c.l[qt] < 16777216.0f); }
  if (__builtin_expect(__any(bad), 0)) att_redo_true_max<NQ>(c, kh, vh);
#pragma unroll
  for (int qt = 0; qt < NQ; ++qt) {
    const float inv = __builtin_amdgcn_rcpf(c.l[qt]);
#pragma unroll
    for (int s = 0; s < 4; ++s) {
      const f32x4 a = c.O[qt][2 * s] * inv, bb = c.O[qt][2 * s + 1] * inv;
      u32x4 w = {cvtpk(a[0], a[1]), cvtpk(a[2], a[3]), cvtpk(bb[0], bb[1]), cvtpk(bb[2], bb[3])};
      asm volatile("global_store_dwordx4 %0, %1, off " OST_POL "\n\ts_nop 1" :: "v"(ob + (qt * 4 + s) * 1024), "v"(w) : "memory");
    }
  }
}

DI bf16x8 q_frag(const f32x4& t0, const f32x4& t1) {
  const f32x4 u0 = t0 * QSCALE, u1 = t1 * QSCALE;
  unsigned a0 = cvtpk(u0[0], u0[1]), a1 = cvtpk(u0[2], u0[3]), b0 = cvtpk(u1[0], u1[1]), b1 = cvtpk(u1[2], u1[3]);
  auto r0 = __builtin_amdgcn_permlane32_swap(a0, b0, false, false);
  auto r1 = __builtin_amdgcn_permlane32_swap(a1, b1, false, false);
  auto s0 = __builtin_amdgcn_permlane16_swap(r0[0], r0[1], false, false);
  auto s1 = __builtin_amdgcn_permlane16_swap(r1[0], r1[1], false, false);
  u32x4 w = {s0[0], s1[0], s0[1], s1[1]};
  return __builtin_bit_cast(bf16x8, w);
}
template <int NR, int WS, int XO, bool Z = false>
DI void q_unit(f32x4 (&qa)[NR][4], const bf16x8 (&wc)[4], bf16x8 (&wn)[4], bf16x8 (&xc)[NR], const bf16x8& lc, bf16x8 (&xn)[NR], bf16x8& ln, unsigned w0, unsigned w1, const unsigned (&xa)[3]) {
#define XADDR(row) (xa[(row) >> 1] + ((row) & 1) * 2048 + XO)
#pragma unroll
  for (int q = 0; q < NR; ++q) {
#pragma unroll
    for (int d = 0; d < 4; ++d) {
      if constexpr (Z) MFMA16_Z(qa[q][d], wc[d], q < NR - 1 ? xc[q] : lc); else MFMA16_V(qa[q][d], wc[d], q < NR - 1 ? xc[q] : lc);
      SBAR();
      const int i = 4 * q + d;
      if (WS >= 0 && i < 8 && !(i & 1)) { const int u = i >> 1; const unsigned a = ((u & 1) ? w1 : w0) + 8192 * WS + 512 * (u >> 1); wn[u] = cat8(ldstr(a), ldstr(a + 256)); }
      if (XO >= 0 && (i & 1) && (i >> 1) < NR) xn[i >> 1] = lds128(XADDR(i >> 1));
      if (XO >= 0 && i == 10) ln = lds128(XADDR(NR - 1) + 512);
      SBAR();
    }
    if (XO >= 0 && q < NR - 1) { xc[q] = lds128(XADDR(q) + 512); SBAR(); }
  }
#undef XADDR
}

#define LDA(p) __hip_atomic_load((p), __ATOMIC_RELAXED, __HIP_MEMORY_SCOPE_AGENT)
#define STA(p, v) __hip_atomic_store((p), (v), __ATOMIC_RELAXED, __HIP_MEMORY_SCOPE_AGENT)
constexpr int SPIN_CAP = 1 << 21;
struct Hs { unsigned* mine; unsigned* peer; unsigned me, x, e; bool ok; };
DI void hs_post(const unsigned* p, unsigned box) { asm volatile("s_mov_b32 m0, %0\n\ts_nop 3\n\tglobal_load_lds_dword %1, off sc1" :: "s"(box), "v"(p) : "memory"); }
DI unsigned hs_take(unsigned box) { WAITV(0); return *(volatile LAS unsigned*)(box + 4 * lane_id()); }
DI void hs_entry(Hs& h, char* wsb, unsigned blk) {
  const unsigned lane = lane_id();
  h.me = (blk >> 3) & 7;
  h.mine = (unsigned*)(wsb + WS_F) + blk * 64;
  h.peer = (unsigned*)(wsb + WS_F) + ((blk & ~56u) + 8 * (lane & 7)) * 64;
  if (lane >= 1 && lane <= 3) STA(h.mine + lane, 0u);
  const unsigned y0 = LDA(h.peer + 32 + h.me);
  unsigned x = rfl(y0) + 1;
  while (__any(y0 == x)) ++x;
  h.x = x; h.e = 0; h.ok = false;
  WAITV(0);
  if (lane == 0) STA(h.mine, x);
}
DI void hs_wait_flag(const Hs& h, unsigned v, int word) {
  for (int spin = 0; !__all(v == 1u) && spin < SPIN_CAP; ++spin) { __builtin_amdgcn_s_sleep(2); v = LDA(h.peer + word); }
  __builtin_amdgcn_fence(__ATOMIC_ACQUIRE, "agent");
}
constexpr unsigned HBOX = 161792;
DI void hs_hook(Hs& h, int k) {
  const unsigned lane = lane_id();
  if (k == 0) hs_post(h.peer, HBOX);
  else if (k == 1) { h.e = hs_take(HBOX); if (lane < 8) STA(h.mine + 32 + lane, h.e); }
  else if (k == 4) hs_post(h.peer + 32 + h.me, HBOX);
  else if (k == 5) { h.ok = __all(hs_take(HBOX) == h.x); hs_post(h.peer, HBOX); }
  else if (k == 6) { const unsigned e = hs_take(HBOX); if (lane < 8 && e != h.e) STA(h.mine + 32 + lane, e); hs_post(h.peer + 1, HBOX); }
  else if (k == 7) {
    unsigned f = hs_take(HBOX);
    if (!h.ok) {
      for (int spin = 0; spin < SPIN_CAP; ++spin) {
        const unsigned e = LDA(h.peer);
        if (lane < 8) STA(h.mine + 32 + lane, e);
        if (__all(LDA(h.peer + 32 + h.me) == h.x)) break;
        __builtin_amdgcn_s_sleep(2);
      }
      WAITV(0);
      const unsigned e = LDA(h.peer);
      if (lane < 8) STA(h.mine + 32 + lane, e);
      f = 0;
    }
    hs_wait_flag(h, f, 1);
  }
}

constexpr unsigned EX = 122880;
template <int NR> DI void q_phase(bf16x8 (&own)[2][2], unsigned wid, unsigned lb) {
  asm volatile("; q phase %0" :: "i"(NR) : "memory");
  const unsigned lane = lane_id(), li = lane & 15, g = lane >> 4, pos = lane & 3, tq = li >> 2;
  unsigned w0 = lb + WRING + 1024 * (wid & 1) + 2048 * g + 64 * tq + 32 * (g & 1) + 8 * pos, w1 = w0 ^ 32u;
  const unsigned xl = XB + 1024 * (li >> 3) + 64 * (li & 7) + 16 * (g ^ (2 * (li >> 3)));
  unsigned xa[3] = {xl + wid * 4096, xl + (wid ^ 1) * 4096, xl + 16384};
  bf16x8 w[4], v[4], xA[NR], xB[NR], l0, l1;
  f32x4 qa[NR][4];
  PIN(w0); PIN(w1); PIN(xa[0]); PIN(xa[1]); if (NR == 5) PIN(xa[2]);
  BAR();
#pragma unroll
  for (int u = 0; u < 4; ++u) { const unsigned a = ((u & 1) ? w1 : w0) + 512 * (u >> 1); w[u] = cat8(ldstr(a), ldstr(a + 256)); }
#pragma unroll
  for (int q = 0; q < NR; ++q) { const unsigned a = xa[q >> 1] + (q & 1) * 2048 + XSLOT; xB[q] = lds128(a); if (q < NR - 1) xA[q] = lds128(a + 512); else l0 = lds128(a + 512); }
  BAR(); q_unit<NR, 1, -1, true>(qa, w, v, xB, xB[NR - 1], xA, l1, w0, w1, xa);
#pragma unroll 1
  for (int n = 0; n < 4; ++n) {
    WAITV(0);
    BAR(); q_unit<NR, 2, 0>(qa, v, w, xA, l0, xB, l1, w0, w1, xa);
    BAR(); q_unit<NR, 3, -1>(qa, w, v, xB, xB[NR - 1], xA, l0, w0, w1, xa);
    BAR(); q_unit<NR, 0, XSLOT>(qa, v, w, xA, l1, xB, l0, w0, w1, xa);
    BAR(); q_unit<NR, 1, -1>(qa, w, v, xB, xB[NR - 1], xA, l1, w0, w1, xa);
  }
  BAR(); q_unit<NR, -1, -1>(qa, v, w, xA, l0, xB, l1, w0, w1, xa);
  asm volatile("s_nop 7" ::: "memory"); SBAR();
  unsigned ea = EX + (lb ? 0 : 20480) + lane * 16; PIN(ea);
#pragma unroll
  for (int s = 0; s < 2; ++s) {
    *(LAS bf16x8*)(ea + wid * 4096 + s * 1024) = q_frag(qa[2][2 * s], qa[2][2 * s + 1]);
    *(LAS bf16x8*)(ea + wid * 4096 + 2048 + s * 1024) = q_frag(qa[3][2 * s], qa[3][2 * s + 1]);
    if constexpr (NR == 5) *(LAS bf16x8*)(ea + 16384 + wid * 2048 + s * 1024) = q_frag(qa[4][2 * s], qa[4][2 * s + 1]);
  }
#pragma unroll
  for (int q = 0; q < 2; ++q)
#pragma unroll
    for (int s = 0; s < 2; ++s) own[q][s] = q_frag(qa[q][2 * s], qa[q][2 * s + 1]);
}

template <int NQ> DI void att_wave(char* ows_pair, const u16* kbase, const u16* vbase, unsigned qt0, unsigned m, unsigned hg0, unsigned lb, Hs& hs, const u16* x) {
  asm volatile("; attention wave NQ=%0" :: "i"(NQ) : "memory");
  STAMP(20);
#pragma unroll 1
  for (int r = 0; r < 2; ++r) {
    AttC<NQ> c;
    STAMP(4 * r);
    if constexpr (NQ == 2) {
      bf16x8 own[2][2];
      if (qt0 < 4) q_phase<5>(own, qt0 >> 1, lb); else q_phase<4>(own, qt0 >> 1, lb);
      unsigned ea = EX + (lb ? 0 : 20480) + (qt0 ^ 2) * 2048 + lane_id() * 16; PIN(ea);
      BAR();
      const bool hi = qt0 & 2;
#pragma unroll
      for (int q = 0; q < 2; ++q)
#pragma unroll
        for (int s = 0; s < 2; ++s) { const bf16x8 rc = lds128(ea + q * 2048 + s * 1024); c.Q[q][s] = hi ? rc : own[q][s]; c.Q[q][2 + s] = hi ? own[q][s] : rc; }
    } else {
      const __amdgpu_buffer_rsrc_t srdX = __builtin_amdgcn_make_buffer_rsrc((void*)x, (short)0, SQ * DM * 2, 0x00020000);
      const unsigned lane = lane_id(), half = lb != 0, vx = ((((lane >> 2) & 7) + 8 * half) * (DM * 2) + (lane >> 5) * 64 + (lane & 3) * 16) ^ (32u * half);
      BAR(); if (r == 0) STAMP(21);
#pragma unroll 1
      for (int n = 1; n < 19; n += 2) {
        BAR();
        if (n < 14) { dma_tile9<16 * DM * 2>(((n & 2) ? XB : XA) + half * 1024, ((n + 3) >> 1) * 128, vx, srdX); WAITV(9); } else WAITV(0);
        BAR(); if (r == 0 && n == 5) STAMP(22); if (r == 0 && n == 13) STAMP(23);
        if (n == 1 && lb == 0 && r == 1 && lane == 0) STA(hs.mine + 1, 1u);
      }
      BAR();
      unsigned ea = EX + (lb ? 0 : 20480) + 16384 + lane_id() * 16; PIN(ea);
#pragma unroll
      for (int s = 0; s < 4; ++s) c.Q[0][s] = lds128(ea + s * 1024);
      __builtin_amdgcn_s_waitcnt(0xC07F);
    }
    { const unsigned lane = lane_id(), li = lane & 15, g = lane >> 4;
      c.kb = lb + 2048 * (li >> 3) + 64 * (li & 7) + 16 * (g ^ (2 * (li >> 3)));
      c.vb0 = lb + 2048 * (g >> 1) + 64 * (4 * (g & 1) + (li >> 2)) + 32 * (g & 1) + 8 * (lane & 3); c.vb1 = c.vb0 ^ 32u;
      PIN(c.kb); PIN(c.vb0); PIN(c.vb1); }
    STAMP(4 * r + 1);
    if (NQ == 1 && lb == 0) hs_hook(hs, 4 * r);
    att_prologue<NQ>(c);
    SBAR();
#pragma unroll 1
    for (int ii = 0; ii < 3; ++ii) { att_step<NQ, 0, true>(c); att_step<NQ, 1, true>(c); att_step<NQ, 2, true>(c); att_step<NQ, 3, true>(c);
      if (NQ == 1 && lb == 0) hs_hook(hs, 4 * r + 1 + ii); }
    att_step<NQ, 0, true>(c); att_step<NQ, 1, true>(c);
    if (NQ == 1) WAITV(0);
    att_step<NQ, 2, true>(c); att_step<NQ, 3, false>(c);
    STAMP(4 * r + 2);
    asm volatile("s_nop 7\n\ts_nop 1" ::: "memory"); SBAR();
    const unsigned h = 8 * ((hg0 + 2 * r) & 3) + m;
    att_epilogue<NQ>(c, ows_pair + (size_t)h * ITEM_B + qt0 * 4096 + lane_id() * 16, kbase + (size_t)h * SK * DH, vbase + (size_t)h * SK * DH);
    STAMP(4 * r + 3);
  }
}

constexpr unsigned STEP_TICKS = 106;
DI void wait_newer(int n) {
  if (n >= 32) { if (n >= 40) WAITV(40); else WAITV(32); }
  else if (n >= 16) { if (n >= 24) WAITV(24); else WAITV(16); }
  else if (n >= 8) WAITV(8); else WAITV(0);
}
DI unsigned oslot_o(unsigned s) { return s == 0 ? 131072 : GRP - 9216 + 9216 * s; }
DI unsigned oslot_w(unsigned s) { return s == 0 ? 140288 : 18432 * (s - 1); }
constexpr unsigned OPART = 93184;
constexpr unsigned OBOX = 55296;
struct OLd {
  __amdgpu_buffer_rsrc_t srd; unsigned vo[9], h0, isw, rot;
  DI void init(const char* op, const u16* wo, unsigned m, unsigned g0, unsigned isw_, unsigned rot_) {
    const unsigned lane = lane_id(); h0 = 8 * g0 + (m & 4); isw = isw_; rot = rot_;
    srd = isw ? __builtin_amdgcn_make_buffer_rsrc((void*)wo, (short)0, HD * DM * 2, 0x00020000) : __builtin_amdgcn_make_buffer_rsrc((void*)op, (short)0, NH * ITEM_B, 0x00020000);
#pragma unroll
    for (int p = 0; p < 9; ++p) { const unsigned n = 64 * p + lane, row = n / 36, ch = n - 36 * row;
      vo[p] = isw ? row * (DM * 2) + ((ch ^ ((row >> 1) & 2)) * 16) + (m & 1) * 576 : p * 4096 + lane * 16; }
  }
#define OPIECE(k) "buffer_load_dwordx4 %" #k ", %1, %2 offen lds\n\ts_add_u32 m0, m0, 1024\n\ts_nop 0\n\t"
  DI void issue(int j, unsigned part) const {
    const unsigned h = ((h0 + (j & 16)) & 31) + (((j >> 2) + rot) & 3), s = j & 3;
    const unsigned so = isw ? ((h * 4 + s) * 32 + 16 * part) * (DM * 2) : h * ITEM_B + s * 1024, lo = isw ? oslot_w(j & 3) + part * 9216 : oslot_o(j & 3);
    asm volatile("s_mov_b32 m0, %0\n\ts_nop 3\n\t" OPIECE(3) OPIECE(4) OPIECE(5) OPIECE(6) OPIECE(7) OPIECE(8) OPIECE(9) OPIECE(10) OPIECE(11)
                 :: "s"(lo), "s"(srd), "s"(so), "v"(vo[0]), "v"(vo[1]), "v"(vo[2]), "v"(vo[3]), "v"(vo[4]), "v"(vo[5]), "v"(vo[6]), "v"(vo[7]), "v"(vo[8]) : "memory", "scc");
  }
  DI void run(unsigned part) const {
#pragma unroll 1
    for (int j = 0; j < 32; ++j) {
      if (j == 0 || j == 30) WAITV(9); else if (j < 30) WAITV(18); else WAITV(0);
      RBAR();
      if (j == 0) issue(2, part);
      if (j + 3 < 32) issue(j + 3, part);
    }
  }
};

DI void loader(const u16* wq, const u16* x, const u16* kbase, const u16* vbase, unsigned m, unsigned hg0, unsigned half, OLd& ol, const char* op, const u16* wo, unsigned g0, unsigned rot) {
  const unsigned lane = lane_id(), row7 = (lane >> 2) & 7, sh = lane >> 5, pos = lane & 3, lb = half * GRP;
  const __amdgpu_buffer_rsrc_t srdW = __builtin_amdgcn_make_buffer_rsrc((void*)wq, (short)0, DM * HD * 2, 0x00020000);
  const __amdgpu_buffer_rsrc_t srdX = __builtin_amdgcn_make_buffer_rsrc((void*)x, (short)0, SQ * DM * 2, 0x00020000);
  const __amdgpu_buffer_rsrc_t srdK = __builtin_amdgcn_make_buffer_rsrc((void*)kbase, (short)0, NH * SK * DH * 2, 0x00020000);
  const __amdgpu_buffer_rsrc_t srdV = __builtin_amdgcn_make_buffer_rsrc((void*)vbase, (short)0, NH * SK * DH * 2, 0x00020000);
  const unsigned vw0 = row7 * (HD * 2) + sh * 64 + pos * 16, vw1 = vw0 ^ 32u;
  const unsigned vx = (row7 * (DM * 2) + sh * 64 + pos * 16) ^ (32u * half);
  const unsigned vk0 = row7 * 256 + sh * 64 + pos * 16, vk1 = vk0 ^ 32u, vv = row7 * 256 + sh * 64 + ((pos ^ (2 * (row7 >> 2))) * 16);
  int cnt = 0;
  auto issue_w = [&](unsigned h, int s) {
    dma_tile8<WNT, 8 * HD * 2>(lb + WRING + (s & 3) * 8192, s * 32 * (HD * 2) + h * (DH * 2), vw0, vw1, srdW);
    return cnt += 8;
  };
  auto issue_x = [&](int e) {
    dma_tile9<16 * DM * 2>(((e & 1) ? XB : XA) + half * 1024, e * 128 + half * 8 * (DM * 2), vx, srdX);
    return cnt += 9;
  };
  auto issue_k = [&](unsigned h, int t) {
    const unsigned q = t & 3, lo = q == 0 ? kslot(0) : q == 1 ? kslot(1) : q == 2 ? kslot(2) : kslot(3);
    dma_tile8<KVNT_, 2048>(lb + lo, h * (SK * DH * 2) + TROT(t) * 8192, vk0, vk1, srdK);
    return cnt += 8;
  };
  auto issue_v = [&](unsigned h, int t) {
    const unsigned q = t & 3, lo = q == 0 ? vslot(0) : q == 1 ? vslot(1) : q == 2 ? vslot(2) : vslot(3);
    dma_tile8<KVNT_, 2048>(lb + lo, h * (SK * DH * 2) + TROT(t) * 8192, vv, vv, srdV);
    return cnt += 8;
  };
  unsigned h = 8 * hg0 + m;
  unsigned tnext = 0;
  int kx = 0;
  int n0, n1, n2, n3;
  issue_w(h, 0); n0 = issue_x(0); n1 = issue_w(h, 1); issue_w(h, 2);
  ol.init(op, wo, m, g0, half ^ 1, rot);
#pragma unroll 1
  for (int r = 0; r < 2; ++r) {
    const unsigned hn = 8 * ((hg0 + 2) & 3) + m;
    wait_newer(cnt - n0); RBAR();
    n2 = issue_x(1);
    if (r == 0) issue_w(h, 3);
    n3 = cnt;
#pragma unroll 1
    for (int G = 1; G < 15; ++G) {
      wait_newer(cnt - n1); RBAR();
      n1 = n2; n2 = n3;
      issue_w(h, G + 3);
      n3 = cnt;
      if (r == 0) { if (G == KS0) kx = issue_k(h, 0); if (G == KS1) issue_k(h, 1); if (G == KS2) issue_v(h, 0); }
    }
    int a0 = cnt, a1, a2;
    wait_newer(cnt - n1); RBAR();
    issue_k(h, 2);
    wait_newer(cnt - n2); RBAR();
    a1 = issue_v(h, 1);
    wait_newer(cnt - n3); RBAR();
    issue_k(h, 3);
    wait_newer(cnt - kx); RBAR();
    a2 = issue_v(h, 2);
    RBAR();
#pragma unroll 1
    for (int i = 0; i < 16; ++i) {
      wait_newer(cnt - a0);
      {
        unsigned now = (unsigned)__builtin_amdgcn_s_memrealtime();
        while (i > 0 && i < 14 && (int)(tnext - now) > 0) { __builtin_amdgcn_s_sleep(1); now = (unsigned)__builtin_amdgcn_s_memrealtime(); }
        tnext = now + STEP_TICKS; }
      RBAR();
      a0 = a1; a1 = a2;
      if (i < 12) issue_k(h, i + 4);
      if (i < 13) issue_v(h, i + 3);
      a2 = cnt;
      if (r == 0) {
        if (i == 12) issue_k(hn, 0);
        if (i == 13) { issue_k(hn, 1); issue_v(hn, 0); }
        if (i == 14) { issue_x(0); issue_w(hn, 2); issue_w(hn, 3); }
        if (i == 15) { n0 = issue_w(hn, 0); n1 = issue_w(hn, 1); }
      } else {
        if (i == 13 && ol.isw) { ol.issue(0, 0); ol.issue(0, 1); ol.issue(1, 0); ol.issue(1, 1); cnt += 36; }
        if (i == 14 && !ol.isw) { ol.issue(0, 0); ol.issue(1, 0); cnt += 18; }
      }
    }
    h = hn;
  }
}

template <int NQT, int NNT> struct OSet { bf16x8 w[NNT], ol; };
template <int NQT, int NNT, bool RD, bool Z> DI void o_unit(f32x4 (&acc)[NQT][NNT], bf16x8 (&of)[NQT - 1], const OSet<NQT, NNT>& cs, OSet<NQT, NNT>& ns, unsigned oa, unsigned wa0, unsigned wa1) {
#pragma unroll
  for (int q = 0; q < NQT; ++q) {
#pragma unroll
    for (int n = 0; n < NNT; ++n) {
      if constexpr (Z) MFMA16_Z(acc[q][n], cs.w[n], q < NQT - 1 ? of[q < NQT - 1 ? q : 0] : cs.ol); else MFMA16_V(acc[q][n], cs.w[n], q < NQT - 1 ? of[q < NQT - 1 ? q : 0] : cs.ol);
      SBAR();
      if (RD && q == 0) { const unsigned a = ((n & 1) ? wa1 : wa0) + 64 * (n >> 1); ns.w[n] = cat8(ldstr(a), ldstr(a + 9216)); }
      if (RD && q == 1 && n == 1) ns.ol = lds128(oa + (NQT - 1) * 1024);
      SBAR();
    }
    if (RD && q < NQT - 1) { of[q] = lds128(oa + q * 1024); SBAR(); }
  }
}
template <int NQT, int NNT> DI void oproj_consumer(char* __restrict__ wsb, unsigned nb, unsigned kq, unsigned blk) {
  asm volatile("; oproj consumer %0 x %1" :: "i"(NQT), "i"(NNT) : "memory");
  constexpr unsigned qh = NQT == 4, q0 = 5 * qh;
  f32x4 acc[NQT][NNT];
  bf16x8 of[NQT - 1]; OSet<NQT, NNT> sa, sb;
  unsigned oa, wa0, wa1;
  { const unsigned lane = lane_id(), li = lane & 15, g = lane >> 4, n0 = (9 * nb + 1) >> 1;
    const unsigned rb = oslot_w(0) + 576 * (4 * g + (li >> 2)) + 8 * (lane & 3);
    oa = oslot_o(0) + q0 * 1024 + lane * 16;
    wa0 = rb + 64 * (n0 >> 1) + 32 * ((n0 ^ g) & 1); wa1 = rb + 64 * ((n0 + 1) >> 1) + 32 * ((n0 ^ g ^ 1) & 1); }
#define NEXTO(j) do { const unsigned d_ = oslot_w(((j) + 1) & 3) - oslot_w((j) & 3); oa += oslot_o(((j) + 1) & 3) - oslot_o((j) & 3); wa0 += d_; wa1 += d_; PIN(oa); PIN(wa0); PIN(wa1); } while (0)
  PIN(oa); PIN(wa0); PIN(wa1);
  STAMP2(24);
  BAR();
  STAMP2(25);
#pragma unroll
  for (int n = 0; n < NNT; ++n) { const unsigned a = ((n & 1) ? wa1 : wa0) + 64 * (n >> 1); sa.w[n] = cat8(ldstr(a), ldstr(a + 9216)); }
#pragma unroll
  for (int q = 0; q < NQT - 1; ++q) of[q] = lds128(oa + q * 1024);
  sa.ol = lds128(oa + (NQT - 1) * 1024);
  NEXTO(0);
  BAR(); o_unit<NQT, NNT, true, true>(acc, of, sa, sb, oa, wa0, wa1); NEXTO(1);
  WAITV(0);
#pragma unroll 1
  for (int j = 2; j < 32; j += 2) {
    BAR(); o_unit<NQT, NNT, true, false>(acc, of, sb, sa, oa, wa0, wa1); NEXTO(j);
    BAR(); o_unit<NQT, NNT, true, false>(acc, of, sa, sb, oa, wa0, wa1); NEXTO(j + 1);
  }
  o_unit<NQT, NNT, false, false>(acc, of, sb, sa, 0, 0, 0);
  asm volatile("s_nop 7\n\ts_nop 1" ::: "memory"); SBAR();
  STAMP2(26);
  const unsigned lane = lane_id();
  if (nb == kq) {
    unsigned ra = OPART + q0 * 5120 + lane * 16; PIN(ra);
#pragma unroll
    for (int q = 0; q < NQT; ++q)
#pragma unroll
      for (int n = 0; n < NNT; ++n) *(LAS f32x4*)(ra + (q * 5 + n) * 1024) = acc[q][n];
  } else {
    const __amdgpu_buffer_rsrc_t srdS = __builtin_amdgcn_make_buffer_rsrc((void*)(wsb + WS_X + (size_t)(blk * 4 + nb) * 46080), (short)0, 46080, 0x00020000);
#pragma unroll
    for (int q = 0; q < NQT; ++q)
#pragma unroll
      for (int u = 0; u < (NNT + 1) / 2; ++u) {
        const f32x4 a = acc[q][2 * u], c2 = acc[q][2 * u + 1 < NNT ? 2 * u + 1 : 2 * u];
        __builtin_amdgcn_raw_buffer_store_b128(u32x4{cvtpk(a[0], a[1]), cvtpk(a[2], a[3]), cvtpk(c2[0], c2[1]), cvtpk(c2[2], c2[3])}, srdS, lane * 16, ((q0 + q) * 3 + u) * 1024, XAUX);
      }
    WAITV(0);
  }
}
DI void oproj_finish(u16* __restrict__ outp, char* __restrict__ wsb, unsigned q, unsigned kq, unsigned blk) {
  const unsigned lane = lane_id(), li = lane & 15, g = lane >> 4, nnt = 5 - (kq & 1);
  BAR();
  STAMP2(27);
  unsigned* fl = (unsigned*)(wsb + WS_F);
  if (q == 8 && lane == 0) STA(fl + blk * 64 + 3, 1u);
  { const unsigned* pf = fl + ((blk & ~48u) + 16 * (lane & 3)) * 64 + 3;
    for (int spin = 0; spin < SPIN_CAP && !__all(LDA(pf) == 1u); ++spin) __builtin_amdgcn_s_sleep(1);
    __builtin_amdgcn_fence(__ATOMIC_ACQUIRE, "wavefront"); }
  STAMP2(28);
  f32x4 px[3][5];
  u32x4 pw[3][3];
#pragma unroll
  for (int p = 0; p < 3; ++p) {
    const unsigned pb = (blk & ~48u) + 16 * ((kq + 1 + p) & 3);
    const __amdgpu_buffer_rsrc_t srdP = __builtin_amdgcn_make_buffer_rsrc((void*)(wsb + WS_X + (size_t)(pb * 4 + kq) * 46080), (short)0, 46080, 0x00020000);
#pragma unroll
    for (int u = 0; u < 3; ++u) pw[p][u] = __builtin_amdgcn_raw_buffer_load_b128(srdP, lane * 16, (q * 3 + (u < 2 ? u : nnt - 3)) * 1024, 16);
  }
  SBAR();
#pragma unroll
  for (int p = 0; p < 3; ++p)
#pragma unroll
    for (int n = 0; n < 5; ++n) { const unsigned lo = pw[p][n >> 1][2 * (n & 1)], hi = pw[p][n >> 1][2 * (n & 1) + 1];
      px[p][n] = f32x4{__uint_as_float(lo << 16), __uint_as_float(lo & 0xffff0000u), __uint_as_float(hi << 16), __uint_as_float(hi & 0xffff0000u)}; }
  const unsigned tb = OPART + q * 5120;
  f32x4 own[5];
#pragma unroll
  for (int n = 0; n < 5; ++n) own[n] = *(const LAS f32x4*)(tb + (n < 4 ? n : nnt - 1) * 1024 + lane * 16);
#pragma unroll
  for (int n = 0; n < 5; ++n) {
    const f32x4 a = (own[n] + px[0][n]) + (px[1][n] + px[2][n]);
    *(LAS u32x2*)(tb + li * 176 + g * 8 + n * 32) = u32x2{cvtpk(a[0], a[1]), cvtpk(a[2], a[3])};
  }
#pragma unroll
  for (unsigned i = 0; i < 3; ++i) {
    const unsigned ch = i * 64 + lane, row = ch / 10, cc = ch - row * 10;
    if (row < 16 && cc < 2 * nnt) {
      *(u32x4*)(outp + (size_t)(16 * q + row) * (2 * DM) + cc * 8) = *(const LAS u32x4*)(tb + row * 176 + cc * 16);
    }
  }
}

__global__ __launch_bounds__(768, 3) void k_fused(const u16* __restrict__ xA, const u16* __restrict__ kA, const u16* __restrict__ vA, const u16* __restrict__ wqA, const u16* __restrict__ woA,
                                                  const u16* __restrict__ xB, const u16* __restrict__ kB, const u16* __restrict__ vB, const u16* __restrict__ wqB, const u16* __restrict__ woB,
                                                  char* __restrict__ wsb, u16* __restrict__ out) {
  extern __shared__ __attribute__((aligned(16))) char smem[];
  const unsigned wid12 = rfl(threadIdx.x >> 6), half = wid12 >= 6, wid = wid12 - 6 * half, lb = half * GRP;
  const unsigned blk = blockIdx.x;
  const unsigned idx = blk >> 3, pair = (blk & 7) * 4 + (idx >> 3), m = idx & 7, br = pair >> 4, b = pair & 15;
  const unsigned hg0 = (blk + half) & 3, kq = m >> 1, g0 = ((blk ^ kq) & 1) ? (blk + 1) & 3 : blk & 3;
  char* op = wsb + WS_O + (size_t)pair * NH * ITEM_B;
  const u16* kb = (br ? kB : kA) + (size_t)b * NH * SK * DH; const u16* vb = (br ? vB : vA) + (size_t)b * NH * SK * DH;
  Hs hs;
  if (wid < 4) {
    att_wave<2>(op, kb, vb, 2 * wid, m, hg0, lb, hs, nullptr);
    const unsigned t = half ? 3 - ((wid + 2) & 3) : wid, nb = 2 * half + (t & 1);
    if (t == 0) oproj_consumer<5, 5>(wsb, nb, kq, blk);
    else if (t == 1) oproj_consumer<5, 4>(wsb, nb, kq, blk);
    else if (t == 2) oproj_consumer<4, 5>(wsb, nb, kq, blk);
    else oproj_consumer<4, 4>(wsb, nb, kq, blk);
  } else if (wid == 4) {
    if (!half) hs_entry(hs, wsb, blk);
    else {
      const __amdgpu_buffer_rsrc_t srdT = __builtin_amdgcn_make_buffer_rsrc((char*)((br ? xB : xA) + (size_t)b * SQ * DM) + m * 20736, (short)0, 20736, 0x00020000);
      __builtin_amdgcn_s_sleep(XWARM);
#pragma unroll
      for (int p = 0; p < 3; ++p) { const unsigned n = p * 64 + lane_id(); dma4(vslot(3), (n < 162 ? n : 0) * 128, srdT); }
      {
        constexpr unsigned LN = (576 - WQW) * 64 / 128;
        const __amdgpu_buffer_rsrc_t srdQ = __builtin_amdgcn_make_buffer_rsrc((char*)((blk & 1) ? wqB : wqA) + (size_t)WQW * 8192 + (size_t)(blk >> 1) * LN * 128, (short)0, LN * 128, 0x00020000);
#pragma unroll
        for (unsigned p = 0; p < (LN + 63) / 64; ++p) { const unsigned n = p * 64 + lane_id(); dma4(vslot(3), (n < LN ? n : 0) * 128, srdQ); } }
    }
    att_wave<1>(op, kb, vb, 8, m, hg0, lb, hs, (br ? xB : xA) + (size_t)b * SQ * DM);
    if (half) {
      OLd ol; ol.init(op, br ? woB : woA, m, g0, 1, pair & 3);
      WAITV(0);
      ol.run(1);
    } else {
      WAITV(0);
      RBAR(); RBAR(); RBAR();
      if (lane_id() == 0) STA(hs.mine + 2, 1u);
#pragma unroll 1
      for (int j = 3; j < 8; ++j) RBAR();
      hs_post(hs.peer + 2, OBOX);
      RBAR(); RBAR();
      hs_wait_flag(hs, hs_take(OBOX), 2);
      RBAR(); RBAR(); RBAR();
      WAITV(0);
    }
    return;
  } else {
    OLd ol;
    loader(br ? wqB : wqA, (br ? xB : xA) + (size_t)b * SQ * DM, kb, vb, m, hg0, half, ol, op, br ? woB : woA, g0, pair & 3);
    ol.run(0);
    if (half) return;
  }
  oproj_finish(out + (size_t)b * SQ * (2 * DM) + br * DM + (m & 1) * 288 + 16 * ((9 * kq + 1) >> 1), wsb, wid < 4 ? wid + 4 * half : 8, kq, blk);
}

extern "C" void kernel_launch(void* const* d_in, const int* in_sizes, int n_in, void* d_out, int out_size, void* d_ws, size_t ws_size, hipStream_t stream) {
  (void)hipFuncSetAttribute((const void*)k_fused, hipFuncAttributeMaxDynamicSharedMemorySize, LDS_BYTES);
  k_fused<<<256, 768, LDS_BYTES, stream>>>((const u16*)d_in[0], (const u16*)d_in[1], (const u16*)d_in[2], (const u16*)d_in[3], (const u16*)d_in[4],
                                           (const u16*)d_in[5], (const u16*)d_in[6], (const u16*)d_in[7], (const u16*)d_in[8], (const u16*)d_in[9], (char*)d_ws, (u16*)d_out);
}
```

```cpp
#include <hip/hip_runtime.h>
#include <stdint.h>

typedef short    bf16x8 __attribute__((ext_vector_type(8)));
typedef short    s16x4  __attribute__((ext_vector_type(4)));
typedef float    f32x4  __attribute__((ext_vector_type(4)));
typedef float    f32x2  __attribute__((ext_vector_type(2)));
typedef __bf16   bf16x2 __attribute__((ext_vector_type(2)));
typedef unsigned u32x4  __attribute__((ext_vector_type(4)));
typedef unsigned u32x2  __attribute__((ext_vector_type(2)));
typedef unsigned short u16;
#define DI __device__ __forceinline__
#define LAS __attribute__((address_space(3)))
#define MFMA16(a, b, c) __builtin_amdgcn_mfma_f32_16x16x32_bf16(a, b, c, 0, 0, 0)
#define SBAR() __builtin_amdgcn_sched_barrier(0)
#define PIN(x) asm volatile("" : "+v"(x))
#define MFMA16_V(acc, a, b) asm volatile("v_mfma_f32_16x16x32_bf16 %0, %1, %2, %0" : "+v"(acc) : "v"(a), "v"(b))
#define MFMA16_Z(d, a, b) asm volatile("v_mfma_f32_16x16x32_bf16 %0, %1, %2, 0" : "=&v"(d) : "v"(a), "v"(b))
#define MFMA16_C(d, a, b, cc) asm volatile("v_mfma_f32_16x16x32_bf16 %0, %1, %2, %3" : "=&v"(d) : "v"(a), "v"(b), "v"(cc))

constexpr int NB = 16, SQ = 144, DM = 576, NH = 32, SK = 512, DH = 128, HD = NH * DH;
constexpr int ITEM_B = 9 * 4 * 1024;
constexpr size_t WS_O = 0;
constexpr size_t WS_X = (size_t)1024 * ITEM_B, WS_F = WS_X + (size_t)256 * 4 * 46080;
constexpr float QSCALE = 0.08838834764831845f * 1.4426950408889634f;

DI unsigned rfl(unsigned x) { return __builtin_amdgcn_readfirstlane(x); }
DI unsigned lane_id() { unsigned l; asm volatile("v_mbcnt_lo_u32_b32 %0, -1, 0\n\tv_mbcnt_hi_u32_b32 %0, -1, %0" : "=v"(l)); return l; }
DI unsigned cvtpk(float lo, float hi) { f32x2 v = {lo, hi}; bf16x2 b = __builtin_convertvector(v, bf16x2); return __builtin_bit_cast(unsigned, b); }
DI bf16x8 lds128(unsigned a) { return *(const LAS bf16x8*)a; }
DI s16x4 ldstr(unsigned a) { return __builtin_bit_cast(s16x4, __builtin_amdgcn_ds_read_tr16_b64_v4i16((LAS s16x4*)a)); }
DI bf16x8 cat8(s16x4 lo, s16x4 hi) { return __builtin_shufflevector(lo, hi, 0, 1, 2, 3, 4, 5, 6, 7); }
DI void dma4(unsigned m0v, unsigned voff, __amdgpu_buffer_rsrc_t srd) {
  asm volatile("s_mov_b32 m0, %0\n\ts_nop 3\n\tbuffer_load_dword %1, %2, 0 offen lds" :: "s"(m0v), "v"(voff), "s"(srd) : "memory");
}
#define TILE8_BODY(POL) \
  "s_nop 3\n\ts_mov_b32 m0, %1\n\ts_mov_b32 %0, %2\n\t" \
  "buffer_load_dwordx4 %3, %5, %0 offen " POL " lds\n\ts_add_u32 m0, m0, 896\n\ts_nop 0\n\tbuffer_load_dwordx4 %3, %5, %0 offen offset:128 " POL " lds\n\ts_add_u32 m0, m0, 1152\n\ts_add_u32 %0, %0, %6\n\t" \
  "buffer_load_dwordx4 %4, %5, %0 offen " POL " lds\n\ts_add_u32 m0, m0, 896\n\ts_nop 0\n\tbuffer_load_dwordx4 %4, %5, %0 offen offset:128 " POL " lds\n\ts_add_u32 m0, m0, 1152\n\ts_add_u32 %0, %0, %6\n\t" \
  "buffer_load_dwordx4 %3, %5, %0 offen " POL " lds\n\ts_add_u32 m0, m0, 896\n\ts_nop 0\n\tbuffer_load_dwordx4 %3, %5, %0 offen offset:128 " POL " lds\n\ts_add_u32 m0, m0, 1152\n\ts_add_u32 %0, %0, %6\n\t" \
  "buffer_load_dwordx4 %4, %5, %0 offen " POL " lds\n\ts_add_u32 m0, m0, 896\n\ts_nop 0\n\tbuffer_load_dwordx4 %4, %5, %0 offen offset:128 " POL " lds"
template <bool NT, int D2> DI void dma_tile8(unsigned lo, unsigned so, unsigned va, unsigned vb, __amdgpu_buffer_rsrc_t srd) {
  unsigned t;
  if constexpr (NT) asm volatile(TILE8_BODY("nt") : "=&s"(t) : "s"(lo), "s"(so), "v"(va), "v"(vb), "s"(srd), "i"(D2) : "memory", "scc");
  else              asm volatile(TILE8_BODY("")   : "=&s"(t) : "s"(lo), "s"(so), "v"(va), "v"(vb), "s"(srd), "i"(D2) : "memory", "scc");
}
template <int D2> DI void dma_tile9(unsigned lo, unsigned so, unsigned va, __amdgpu_buffer_rsrc_t srd) {
  unsigned t;
  asm volatile("s_nop 3\n\ts_mov_b32 m0, %1\n\ts_mov_b32 %0, %2\n\t.rept 9\n\t"
               "buffer_load_dwordx4 %3, %4, %0 offen lds\n\ts_add_u32 m0, m0, 2048\n\ts_add_u32 %0, %0, %5\n\t.endr"
               : "=&s"(t) : "s"(lo), "s"(so), "v"(va), "s"(srd), "i"(D2) : "memory", "scc");
}
#define WAITV(n) asm volatile("s_waitcnt vmcnt(" #n ")" ::: "memory")
#define BAR() do { asm volatile("" ::: "memory"); __builtin_amdgcn_s_waitcnt(0xC07F); asm volatile("s_barrier" ::: "memory"); } while (0)
#define RBAR() asm volatile("s_barrier" ::: "memory")
constexpr unsigned GRP = 65536, WRING = 3 * 8192, XSLOT = 18432, XB = 126976, XA = XB + XSLOT, LDS_BYTES = 163840;
constexpr unsigned kslot(int i) { return 8192 * ((i & 3) == 0 ? 0 : (i & 3) == 1 ? 1 : (i & 3) == 2 ? 5 : 3); }
constexpr unsigned vslot(int i) { return 8192 * ((i & 3) == 0 ? 2 : (i & 3) == 1 ? 6 : (i & 3) == 2 ? 4 : 7); }
static_assert(XA + XSLOT == LDS_BYTES, "LDS map");


DI float red_g_max(float m) {
  auto r = __builtin_amdgcn_permlane32_swap(__float_as_uint(m), __float_as_uint(m), false, false);
  m = fmaxf(__uint_as_float(r[0]), __uint_as_float(r[1]));
  auto s = __builtin_amdgcn_permlane16_swap(__float_as_uint(m), __float_as_uint(m), false, false);
  return fmaxf(__uint_as_float(s[0]), __uint_as_float(s[1]));
}
DI float red_g_sum(float m) {
  auto r = __builtin_amdgcn_permlane32_swap(__float_as_uint(m), __float_as_uint(m), false, false);
  m = __uint_as_float(r[0]) + __uint_as_float(r[1]);
  auto s = __builtin_amdgcn_permlane16_swap(__float_as_uint(m), __float_as_uint(m), false, false);
  return __uint_as_float(s[0]) + __uint_as_float(s[1]);
}

template <int NQ> struct AttC {
  f32x4 O[NQ][8]; bf16x8 Q[NQ][4]; f32x4 S[2][NQ][2]; f32x4 negm[NQ]; float l[NQ];
  unsigned kb, vb0, vb1;
};
template <int NQ> struct Tab;
template <> struct Tab<2> {
  static constexpr int NPRE = 6;
  static constexpr int pre[6] = {100, 101, 102, 103, 104, 105};
  static constexpr int gx[16][2] = {{106, 0}, {107, 0}, {200, 201}, {110, 0}, {202, 203}, {111, 0}, {112, 0}, {113, 0}, {114, 0}, {210, 211}, {115, 0}, {116, 0}, {117, 0}, {300, 310}, {212, 213}, {301, 311}};
  static constexpr int gy[16] = {302, 312, 303, 313, 304, 314, 305, 315, 306, 316, 307, 317, 0, 0, 0, 0};
  static constexpr int all[24] = {100, 101, 102, 103, 104, 105, 106, 107, 110, 200, 201, 202, 203, 111, 112, 113, 114, 115, 116, 117, 210, 211, 212, 213};
  static constexpr int ady[16] = {300, 310, 301, 311, 302, 312, 303, 313, 304, 314, 305, 315, 306, 316, 307, 317};
};
template <> struct Tab<1> {
  static constexpr int NPRE = 4;
  static constexpr int pre[4] = {100, 101, 102, 103};
  static constexpr int gx[8][2] = {{104, 0}, {105, 0}, {200, 201}, {106, 0}, {107, 0}, {300, 301}, {202, 203}, {302, 303}};
  static constexpr int gy[8] = {304, 305, 306, 307, 0, 0, 0, 0};
  static constexpr int all[12] = {100, 101, 102, 103, 104, 105, 106, 107, 200, 201, 202, 203};
  static constexpr int ady[8] = {300, 301, 302, 303, 304, 305, 306, 307};
};
template <int NQ, int P, bool QK> struct Step {
  static constexpr int X = P & 1;
  static constexpr unsigned ks_ = kslot(P + 1), vs_ = vslot(P);
  bf16x8 fr[4]; float e[NQ][8]; unsigned pw[NQ][4]; u32x4 Pw[NQ];
  template <int OP> DI void op(AttC<NQ>& c) {
    constexpr int ty = OP / 100, qt = (OP / 10) % 10, j = OP % 10;
    if constexpr (ty == 1) { e[qt][j] = c.S[X][qt][j >> 2][j & 3]; asm volatile("v_exp_f32 %0, %0" : "+v"(e[qt][j])); }
    if constexpr (ty == 2) asm volatile("v_cvt_pk_bf16_f32 %0, %1, %2" : "=v"(pw[qt][j]) : "v"(e[qt][2 * j]), "v"(e[qt][2 * j + 1]));
    if constexpr (ty == 3) asm volatile("v_add_f32 %0, %0, %1" : "+v"(c.l[qt]) : "v"(e[qt][j]));
  }
  template <int R> DI void rd(AttC<NQ>& c) {
    if constexpr (R < 8) fr[R & 3] = lds128(c.kb + ks_ + 4096 * (R & 1) + 512 * (R >> 1));
    else if constexpr (R < 16) {
      constexpr int dt = R - 8;
      const unsigned vb = ((dt & 1) ? c.vb1 : c.vb0) + vs_ + 512 * (dt >> 1);
      fr[R & 3] = cat8(ldstr(vb), ldstr(vb + 4096));
    }
    SBAR();
  }
  template <int I> DI void gx(AttC<NQ>& c) {
    constexpr int K = I / NQ, qt = I % NQ, s = K >> 1, kt = K & 1;
    if constexpr (s == 0) MFMA16_C(c.S[X ^ 1][qt][kt], fr[K & 3], c.Q[qt][s], c.negm[qt]); else MFMA16_V(c.S[X ^ 1][qt][kt], fr[K & 3], c.Q[qt][s]);
    if constexpr (qt == NQ - 1) { SBAR(); rd<K + 4>(c); }
    op<Tab<NQ>::gx[I][0]>(c); op<Tab<NQ>::gx[I][1]>(c);
    if constexpr (I + 1 < 8 * NQ) gx<I + 1>(c);
  }
  template <int I> DI void gy(AttC<NQ>& c) {
    constexpr int Dt = I / NQ, qt = I % NQ;
    MFMA16_V(c.O[qt][Dt], fr[Dt & 3], Pw[qt]);
    if constexpr (qt == NQ - 1) { SBAR(); rd<Dt + 12>(c); }
    op<(QK ? Tab<NQ>::gy[I] : Tab<NQ>::ady[I])>(c);
    if constexpr (I + 1 < 8 * NQ) gy<I + 1>(c);
  }
  template <int I> DI void pre(AttC<NQ>& c) {
    op<(QK ? Tab<NQ>::pre[I < Tab<NQ>::NPRE ? I : 0] : Tab<NQ>::all[I])>(c);
    if constexpr (I + 1 < (QK ? Tab<NQ>::NPRE : 12 * NQ)) pre<I + 1>(c);
  }
  DI void run(AttC<NQ>& c) {
    if constexpr (QK) {
      rd<0>(c); rd<1>(c); rd<2>(c); rd<3>(c);
      pre<0>(c);
      gx<0>(c);
    } else {
      rd<8>(c); rd<9>(c); rd<10>(c); rd<11>(c);
      pre<0>(c);
      asm volatile("s_nop 1");
    }
#pragma unroll
    for (int qt = 0; qt < NQ; ++qt) Pw[qt] = u32x4{pw[qt][0], pw[qt][1], pw[qt][2], pw[qt][3]};
    gy<0>(c);
  }
};

template <int NQ, int P, bool QK> DI void att_step(AttC<NQ>& c) { BAR(); SBAR();
  Step<NQ, P, QK> s; s.run(c); SBAR(); }

template <int NQ> DI void att_prologue(AttC<NQ>& c) {
#pragma unroll
  for (int s = 0; s < 4; ++s)
#pragma unroll
    for (int kt = 0; kt < 2; ++kt) {
      const bf16x8 kf = lds128(c.kb + 4096 * kt + 512 * s);
#pragma unroll
      for (int qt = 0; qt < NQ; ++qt) c.S[0][qt][kt] = MFMA16(kf, c.Q[qt][s], s == 0 ? (f32x4{0, 0, 0, 0}) : c.S[0][qt][kt]);
    }
#pragma unroll
  for (int qt = 0; qt < NQ; ++qt) {
    float mx = c.S[0][qt][0][0];
#pragma unroll
    for (int j = 1; j < 8; ++j) mx = fmaxf(mx, c.S[0][qt][j >> 2][j & 3]);
    mx = red_g_max(mx);
    c.negm[qt] = f32x4{-mx, -mx, -mx, -mx};
    c.S[0][qt][0] += c.negm[qt]; c.S[0][qt][1] += c.negm[qt];
    c.l[qt] = 0.f;
#pragma unroll
    for (int dt = 0; dt < 8; ++dt) c.O[qt][dt] = f32x4{0, 0, 0, 0};
  }
}
template <int NQ> DI void att_redo_true_max(AttC<NQ>& c, const u16* __restrict__ kh, const u16* __restrict__ vh) {
  const unsigned lane = lane_id(), li = lane & 15, g = lane >> 4;
  const u16* kp = kh + li * DH + g * 8;
  float mx[NQ];
#pragma unroll
  for (int qt = 0; qt < NQ; ++qt) mx[qt] = -__builtin_inff();
#pragma unroll 1
  for (int kt = 0; kt < SK / 16; ++kt) {
    f32x4 s4[NQ];
#pragma unroll
    for (int qt = 0; qt < NQ; ++qt) s4[qt] = f32x4{0, 0, 0, 0};
#pragma unroll
    for (int s = 0; s < 4; ++s) {
      const bf16x8 kf = *(const bf16x8*)(kp + kt * 16 * DH + s * 32);
#pragma unroll
      for (int qt = 0; qt < NQ; ++qt) s4[qt] = MFMA16(kf, c.Q[qt][s], s4[qt]);
    }
#pragma unroll
    for (int qt = 0; qt < NQ; ++qt) mx[qt] = fmaxf(fmaxf(fmaxf(mx[qt], s4[qt][0]), fmaxf(s4[qt][1], s4[qt][2])), s4[qt][3]);
  }
#pragma unroll
  for (int qt = 0; qt < NQ; ++qt) {
    mx[qt] = red_g_max(mx[qt]); c.l[qt] = 0.f;
#pragma unroll
    for (int dt = 0; dt < 8; ++dt) c.O[qt][dt] = f32x4{0, 0, 0, 0};
  }
#pragma unroll 1
  for (int st = 0; st < SK / 32; ++st) {
    u32x4 pw[NQ];
#pragma unroll
    for (int kt = 0; kt < 2; ++kt) {
      f32x4 s4[NQ];
#pragma unroll
      for (int qt = 0; qt < NQ; ++qt) s4[qt] = f32x4{-mx[qt], -mx[qt], -mx[qt], -mx[qt]};
#pragma unroll
      for (int s = 0; s < 4; ++s) {
        const bf16x8 kf = *(const bf16x8*)(kp + (2 * st + kt) * 16 * DH + s * 32);
#pragma unroll
        for (int qt = 0; qt < NQ; ++qt) s4[qt] = MFMA16(kf, c.Q[qt][s], s4[qt]);
      }
#pragma unroll
      for (int qt = 0; qt < NQ; ++qt) {
        float e[4];
#pragma unroll
        for (int j = 0; j < 4; ++j) { e[j] = __builtin_amdgcn_exp2f(s4[qt][j]); c.l[qt] += e[j]; }
        pw[qt][2 * kt] = cvtpk(e[0], e[1]); pw[qt][2 * kt + 1] = cvtpk(e[2], e[3]);
      }
    }
    const u16* vp = vh + (32 * st + 4 * g) * DH + li;
#pragma unroll
    for (int dt = 0; dt < 8; ++dt) {
      bf16x8 vf;
#pragma unroll
      for (int j = 0; j < 8; ++j) vf[j] = (short)vp[(16 * (j >> 2) + (j & 3)) * DH + 16 * dt];
#pragma unroll
      for (int qt = 0; qt < NQ; ++qt) c.O[qt][dt] = MFMA16(vf, __builtin_bit_cast(bf16x8, pw[qt]), c.O[qt][dt]);
      SBAR();
    }
  }
#pragma unroll
  for (int qt = 0; qt < NQ; ++qt) c.l[qt] = red_g_sum(c.l[qt]);
}
template <int NQ> DI void att_epilogue(AttC<NQ>& c, char* ob, const u16* kh, const u16* vh) {
  bool bad = false;
#pragma unroll
  for (int qt = 0; qt < NQ; ++qt) { c.l[qt] = red_g_sum(c.l[qt]); bad |= !(c.l[qt] < 16777216.0f); }
  if (__builtin_expect(__any(bad), 0)) att_redo_true_max<NQ>(c, kh, vh);
#pragma unroll
  for (int qt = 0; qt < NQ; ++qt) {
    const float inv = __builtin_amdgcn_rcpf(c.l[qt]);
#pragma unroll
    for (int s = 0; s < 4; ++s) {
      const f32x4 a = c.O[qt][2 * s] * inv, bb = c.O[qt][2 * s + 1] * inv;
      u32x4 w = {cvtpk(a[0], a[1]), cvtpk(a[2], a[3]), cvtpk(bb[0], bb[1]), cvtpk(bb[2], bb[3])};
      asm volatile("global_store_dwordx4 %0, %1, off sc1\n\ts_nop 1" :: "v"(ob + (qt * 4 + s) * 1024), "v"(w) : "memory");
    }
  }
}

DI bf16x8 q_frag(const f32x4& t0, const f32x4& t1) {
  const f32x4 u0 = t0 * QSCALE, u1 = t1 * QSCALE;
  unsigned a0 = cvtpk(u0[0], u0[1]), a1 = cvtpk(u0[2], u0[3]), b0 = cvtpk(u1[0], u1[1]), b1 = cvtpk(u1[2], u1[3]);
  auto r0 = __builtin_amdgcn_permlane32_swap(a0, b0, false, false);
  auto r1 = __builtin_amdgcn_permlane32_swap(a1, b1, false, false);
  auto s0 = __builtin_amdgcn_permlane16_swap(r0[0], r0[1], false, false);
  auto s1 = __builtin_amdgcn_permlane16_swap(r1[0], r1[1], false, false);
  u32x4 w = {s0[0], s1[0], s0[1], s1[1]};
  return __builtin_bit_cast(bf16x8, w);
}
template <int NR, int WS, int XO, bool Z = false>
DI void q_unit(f32x4 (&qa)[NR][4], const bf16x8 (&wc)[4], bf16x8 (&wn)[4], bf16x8 (&xc)[NR], const bf16x8& lc, bf16x8 (&xn)[NR], bf16x8& ln, unsigned w0, unsigned w1, const unsigned (&xa)[3]) {
#define XADDR(row) (xa[(row) >> 1] + ((row) & 1) * 2048 + XO)
#pragma unroll
  for (int q = 0; q < NR; ++q) {
#pragma unroll
    for (int d = 0; d < 4; ++d) {
      if constexpr (Z) MFMA16_Z(qa[q][d], wc[d], q < NR - 1 ? xc[q] : lc); else MFMA16_V(qa[q][d], wc[d], q < NR - 1 ? xc[q] : lc);
      SBAR();
      const int i = 4 * q + d;
      if (WS >= 0 && i < 8 && !(i & 1)) { const int u = i >> 1; const unsigned a = ((u & 1) ? w1 : w0) + 8192 * WS + 512 * (u >> 1); wn[u] = cat8(ldstr(a), ldstr(a + 256)); }
      if (XO >= 0 && (i & 1) && (i >> 1) < NR) xn[i >> 1] = lds128(XADDR(i >> 1));
      if (XO >= 0 && i == 10) ln = lds128(XADDR(NR - 1) + 512);
      SBAR();
    }
    if (XO >= 0 && q < NR - 1) { xc[q] = lds128(XADDR(q) + 512); SBAR(); }
  }
#undef XADDR
}

#define LDA(p) __hip_atomic_load((p), __ATOMIC_RELAXED, __HIP_MEMORY_SCOPE_AGENT)
#define STA(p, v) __hip_atomic_store((p), (v), __ATOMIC_RELAXED, __HIP_MEMORY_SCOPE_AGENT)
constexpr int SPIN_CAP = 1 << 21;
struct Hs { unsigned* mine; unsigned* peer; unsigned me, x, e; bool ok; };
DI void hs_post(const unsigned* p, unsigned box) { asm volatile("s_mov_b32 m0, %0\n\ts_nop 3\n\tglobal_load_lds_dword %1, off sc1" :: "s"(box), "v"(p) : "memory"); }
DI unsigned hs_take(unsigned box) { WAITV(0); return *(volatile LAS unsigned*)(box + 4 * lane_id()); }
DI void hs_entry(Hs& h, char* wsb, unsigned blk) {
  const unsigned lane = lane_id();
  h.me = (blk >> 3) & 7;
  h.mine = (unsigned*)(wsb + WS_F) + blk * 64;
  h.peer = (unsigned*)(wsb + WS_F) + ((blk & ~56u) + 8 * (lane & 7)) * 64;
  if (lane >= 1 && lane <= 3) STA(h.mine + lane, 0u);
  const unsigned y0 = LDA(h.peer + 32 + h.me);
  unsigned x = rfl(y0) + 1;
  while (__any(y0 == x)) ++x;
  h.x = x; h.e = 0; h.ok = false;
  WAITV(0);
  if (lane == 0) STA(h.mine, x);
}
DI void hs_wait_flag(const Hs& h, unsigned v, int word) {
  for (int spin = 0; !__all(v == 1u) && spin < SPIN_CAP; ++spin) { __builtin_amdgcn_s_sleep(2); v = LDA(h.peer + word); }
  __builtin_amdgcn_fence(__ATOMIC_ACQUIRE, "agent");
}
constexpr unsigned HBOX = 161792;
DI void hs_hook(Hs& h, int k) {
  const unsigned lane = lane_id();
  if (k == 0) hs_post(h.peer, HBOX);
  else if (k == 1) { h.e = hs_take(HBOX); if (lane < 8) STA(h.mine + 32 + lane, h.e); }
  else if (k == 4) hs_post(h.peer + 32 + h.me, HBOX);
  else if (k == 5) { h.ok = __all(hs_take(HBOX) == h.x); hs_post(h.peer, HBOX); }
  else if (k == 6) { const unsigned e = hs_take(HBOX); if (lane < 8 && e != h.e) STA(h.mine + 32 + lane, e); hs_post(h.peer + 1, HBOX); }
  else if (k == 7) {
    unsigned f = hs_take(HBOX);
    if (!h.ok) {
      for (int spin = 0; spin < SPIN_CAP; ++spin) {
        const unsigned e = LDA(h.peer);
        if (lane < 8) STA(h.mine + 32 + lane, e);
        if (__all(LDA(h.peer + 32 + h.me) == h.x)) break;
        __builtin_amdgcn_s_sleep(2);
      }
      WAITV(0);
      const unsigned e = LDA(h.peer);
      if (lane < 8) STA(h.mine + 32 + lane, e);
      f = 0;
    }
    hs_wait_flag(h, f, 1);
  }
}

constexpr unsigned EX = 122880;
template <int NR> DI void q_phase(bf16x8 (&own)[2][2], unsigned wid, unsigned lb) {
  asm volatile("; q phase %0" :: "i"(NR) : "memory");
  const unsigned lane = lane_id(), li = lane & 15, g = lane >> 4, pos = lane & 3, tq = li >> 2;
  unsigned w0 = lb + WRING + 1024 * (wid & 1) + 2048 * g + 64 * tq + 32 * (g & 1) + 8 * pos, w1 = w0 ^ 32u;
  const unsigned xl = XB + 1024 * (li >> 3) + 64 * (li & 7) + 16 * (g ^ (2 * (li >> 3)));
  unsigned xa[3] = {xl + wid * 4096, xl + (wid ^ 1) * 4096, xl + 16384};
  bf16x8 w[4], v[4], xA[NR], xB[NR], l0, l1;
  f32x4 qa[NR][4];
  PIN(w0); PIN(w1); PIN(xa[0]); PIN(xa[1]); if (NR == 5) PIN(xa[2]);
  BAR();
#pragma unroll
  for (int u = 0; u < 4; ++u) { const unsigned a = ((u & 1) ? w1 : w0) + 512 * (u >> 1); w[u] = cat8(ldstr(a), ldstr(a + 256)); }
#pragma unroll
  for (int q = 0; q < NR; ++q) { const unsigned a = xa[q >> 1] + (q & 1) * 2048 + XSLOT; xB[q] = lds128(a); if (q < NR - 1) xA[q] = lds128(a + 512); else l0 = lds128(a + 512); }
  BAR(); q_unit<NR, 1, -1, true>(qa, w, v, xB, xB[NR - 1], xA, l1, w0, w1, xa);
#pragma unroll 1
  for (int n = 0; n < 4; ++n) {
    WAITV(0);
    BAR(); q_unit<NR, 2, 0>(qa, v, w, xA, l0, xB, l1, w0, w1, xa);
    BAR(); q_unit<NR, 3, -1>(qa, w, v, xB, xB[NR - 1], xA, l0, w0, w1, xa);
    BAR(); q_unit<NR, 0, XSLOT>(qa, v, w, xA, l1, xB, l0, w0, w1, xa);
    BAR(); q_unit<NR, 1, -1>(qa, w, v, xB, xB[NR - 1], xA, l1, w0, w1, xa);
  }
  BAR(); q_unit<NR, -1, -1>(qa, v, w, xA, l0, xB, l1, w0, w1, xa);
  asm volatile("s_nop 7" ::: "memory"); SBAR();
  unsigned ea = EX + (lb ? 0 : 20480) + lane * 16; PIN(ea);
#pragma unroll
  for (int s = 0; s < 2; ++s) {
    *(LAS bf16x8*)(ea + wid * 4096 + s * 1024) = q_frag(qa[2][2 * s], qa[2][2 * s + 1]);
    *(LAS bf16x8*)(ea + wid * 4096 + 2048 + s * 1024) = q_frag(qa[3][2 * s], qa[3][2 * s + 1]);
    if constexpr (NR == 5) *(LAS bf16x8*)(ea + 16384 + wid * 2048 + s * 1024) = q_frag(qa[4][2 * s], qa[4][2 * s + 1]);
  }
#pragma unroll
  for (int q = 0; q < 2; ++q)
#pragma unroll
    for (int s = 0; s < 2; ++s) own[q][s] = q_frag(qa[q][2 * s], qa[q][2 * s + 1]);
}

template <int NQ> DI void att_wave(char* ows_pair, const u16* kbase, const u16* vbase, unsigned qt0, unsigned m, unsigned hg0, unsigned lb, Hs& hs, const u16* x) {
  asm volatile("; attention wave NQ=%0" :: "i"(NQ) : "memory");
#pragma unroll 1
  for (int r = 0; r < 2; ++r) {
    AttC<NQ> c;
    if constexpr (NQ == 2) {
      bf16x8 own[2][2];
      if (qt0 < 4) q_phase<5>(own, qt0 >> 1, lb); else q_phase<4>(own, qt0 >> 1, lb);
      unsigned ea = EX + (lb ? 0 : 20480) + (qt0 ^ 2) * 2048 + lane_id() * 16; PIN(ea);
      BAR();
      const bool hi = qt0 & 2;
#pragma unroll
      for (int q = 0; q < 2; ++q)
#pragma unroll
        for (int s = 0; s < 2; ++s) { const bf16x8 rc = lds128(ea + q * 2048 + s * 1024); c.Q[q][s] = hi ? rc : own[q][s]; c.Q[q][2 + s] = hi ? own[q][s] : rc; }
    } else {
      const __amdgpu_buffer_rsrc_t srdX = __builtin_amdgcn_make_buffer_rsrc((void*)x, (short)0, SQ * DM * 2, 0x00020000);
      const unsigned lane = lane_id(), half = lb != 0, vx = ((((lane >> 2) & 7) + 8 * half) * (DM * 2) + (lane >> 5) * 64 + (lane & 3) * 16) ^ (32u * half);
      BAR();
#pragma unroll 1
      for (int n = 1; n < 19; n += 2) {
        BAR();
        if (n < 14) { dma_tile9<16 * DM * 2>(((n & 2) ? XB : XA) + half * 1024, ((n + 3) >> 1) * 128, vx, srdX); WAITV(9); } else WAITV(0);
        BAR();         if (n == 1 && lb == 0 && r == 1 && lane == 0) STA(hs.mine + 1, 1u);
      }
      BAR();
      unsigned ea = EX + (lb ? 0 : 20480) + 16384 + lane_id() * 16; PIN(ea);
#pragma unroll
      for (int s = 0; s < 4; ++s) c.Q[0][s] = lds128(ea + s * 1024);
      __builtin_amdgcn_s_waitcnt(0xC07F);
    }
    { const unsigned lane = lane_id(), li = lane & 15, g = lane >> 4;
      c.kb = lb + 2048 * (li >> 3) + 64 * (li & 7) + 16 * (g ^ (2 * (li >> 3)));
      c.vb0 = lb + 2048 * (g >> 1) + 64 * (4 * (g & 1) + (li >> 2)) + 32 * (g & 1) + 8 * (lane & 3); c.vb1 = c.vb0 ^ 32u;
      PIN(c.kb); PIN(c.vb0); PIN(c.vb1); }
    if (NQ == 1 && lb == 0) hs_hook(hs, 4 * r);
    att_prologue<NQ>(c);
    SBAR();
#pragma unroll 1
    for (int ii = 0; ii < 3; ++ii) { att_step<NQ, 0, true>(c); att_step<NQ, 1, true>(c); att_step<NQ, 2, true>(c); att_step<NQ, 3, true>(c);
      if (NQ == 1 && lb == 0) hs_hook(hs, 4 * r + 1 + ii); }
    att_step<NQ, 0, true>(c); att_step<NQ, 1, true>(c);
    if (NQ == 1) WAITV(0);
    att_step<NQ, 2, true>(c); att_step<NQ, 3, false>(c);
    asm volatile("s_nop 7\n\ts_nop 1" ::: "memory"); SBAR();
    const unsigned h = 8 * ((hg0 + 2 * r) & 3) + m;
    att_epilogue<NQ>(c, ows_pair + (size_t)h * ITEM_B + qt0 * 4096 + lane_id() * 16, kbase + (size_t)h * SK * DH, vbase + (size_t)h * SK * DH);
  }
}

constexpr unsigned STEP_TICKS = 106;
DI void wait_newer(int n) {
  if (n >= 32) { if (n >= 40) WAITV(40); else WAITV(32); }
  else if (n >= 16) { if (n >= 24) WAITV(24); else WAITV(16); }
  else if (n >= 8) WAITV(8); else WAITV(0);
}
DI unsigned oslot_o(unsigned s) { return s == 0 ? 131072 : GRP - 9216 + 9216 * s; }
DI unsigned oslot_w(unsigned s) { return s == 0 ? 140288 : 18432 * (s - 1); }
constexpr unsigned OPART = 93184;
constexpr unsigned OBOX = 55296;
struct OLd {
  __amdgpu_buffer_rsrc_t srd; unsigned vo[9], h0, isw, rot;
  DI void init(const char* op, const u16* wo, unsigned m, unsigned g0, unsigned isw_, unsigned rot_) {
    const unsigned lane = lane_id(); h0 = 8 * g0 + (m & 4); isw = isw_; rot = rot_;
    srd = isw ? __builtin_amdgcn_make_buffer_rsrc((void*)wo, (short)0, HD * DM * 2, 0x00020000) : __builtin_amdgcn_make_buffer_rsrc((void*)op, (short)0, NH * ITEM_B, 0x00020000);
#pragma unroll
    for (int p = 0; p < 9; ++p) { const unsigned n = 64 * p + lane, row = n / 36, ch = n - 36 * row;
      vo[p] = isw ? row * (DM * 2) + ((ch ^ ((row >> 1) & 2)) * 16) + (m & 1) * 576 : p * 4096 + lane * 16; }
  }
#define OPIECE(k) "buffer_load_dwordx4 %" #k ", %1, %2 offen lds\n\ts_add_u32 m0, m0, 1024\n\ts_nop 0\n\t"
  DI void issue(int j, unsigned part) const {
    const unsigned h = ((h0 + (j & 16)) & 31) + (((j >> 2) + rot) & 3), s = j & 3;
    const unsigned so = isw ? ((h * 4 + s) * 32 + 16 * part) * (DM * 2) : h * ITEM_B + s * 1024, lo = isw ? oslot_w(j & 3) + part * 9216 : oslot_o(j & 3);
    asm volatile("s_mov_b32 m0, %0\n\ts_nop 3\n\t" OPIECE(3) OPIECE(4) OPIECE(5) OPIECE(6) OPIECE(7) OPIECE(8) OPIECE(9) OPIECE(10) OPIECE(11)
                 :: "s"(lo), "s"(srd), "s"(so), "v"(vo[0]), "v"(vo[1]), "v"(vo[2]), "v"(vo[3]), "v"(vo[4]), "v"(vo[5]), "v"(vo[6]), "v"(vo[7]), "v"(vo[8]) : "memory", "scc");
  }
  DI void run(unsigned part) const {
#pragma unroll 1
    for (int j = 0; j < 32; ++j) {
      if (j == 0 || j == 30) WAITV(9); else if (j < 30) WAITV(18); else WAITV(0);
      RBAR();
      if (j == 0) issue(2, part);
      if (j + 3 < 32) issue(j + 3, part);
    }
  }
};

DI void loader(const u16* wq, const u16* x, const u16* kbase, const u16* vbase, unsigned m, unsigned hg0, unsigned half, OLd& ol, const char* op, const u16* wo, unsigned g0, unsigned rot) {
  const unsigned lane = lane_id(), row7 = (lane >> 2) & 7, sh = lane >> 5, pos = lane & 3, lb = half * GRP;
  const __amdgpu_buffer_rsrc_t srdW = __builtin_amdgcn_make_buffer_rsrc((void*)wq, (short)0, DM * HD * 2, 0x00020000);
  const __amdgpu_buffer_rsrc_t srdX = __builtin_amdgcn_make_buffer_rsrc((void*)x, (short)0, SQ * DM * 2, 0x00020000);
  const __amdgpu_buffer_rsrc_t srdK = __builtin_amdgcn_make_buffer_rsrc((void*)kbase, (short)0, NH * SK * DH * 2, 0x00020000);
  const __amdgpu_buffer_rsrc_t srdV = __builtin_amdgcn_make_buffer_rsrc((void*)vbase, (short)0, NH * SK * DH * 2, 0x00020000);
  const unsigned vw0 = row7 * (HD * 2) + sh * 64 + pos * 16, vw1 = vw0 ^ 32u;
  const unsigned vx = (row7 * (DM * 2) + sh * 64 + pos * 16) ^ (32u * half);
  const unsigned vk0 = row7 * 256 + sh * 64 + pos * 16, vk1 = vk0 ^ 32u, vv = row7 * 256 + sh * 64 + ((pos ^ (2 * (row7 >> 2))) * 16);
  int cnt = 0;
  auto issue_w = [&](unsigned h, int s) {
    dma_tile8<false, 8 * HD * 2>(lb + WRING + (s & 3) * 8192, s * 32 * (HD * 2) + h * (DH * 2), vw0, vw1, srdW);
    return cnt += 8;
  };
  auto issue_x = [&](int e) {
    dma_tile9<16 * DM * 2>(((e & 1) ? XB : XA) + half * 1024, e * 128 + half * 8 * (DM * 2), vx, srdX);
    return cnt += 9;
  };
  auto issue_k = [&](unsigned h, int t) {
    const unsigned q = t & 3, lo = q == 0 ? kslot(0) : q == 1 ? kslot(1) : q == 2 ? kslot(2) : kslot(3);
    dma_tile8<true, 2048>(lb + lo, h * (SK * DH * 2) + t * 8192, vk0, vk1, srdK);
    return cnt += 8;
  };
  auto issue_v = [&](unsigned h, int t) {
    const unsigned q = t & 3, lo = q == 0 ? vslot(0) : q == 1 ? vslot(1) : q == 2 ? vslot(2) : vslot(3);
    dma_tile8<true, 2048>(lb + lo, h * (SK * DH * 2) + t * 8192, vv, vv, srdV);
    return cnt += 8;
  };
  unsigned h = 8 * hg0 + m;
  unsigned tnext = 0;
  int kx = 0;
  int n0, n1, n2, n3;
  issue_w(h, 0); n0 = issue_x(0); n1 = issue_w(h, 1); issue_w(h, 2);
  ol.init(op, wo, m, g0, half ^ 1, rot);
#pragma unroll 1
  for (int r = 0; r < 2; ++r) {
    const unsigned hn = 8 * ((hg0 + 2) & 3) + m;
    wait_newer(cnt - n0); RBAR();
    n2 = issue_x(1);
    if (r == 0) issue_w(h, 3);
    n3 = cnt;
#pragma unroll 1
    for (int G = 1; G < 15; ++G) {
      wait_newer(cnt - n1); RBAR();
      n1 = n2; n2 = n3;
      issue_w(h, G + 3);
      n3 = cnt;
      if (r == 0) { if (G == 8) kx = issue_k(h, 0); if (G == 11) issue_k(h, 1); if (G == 14) issue_v(h, 0); }
    }
    int a0 = cnt, a1, a2;
    wait_newer(cnt - n1); RBAR();
    issue_k(h, 2);
    wait_newer(cnt - n2); RBAR();
    a1 = issue_v(h, 1);
    wait_newer(cnt - n3); RBAR();
    issue_k(h, 3);
    wait_newer(cnt - kx); RBAR();
    a2 = issue_v(h, 2);
    RBAR();
#pragma unroll 1
    for (int i = 0; i < 16; ++i) {
      wait_newer(cnt - a0);
      {
        unsigned now = (unsigned)__builtin_amdgcn_s_memrealtime();
        while (i > 0 && i < 14 && (int)(tnext - now) > 0) { __builtin_amdgcn_s_sleep(1); now = (unsigned)__builtin_amdgcn_s_memrealtime(); }
        tnext = now + STEP_TICKS; }
      RBAR();
      a0 = a1; a1 = a2;
      if (i < 12) issue_k(h, i + 4);
      if (i < 13) issue_v(h, i + 3);
      a2 = cnt;
      if (r == 0) {
        if (i == 12) issue_k(hn, 0);
        if (i == 13) { issue_k(hn, 1); issue_v(hn, 0); }
        if (i == 14) { issue_x(0); issue_w(hn, 2); issue_w(hn, 3); }
        if (i == 15) { n0 = issue_w(hn, 0); n1 = issue_w(hn, 1); }
      } else {
        if (i == 13 && ol.isw) { ol.issue(0, 0); ol.issue(0, 1); ol.issue(1, 0); ol.issue(1, 1); cnt += 36; }
        if (i == 14 && !ol.isw) { ol.issue(0, 0); ol.issue(1, 0); cnt += 18; }
      }
    }
    h = hn;
  }
}

template <int NQT, int NNT> struct OSet { bf16x8 w[NNT], ol; };
template <int NQT, int NNT, bool RD, bool Z> DI void o_unit(f32x4 (&acc)[NQT][NNT], bf16x8 (&of)[NQT - 1], const OSet<NQT, NNT>& cs, OSet<NQT, NNT>& ns, unsigned oa, unsigned wa0, unsigned wa1) {
#pragma unroll
  for (int q = 0; q < NQT; ++q) {
#pragma unroll
    for (int n = 0; n < NNT; ++n) {
      if constexpr (Z) MFMA16_Z(acc[q][n], cs.w[n], q < NQT - 1 ? of[q < NQT - 1 ? q : 0] : cs.ol); else MFMA16_V(acc[q][n], cs.w[n], q < NQT - 1 ? of[q < NQT - 1 ? q : 0] : cs.ol);
      SBAR();
      if (RD && q == 0) { const unsigned a = ((n & 1) ? wa1 : wa0) + 64 * (n >> 1); ns.w[n] = cat8(ldstr(a), ldstr(a + 9216)); }
      if (RD && q == 1 && n == 1) ns.ol = lds128(oa + (NQT - 1) * 1024);
      SBAR();
    }
    if (RD && q < NQT - 1) { of[q] = lds128(oa + q * 1024); SBAR(); }
  }
}
template <int NQT, int NNT> DI void oproj_consumer(char* __restrict__ wsb, unsigned nb, unsigned kq, unsigned blk) {
  asm volatile("; oproj consumer %0 x %1" :: "i"(NQT), "i"(NNT) : "memory");
  constexpr unsigned qh = NQT == 4, q0 = 5 * qh;
  f32x4 acc[NQT][NNT];
  bf16x8 of[NQT - 1]; OSet<NQT, NNT> sa, sb;
  unsigned oa, wa0, wa1;
  { const unsigned lane = lane_id(), li = lane & 15, g = lane >> 4, n0 = (9 * nb + 1) >> 1;
    const unsigned rb = oslot_w(0) + 576 * (4 * g + (li >> 2)) + 8 * (lane & 3);
    oa = oslot_o(0) + q0 * 1024 + lane * 16;
    wa0 = rb + 64 * (n0 >> 1) + 32 * ((n0 ^ g) & 1); wa1 = rb + 64 * ((n0 + 1) >> 1) + 32 * ((n0 ^ g ^ 1) & 1); }
#define NEXTO(j) do { const unsigned d_ = oslot_w(((j) + 1) & 3) - oslot_w((j) & 3); oa += oslot_o(((j) + 1) & 3) - oslot_o((j) & 3); wa0 += d_; wa1 += d_; PIN(oa); PIN(wa0); PIN(wa1); } while (0)
  PIN(oa); PIN(wa0); PIN(wa1);
  BAR();
#pragma unroll
  for (int n = 0; n < NNT; ++n) { const unsigned a = ((n & 1) ? wa1 : wa0) + 64 * (n >> 1); sa.w[n] = cat8(ldstr(a), ldstr(a + 9216)); }
#pragma unroll
  for (int q = 0; q < NQT - 1; ++q) of[q] = lds128(oa + q * 1024);
  sa.ol = lds128(oa + (NQT - 1) * 1024);
  NEXTO(0);
  BAR(); o_unit<NQT, NNT, true, true>(acc, of, sa, sb, oa, wa0, wa1); NEXTO(1);
  WAITV(0);
#pragma unroll 1
  for (int j = 2; j < 32; j += 2) {
    BAR(); o_unit<NQT, NNT, true, false>(acc, of, sb, sa, oa, wa0, wa1); NEXTO(j);
    BAR(); o_unit<NQT, NNT, true, false>(acc, of, sa, sb, oa, wa0, wa1); NEXTO(j + 1);
  }
  o_unit<NQT, NNT, false, false>(acc, of, sb, sa, 0, 0, 0);
  asm volatile("s_nop 7\n\ts_nop 1" ::: "memory"); SBAR();
  const unsigned lane = lane_id();
  if (nb == kq) {
    unsigned ra = OPART + q0 * 5120 + lane * 16; PIN(ra);
#pragma unroll
    for (int q = 0; q < NQT; ++q)
#pragma unroll
      for (int n = 0; n < NNT; ++n) *(LAS f32x4*)(ra + (q * 5 + n) * 1024) = acc[q][n];
  } else {
    const __amdgpu_buffer_rsrc_t srdS = __builtin_amdgcn_make_buffer_rsrc((void*)(wsb + WS_X + (size_t)(blk * 4 + nb) * 46080), (short)0, 46080, 0x00020000);
#pragma unroll
    for (int q = 0; q < NQT; ++q)
#pragma unroll
      for (int u = 0; u < (NNT + 1) / 2; ++u) {
        const f32x4 a = acc[q][2 * u], c2 = acc[q][2 * u + 1 < NNT ? 2 * u + 1 : 2 * u];
        __builtin_amdgcn_raw_buffer_store_b128(u32x4{cvtpk(a[0], a[1]), cvtpk(a[2], a[3]), cvtpk(c2[0], c2[1]), cvtpk(c2[2], c2[3])}, srdS, lane * 16, ((q0 + q) * 3 + u) * 1024, 16);
      }
    WAITV(0);
  }
}
DI void oproj_finish(u16* __restrict__ outp, char* __restrict__ wsb, unsigned q, unsigned kq, unsigned blk) {
  const unsigned lane = lane_id(), li = lane & 15, g = lane >> 4, nnt = 5 - (kq & 1);
  BAR();
  unsigned* fl = (unsigned*)(wsb + WS_F);
  if (q == 8 && lane == 0) STA(fl + blk * 64 + 3, 1u);
  { const unsigned* pf = fl + ((blk & ~48u) + 16 * (lane & 3)) * 64 + 3;
    for (int spin = 0; spin < SPIN_CAP && !__all(LDA(pf) == 1u); ++spin) __builtin_amdgcn_s_sleep(1);
    __builtin_amdgcn_fence(__ATOMIC_ACQUIRE, "wavefront"); }
  f32x4 px[3][5];
  u32x4 pw[3][3];
#pragma unroll
  for (int p = 0; p < 3; ++p) {
    const unsigned pb = (blk & ~48u) + 16 * ((kq + 1 + p) & 3);
    const __amdgpu_buffer_rsrc_t srdP = __builtin_amdgcn_make_buffer_rsrc((void*)(wsb + WS_X + (size_t)(pb * 4 + kq) * 46080), (short)0, 46080, 0x00020000);
#pragma unroll
    for (int u = 0; u < 3; ++u) pw[p][u] = __builtin_amdgcn_raw_buffer_load_b128(srdP, lane * 16, (q * 3 + (u < 2 ? u : nnt - 3)) * 1024, 16);
  }
  SBAR();
#pragma unroll
  for (int p = 0; p < 3; ++p)
#pragma unroll
    for (int n = 0; n < 5; ++n) { const unsigned lo = pw[p][n >> 1][2 * (n & 1)], hi = pw[p][n >> 1][2 * (n & 1) + 1];
      px[p][n] = f32x4{__uint_as_float(lo << 16), __uint_as_float(lo & 0xffff0000u), __uint_as_float(hi << 16), __uint_as_float(hi & 0xffff0000u)}; }
  const unsigned tb = OPART + q * 5120;
  f32x4 own[5];
#pragma unroll
  for (int n = 0; n < 5; ++n) own[n] = *(const LAS f32x4*)(tb + (n < 4 ? n : nnt - 1) * 1024 + lane * 16);
#pragma unroll
  for (int n = 0; n < 5; ++n) {
    const f32x4 a = (own[n] + px[0][n]) + (px[1][n] + px[2][n]);
    *(LAS u32x2*)(tb + li * 176 + g * 8 + n * 32) = u32x2{cvtpk(a[0], a[1]), cvtpk(a[2], a[3])};
  }
#pragma unroll
  for (unsigned i = 0; i < 3; ++i) {
    const unsigned ch = i * 64 + lane, row = ch / 10, cc = ch - row * 10;
    if (row < 16 && cc < 2 * nnt) {
      *(u32x4*)(outp + (size_t)(16 * q + row) * (2 * DM) + cc * 8) = *(const LAS u32x4*)(tb + row * 176 + cc * 16);
    }
  }
}

__global__ __launch_bounds__(768, 3) void k_fused(const u16* __restrict__ xA, const u16* __restrict__ kA, const u16* __restrict__ vA, const u16* __restrict__ wqA, const u16* __restrict__ woA,
                                                  const u16* __restrict__ xB, const u16* __restrict__ kB, const u16* __restrict__ vB, const u16* __restrict__ wqB, const u16* __restrict__ woB,
                                                  char* __restrict__ wsb, u16* __restrict__ out) {
  extern __shared__ __attribute__((aligned(16))) char smem[];
  unsigned long long pc0; asm volatile("s_getpc_b64 %0" : "=s"(pc0));
  const unsigned wid12 = rfl(threadIdx.x >> 6), half = wid12 >= 6, wid = wid12 - 6 * half, lb = half * GRP;
  const unsigned blk = blockIdx.x;
  const unsigned idx = blk >> 3, pair = (blk & 7) * 4 + (idx >> 3), m = idx & 7, br = pair >> 4, b = pair & 15;
  const unsigned hg0 = (blk + half) & 3, kq = m >> 1, g0 = ((blk ^ kq) & 1) ? (blk + 1) & 3 : blk & 3;
  char* op = wsb + WS_O + (size_t)pair * NH * ITEM_B;
  const u16* kb = (br ? kB : kA) + (size_t)b * NH * SK * DH; const u16* vb = (br ? vB : vA) + (size_t)b * NH * SK * DH;
  Hs hs;
  if (wid < 4) {
    att_wave<2>(op, kb, vb, 2 * wid, m, hg0, lb, hs, nullptr);
    const unsigned t = half ? 3 - ((wid + 2) & 3) : wid, nb = 2 * half + (t & 1);
    if (t == 0) oproj_consumer<5, 5>(wsb, nb, kq, blk);
    else if (t == 1) oproj_consumer<5, 4>(wsb, nb, kq, blk);
    else if (t == 2) oproj_consumer<4, 5>(wsb, nb, kq, blk);
    else oproj_consumer<4, 4>(wsb, nb, kq, blk);
  } else if (wid == 4) {
    if (!half) hs_entry(hs, wsb, blk);
    else {
      const __amdgpu_buffer_rsrc_t srdT = __builtin_amdgcn_make_buffer_rsrc((char*)((br ? xB : xA) + (size_t)b * SQ * DM) + m * 20736, (short)0, 20736, 0x00020000);
      __builtin_amdgcn_s_sleep(10);
      {
        const unsigned l = lane_id(), n = idx * 17 + (l < 17 ? l : 0);
        asm volatile("s_mov_b32 m0, %0\n\ts_nop 3\n\tglobal_load_lds_dword %1, %2" :: "s"(vslot(3)), "v"(n * 128), "s"(pc0) : "memory"); }
#pragma unroll
      for (int p = 0; p < 3; ++p) { const unsigned n = p * 64 + lane_id(); dma4(vslot(3), (n < 162 ? n : 0) * 128, srdT); }
      {
        constexpr unsigned LN = (576 - 256) * 64 / 128;
        const __amdgpu_buffer_rsrc_t srdQ = __builtin_amdgcn_make_buffer_rsrc((char*)((blk & 1) ? wqB : wqA) + (size_t)256 * 8192 + (size_t)(blk >> 1) * LN * 128, (short)0, LN * 128, 0x00020000);
#pragma unroll
        for (unsigned p = 0; p < (LN + 63) / 64; ++p) { const unsigned n = p * 64 + lane_id(); dma4(vslot(3), (n < LN ? n : 0) * 128, srdQ); } }
    }
    att_wave<1>(op, kb, vb, 8, m, hg0, lb, hs, (br ? xB : xA) + (size_t)b * SQ * DM);
    if (half) {
      OLd ol; ol.init(op, br ? woB : woA, m, g0, 1, pair & 3);
      WAITV(0);
      ol.run(1);
    } else {
      WAITV(0);
      RBAR(); RBAR(); RBAR();
      if (lane_id() == 0) STA(hs.mine + 2, 1u);
#pragma unroll 1
      for (int j = 3; j < 8; ++j) RBAR();
      hs_post(hs.peer + 2, OBOX);
      RBAR(); RBAR();
      hs_wait_flag(hs, hs_take(OBOX), 2);
      RBAR(); RBAR(); RBAR();
      WAITV(0);
    }
    return;
  } else {
    OLd ol;
    loader(br ? wqB : wqA, (br ? xB : xA) + (size_t)b * SQ * DM, kb, vb, m, hg0, half, ol, op, br ? woB : woA, g0, pair & 3);
    ol.run(0);
    if (half) return;
  }
  oproj_finish(out + (size_t)b * SQ * (2 * DM) + br * DM + (m & 1) * 288 + 16 * ((9 * kq + 1) >> 1), wsb, wid < 4 ? wid + 4 * half : 8, kq, blk);
}

extern "C" void kernel_launch(void* const* d_in, const int* in_sizes, int n_in, void* d_out, int out_size, void* d_ws, size_t ws_size, hipStream_t stream) {
  (void)hipFuncSetAttribute((const void*)k_fused, hipFuncAttributeMaxDynamicSharedMemorySize, LDS_BYTES);
  k_fused<<<256, 768, LDS_BYTES, stream>>>((const u16*)d_in[0], (const u16*)d_in[1], (const u16*)d_in[2], (const u16*)d_in[3], (const u16*)d_in[4],
                                           (const u16*)d_in[5], (const u16*)d_in[6], (const u16*)d_in[7], (const u16*)d_in[8], (const u16*)d_in[9], (char*)d_ws, (u16*)d_out);
}
```

```cpp
#include <hip/hip_runtime.h>
#include <stdint.h>

typedef short    bf16x8 __attribute__((ext_vector_type(8)));
typedef short    s16x4  __attribute__((ext_vector_type(4)));
typedef float    f32x4  __attribute__((ext_vector_type(4)));
typedef float    f32x2  __attribute__((ext_vector_type(2)));
typedef __bf16   bf16x2 __attribute__((ext_vector_type(2)));
typedef unsigned u32x4  __attribute__((ext_vector_type(4)));
typedef unsigned u32x2  __attribute__((ext_vector_type(2)));
typedef unsigned short u16;
#define DI __device__ __forceinline__
#define LAS __attribute__((address_space(3)))
#define MFMA16(a, b, c) __builtin_amdgcn_mfma_f32_16x16x32_bf16(a, b, c, 0, 0, 0)
#define SBAR() __builtin_amdgcn_sched_barrier(0)
#define PIN(x) asm volatile("" : "+v"(x))
#define MFMA16_V(acc, a, b) asm volatile("v_mfma_f32_16x16x32_bf16 %0, %1, %2, %0" : "+v"(acc) : "v"(a), "v"(b))
#define MFMA16_Z(d, a, b) asm volatile("v_mfma_f32_16x16x32_bf16 %0, %1, %2, 0" : "=&v"(d) : "v"(a), "v"(b))
#define MFMA16_C(d, a, b, cc) asm volatile("v_mfma_f32_16x16x32_bf16 %0, %1, %2, %3" : "=&v"(d) : "v"(a), "v"(b), "v"(cc))

constexpr int NB = 16, SQ = 144, DM = 576, NH = 32, SK = 512, DH = 128, HD = NH * DH;
constexpr int ITEM_B = 9 * 4 * 1024;
constexpr size_t WS_O = 0;
constexpr size_t WS_X = (size_t)1024 * ITEM_B, WS_F = WS_X + (size_t)256 * 4 * 46080;
constexpr float QSCALE = 0.08838834764831845f * 1.4426950408889634f;

DI unsigned rfl(unsigned x) { return __builtin_amdgcn_readfirstlane(x); }
DI unsigned lane_id() { unsigned l; asm volatile("v_mbcnt_lo_u32_b32 %0, -1, 0\n\tv_mbcnt_hi_u32_b32 %0, -1, %0" : "=v"(l)); return l; }
DI unsigned cvtpk(float lo, float hi) { f32x2 v = {lo, hi}; bf16x2 b = __builtin_convertvector(v, bf16x2); return __builtin_bit_cast(unsigned, b); }
DI bf16x8 lds128(unsigned a) { return *(const LAS bf16x8*)a; }
DI s16x4 ldstr(unsigned a) { return __builtin_bit_cast(s16x4, __builtin_amdgcn_ds_read_tr16_b64_v4i16((LAS s16x4*)a)); }
DI bf16x8 cat8(s16x4 lo, s16x4 hi) { return __builtin_shufflevector(lo, hi, 0, 1, 2, 3, 4, 5, 6, 7); }
DI void dma4(unsigned m0v, unsigned voff, __amdgpu_buffer_rsrc_t srd) {
  asm volatile("s_mov_b32 m0, %0\n\ts_nop 3\n\tbuffer_load_dword %1, %2, 0 offen lds" :: "s"(m0v), "v"(voff), "s"(srd) : "memory");
}
#define TILE8_BODY(POL) \
  "s_nop 3\n\ts_mov_b32 m0, %1\n\ts_mov_b32 %0, %2\n\t" \
  "buffer_load_dwordx4 %3, %5, %0 offen " POL " lds\n\ts_add_u32 m0, m0, 896\n\ts_nop 0\n\tbuffer_load_dwordx4 %3, %5, %0 offen offset:128 " POL " lds\n\ts_add_u32 m0, m0, 1152\n\ts_add_u32 %0, %0, %6\n\t" \
  "buffer_load_dwordx4 %4, %5, %0 offen " POL " lds\n\ts_add_u32 m0, m0, 896\n\ts_nop 0\n\tbuffer_load_dwordx4 %4, %5, %0 offen offset:128 " POL " lds\n\ts_add_u32 m0, m0, 1152\n\ts_add_u32 %0, %0, %6\n\t" \
  "buffer_load_dwordx4 %3, %5, %0 offen " POL " lds\n\ts_add_u32 m0, m0, 896\n\ts_nop 0\n\tbuffer_load_dwordx4 %3, %5, %0 offen offset:128 " POL " lds\n\ts_add_u32 m0, m0, 1152\n\ts_add_u32 %0, %0, %6\n\t" \
  "buffer_load_dwordx4 %4, %5, %0 offen " POL " lds\n\ts_add_u32 m0, m0, 896\n\ts_nop 0\n\tbuffer_load_dwordx4 %4, %5, %0 offen offset:128 " POL " lds"
template <bool NT, int D2> DI void dma_tile8(unsigned lo, unsigned so, unsigned va, unsigned vb, __amdgpu_buffer_rsrc_t srd) {
  unsigned t;
  if constexpr (NT) asm volatile(TILE8_BODY("nt") : "=&s"(t) : "s"(lo), "s"(so), "v"(va), "v"(vb), "s"(srd), "i"(D2) : "memory", "scc");
  else              asm volatile(TILE8_BODY("")   : "=&s"(t) : "s"(lo), "s"(so), "v"(va), "v"(vb), "s"(srd), "i"(D2) : "memory", "scc");
}
template <int D2> DI void dma_tile9(unsigned lo, unsigned so, unsigned va, __amdgpu_buffer_rsrc_t srd) {
  unsigned t;
  asm volatile("s_nop 3\n\ts_mov_b32 m0, %1\n\ts_mov_b32 %0, %2\n\t.rept 9\n\t"
               "buffer_load_dwordx4 %3, %4, %0 offen lds\n\ts_add_u32 m0, m0, 2048\n\ts_add_u32 %0, %0, %5\n\t.endr"
               : "=&s"(t) : "s"(lo), "s"(so), "v"(va), "s"(srd), "i"(D2) : "memory", "scc");
}
#define WAITV(n) asm volatile("s_waitcnt vmcnt(" #n ")" ::: "memory")
#define BAR() do { asm volatile("" ::: "memory"); __builtin_amdgcn_s_waitcnt(0xC07F); asm volatile("s_barrier" ::: "memory"); } while (0)
#define RBAR() asm volatile("s_barrier" ::: "memory")
constexpr unsigned GRP = 65536, WRING = 3 * 8192, XSLOT = 18432, XB = 126976, XA = XB + XSLOT, LDS_BYTES = 163840;
constexpr unsigned kslot(int i) { return 8192 * ((i & 3) == 0 ? 0 : (i & 3) == 1 ? 1 : (i & 3) == 2 ? 5 : 3); }
constexpr unsigned vslot(int i) { return 8192 * ((i & 3) == 0 ? 2 : (i & 3) == 1 ? 6 : (i & 3) == 2 ? 4 : 7); }
static_assert(XA + XSLOT == LDS_BYTES, "LDS map");


DI float red_g_max(float m) {
  auto r = __builtin_amdgcn_permlane32_swap(__float_as_uint(m), __float_as_uint(m), false, false);
  m = fmaxf(__uint_as_float(r[0]), __uint_as_float(r[1]));
  auto s = __builtin_amdgcn_permlane16_swap(__float_as_uint(m), __float_as_uint(m), false, false);
  return fmaxf(__uint_as_float(s[0]), __uint_as_float(s[1]));
}
DI float red_g_sum(float m) {
  auto r = __builtin_amdgcn_permlane32_swap(__float_as_uint(m), __float_as_uint(m), false, false);
  m = __uint_as_float(r[0]) + __uint_as_float(r[1]);
  auto s = __builtin_amdgcn_permlane16_swap(__float_as_uint(m), __float_as_uint(m), false, false);
  return __uint_as_float(s[0]) + __uint_as_float(s[1]);
}

template <int NQ> struct AttC {
  f32x4 O[NQ][8]; bf16x8 Q[NQ][4]; f32x4 S[2][NQ][2]; f32x4 negm[NQ]; float l[NQ];
  unsigned kb, vb0, vb1;
};
template <int NQ> struct Tab;
template <> struct Tab<2> {
  static constexpr int NPRE = 6;
  static constexpr int pre[6] = {100, 101, 102, 103, 104, 105};
  static constexpr int gx[16][2] = {{106, 0}, {107, 0}, {200, 201}, {110, 0}, {202, 203}, {111, 0}, {112, 0}, {113, 0}, {114, 0}, {210, 211}, {115, 0}, {116, 0}, {117, 0}, {300, 310}, {212, 213}, {301, 311}};
  static constexpr int gy[16] = {302, 312, 303, 313, 304, 314, 305, 315, 306, 316, 307, 317, 0, 0, 0, 0};
  static constexpr int all[24] = {100, 101, 102, 103, 104, 105, 106, 107, 110, 200, 201, 202, 203, 111, 112, 113, 114, 115, 116, 117, 210, 211, 212, 213};
  static constexpr int ady[16] = {300, 310, 301, 311, 302, 312, 303, 313, 304, 314, 305, 315, 306, 316, 307, 317};
};
template <> struct Tab<1> {
  static constexpr int NPRE = 4;
  static constexpr int pre[4] = {100, 101, 102, 103};
  static constexpr int gx[8][2] = {{104, 0}, {105, 0}, {200, 201}, {106, 0}, {107, 0}, {300, 301}, {202, 203}, {302, 303}};
  static constexpr int gy[8] = {304, 305, 306, 307, 0, 0, 0, 0};
  static constexpr int all[12] = {100, 101, 102, 103, 104, 105, 106, 107, 200, 201, 202, 203};
  static constexpr int ady[8] = {300, 301, 302, 303, 304, 305, 306, 307};
};
template <int NQ, int P, bool QK> struct Step {
  static constexpr int X = P & 1;
  static constexpr unsigned ks_ = kslot(P + 1), vs_ = vslot(P);
  bf16x8 fr[4]; float e[NQ][8]; unsigned pw[NQ][4]; u32x4 Pw[NQ];
  template <int OP> DI void op(AttC<NQ>& c) {
    constexpr int ty = OP / 100, qt = (OP / 10) % 10, j = OP % 10;
    if constexpr (ty == 1) { e[qt][j] = c.S[X][qt][j >> 2][j & 3]; asm volatile("v_exp_f32 %0, %0" : "+v"(e[qt][j])); }
    if constexpr (ty == 2) asm volatile("v_cvt_pk_bf16_f32 %0, %1, %2" : "=v"(pw[qt][j]) : "v"(e[qt][2 * j]), "v"(e[qt][2 * j + 1]));
    if constexpr (ty == 3) asm volatile("v_add_f32 %0, %0, %1" : "+v"(c.l[qt]) : "v"(e[qt][j]));
  }
  template <int R> DI void rd(AttC<NQ>& c) {
    if constexpr (R < 8) fr[R & 3] = lds128(c.kb + ks_ + 4096 * (R & 1) + 512 * (R >> 1));
    else if constexpr (R < 16) {
      constexpr int dt = R - 8;
      const unsigned vb = ((dt & 1) ? c.vb1 : c.vb0) + vs_ + 512 * (dt >> 1);
      fr[R & 3] = cat8(ldstr(vb), ldstr(vb + 4096));
    }
    SBAR();
  }
  template <int I> DI void gx(AttC<NQ>& c) {
    constexpr int K = I / NQ, qt = I % NQ, s = K >> 1, kt = K & 1;
    if constexpr (s == 0) MFMA16_C(c.S[X ^ 1][qt][kt], fr[K & 3], c.Q[qt][s], c.negm[qt]); else MFMA16_V(c.S[X ^ 1][qt][kt], fr[K & 3], c.Q[qt][s]);
    if constexpr (qt == NQ - 1) { SBAR(); rd<K + 4>(c); }
    op<Tab<NQ>::gx[I][0]>(c); op<Tab<NQ>::gx[I][1]>(c);
    if constexpr (I + 1 < 8 * NQ) gx<I + 1>(c);
  }
  template <int I> DI void gy(AttC<NQ>& c) {
    constexpr int Dt = I / NQ, qt = I % NQ;
    MFMA16_V(c.O[qt][Dt], fr[Dt & 3], Pw[qt]);
    if constexpr (qt == NQ - 1) { SBAR(); rd<Dt + 12>(c); }
    op<(QK ? Tab<NQ>::gy[I] : Tab<NQ>::ady[I])>(c);
    if constexpr (I + 1 < 8 * NQ) gy<I + 1>(c);
  }
  template <int I> DI void pre(AttC<NQ>& c) {
    op<(QK ? Tab<NQ>::pre[I < Tab<NQ>::NPRE ? I : 0] : Tab<NQ>::all[I])>(c);
    if constexpr (I + 1 < (QK ? Tab<NQ>::NPRE : 12 * NQ)) pre<I + 1>(c);
  }
  DI void run(AttC<NQ>& c) {
    if constexpr (QK) {
      rd<0>(c); rd<1>(c); rd<2>(c); rd<3>(c);
      pre<0>(c);
      gx<0>(c);
    } else {
      rd<8>(c); rd<9>(c); rd<10>(c); rd<11>(c);
      pre<0>(c);
      asm volatile("s_nop 1");
    }
#pragma unroll
    for (int qt = 0; qt < NQ; ++qt) Pw[qt] = u32x4{pw[qt][0], pw[qt][1], pw[qt][2], pw[qt][3]};
    gy<0>(c);
  }
};

template <int NQ, int P, bool QK> DI void att_step(AttC<NQ>& c) { BAR(); SBAR();
  Step<NQ, P, QK> s; s.run(c); SBAR(); }

template <int NQ> DI void att_prologue(AttC<NQ>& c) {
#pragma unroll
  for (int s = 0; s < 4; ++s)
#pragma unroll
    for (int kt = 0; kt < 2; ++kt) {
      const bf16x8 kf = lds128(c.kb + 4096 * kt + 512 * s);
#pragma unroll
      for (int qt = 0; qt < NQ; ++qt) c.S[0][qt][kt] = MFMA16(kf, c.Q[qt][s], s == 0 ? (f32x4{0, 0, 0, 0}) : c.S[0][qt][kt]);
    }
#pragma unroll
  for (int qt = 0; qt < NQ; ++qt) {
    float mx = c.S[0][qt][0][0];
#pragma unroll
    for (int j = 1; j < 8; ++j) mx = fmaxf(mx, c.S[0][qt][j >> 2][j & 3]);
    mx = red_g_max(mx);
    c.negm[qt] = f32x4{-mx, -mx, -mx, -mx};
    c.S[0][qt][0] += c.negm[qt]; c.S[0][qt][1] += c.negm[qt];
    c.l[qt] = 0.f;
#pragma unroll
    for (int dt = 0; dt < 8; ++dt) c.O[qt][dt] = f32x4{0, 0, 0, 0};
  }
}
template <int NQ> DI void att_redo_true_max(AttC<NQ>& c, const u16* __restrict__ kh, const u16* __restrict__ vh) {
  const unsigned lane = lane_id(), li = lane & 15, g = lane >> 4;
  const u16* kp = kh + li * DH + g * 8;
  float mx[NQ];
#pragma unroll
  for (int qt = 0; qt < NQ; ++qt) mx[qt] = -__builtin_inff();
#pragma unroll 1
  for (int kt = 0; kt < SK / 16; ++kt) {
    f32x4 s4[NQ];
#pragma unroll
    for (int qt = 0; qt < NQ; ++qt) s4[qt] = f32x4{0, 0, 0, 0};
#pragma unroll
    for (int s = 0; s < 4; ++s) {
      const bf16x8 kf = *(const bf16x8*)(kp + kt * 16 * DH + s * 32);
#pragma unroll
      for (int qt = 0; qt < NQ; ++qt) s4[qt] = MFMA16(kf, c.Q[qt][s], s4[qt]);
    }
#pragma unroll
    for (int qt = 0; qt < NQ; ++qt) mx[qt] = fmaxf(fmaxf(fmaxf(mx[qt], s4[qt][0]), fmaxf(s4[qt][1], s4[qt][2])), s4[qt][3]);
  }
#pragma unroll
  for (int qt = 0; qt < NQ; ++qt) {
    mx[qt] = red_g_max(mx[qt]); c.l[qt] = 0.f;
#pragma unroll
    for (int dt = 0; dt < 8; ++dt) c.O[qt][dt] = f32x4{0, 0, 0, 0};
  }
#pragma unroll 1
  for (int st = 0; st < SK / 32; ++st) {
    u32x4 pw[NQ];
#pragma unroll
    for (int kt = 0; kt < 2; ++kt) {
      f32x4 s4[NQ];
#pragma unroll
      for (int qt = 0; qt < NQ; ++qt) s4[qt] = f32x4{-mx[qt], -mx[qt], -mx[qt], -mx[qt]};
#pragma unroll
      for (int s = 0; s < 4; ++s) {
        const bf16x8 kf = *(const bf16x8*)(kp + (2 * st + kt) * 16 * DH + s * 32);
#pragma unroll
        for (int qt = 0; qt < NQ; ++qt) s4[qt] = MFMA16(kf, c.Q[qt][s], s4[qt]);
      }
#pragma unroll
      for (int qt = 0; qt < NQ; ++qt) {
        float e[4];
#pragma unroll
        for (int j = 0; j < 4; ++j) { e[j] = __builtin_amdgcn_exp2f(s4[qt][j]); c.l[qt] += e[j]; }
        pw[qt][2 * kt] = cvtpk(e[0], e[1]); pw[qt][2 * kt + 1] = cvtpk(e[2], e[3]);
      }
    }
    const u16* vp = vh + (32 * st + 4 * g) * DH + li;
#pragma unroll
    for (int dt = 0; dt < 8; ++dt) {
      bf16x8 vf;
#pragma unroll
      for (int j = 0; j < 8; ++j) vf[j] = (short)vp[(16 * (j >> 2) + (j & 3)) * DH + 16 * dt];
#pragma unroll
      for (int qt = 0; qt < NQ; ++qt) c.O[qt][dt] = MFMA16(vf, __builtin_bit_cast(bf16x8, pw[qt]), c.O[qt][dt]);
      SBAR();
    }
  }
#pragma unroll
  for (int qt = 0; qt < NQ; ++qt) c.l[qt] = red_g_sum(c.l[qt]);
}
template <int NQ> DI void att_epilogue(AttC<NQ>& c, char* ob, const u16* kh, const u16* vh) {
  bool bad = false;
#pragma unroll
  for (int qt = 0; qt < NQ; ++qt) { c.l[qt] = red_g_sum(c.l[qt]); bad |= !(c.l[qt] < 1099511627776.0f);     }
  if (__builtin_expect(__any(bad), 0)) att_redo_true_max<NQ>(c, kh, vh);
#pragma unroll
  for (int qt = 0; qt < NQ; ++qt) {
    const float inv = __builtin_amdgcn_rcpf(c.l[qt]);
#pragma unroll
    for (int s = 0; s < 4; ++s) {
      const f32x4 a = c.O[qt][2 * s] * inv, bb = c.O[qt][2 * s + 1] * inv;
      u32x4 w = {cvtpk(a[0], a[1]), cvtpk(a[2], a[3]), cvtpk(bb[0], bb[1]), cvtpk(bb[2], bb[3])};
      asm volatile("global_store_dwordx4 %0, %1, off sc1\n\ts_nop 1" :: "v"(ob + (qt * 4 + s) * 1024), "v"(w) : "memory");
    }
  }
}

DI bf16x8 q_frag(const f32x4& t0, const f32x4& t1) {
  const f32x4 u0 = t0 * QSCALE, u1 = t1 * QSCALE;
  unsigned a0 = cvtpk(u0[0], u0[1]), a1 = cvtpk(u0[2], u0[3]), b0 = cvtpk(u1[0], u1[1]), b1 = cvtpk(u1[2], u1[3]);
  auto r0 = __builtin_amdgcn_permlane32_swap(a0, b0, false, false);
  auto r1 = __builtin_amdgcn_permlane32_swap(a1, b1, false, false);
  auto s0 = __builtin_amdgcn_permlane16_swap(r0[0], r0[1], false, false);
  auto s1 = __builtin_amdgcn_permlane16_swap(r1[0], r1[1], false, false);
  u32x4 w = {s0[0], s1[0], s0[1], s1[1]};
  return __builtin_bit_cast(bf16x8, w);
}
template <int NR, int WS, int XO, bool Z = false>
DI void q_unit(f32x4 (&qa)[NR][4], const bf16x8 (&wc)[4], bf16x8 (&wn)[4], bf16x8 (&xc)[NR], const bf16x8& lc, bf16x8 (&xn)[NR], bf16x8& ln, unsigned w0, unsigned w1, const unsigned (&xa)[3]) {
#define XADDR(row) (xa[(row) >> 1] + ((row) & 1) * 2048 + XO)
#pragma unroll
  for (int q = 0; q < NR; ++q) {
#pragma unroll
    for (int d = 0; d < 4; ++d) {
      if constexpr (Z) MFMA16_Z(qa[q][d], wc[d], q < NR - 1 ? xc[q] : lc); else MFMA16_V(qa[q][d], wc[d], q < NR - 1 ? xc[q] : lc);
      SBAR();
      const int i = 4 * q + d;
      if (WS >= 0 && i < 8 && !(i & 1)) { const int u = i >> 1; const unsigned a = ((u & 1) ? w1 : w0) + 8192 * WS + 512 * (u >> 1); wn[u] = cat8(ldstr(a), ldstr(a + 256)); }
      if (XO >= 0 && (i & 1) && (i >> 1) < NR) xn[i >> 1] = lds128(XADDR(i >> 1));
      if (XO >= 0 && i == 10) ln = lds128(XADDR(NR - 1) + 512);
      SBAR();
    }
    if (XO >= 0 && q < NR - 1) { xc[q] = lds128(XADDR(q) + 512); SBAR(); }
  }
#undef XADDR
}

#define LDA(p) __hip_atomic_load((p), __ATOMIC_RELAXED, __HIP_MEMORY_SCOPE_AGENT)
#define STA(p, v) __hip_atomic_store((p), (v), __ATOMIC_RELAXED, __HIP_MEMORY_SCOPE_AGENT)
constexpr int SPIN_CAP = 1 << 21;
struct Hs { unsigned* mine; unsigned* peer; unsigned me, x, e; bool ok; };
DI void hs_post(const unsigned* p, unsigned box) { asm volatile("s_mov_b32 m0, %0\n\ts_nop 3\n\tglobal_load_lds_dword %1, off sc1" :: "s"(box), "v"(p) : "memory"); }
DI unsigned hs_take(unsigned box) { WAITV(0); return *(volatile LAS unsigned*)(box + 4 * lane_id()); }
DI void hs_entry(Hs& h, char* wsb, unsigned blk) {
  const unsigned lane = lane_id();
  h.me = (blk >> 3) & 7;
  h.mine = (unsigned*)(wsb + WS_F) + blk * 64;
  h.peer = (unsigned*)(wsb + WS_F) + ((blk & ~56u) + 8 * (lane & 7)) * 64;
  if (lane >= 1 && lane <= 3) STA(h.mine + lane, 0u);
  const unsigned y0 = LDA(h.peer + 32 + h.me);
  unsigned x = rfl(y0) + 1;
  while (__any(y0 == x)) ++x;
  h.x = x; h.e = 0; h.ok = false;
  WAITV(0);
  if (lane == 0) STA(h.mine, x);
}
DI void hs_wait_flag(const Hs& h, unsigned v, int word) {
  for (int spin = 0; !__all(v == 1u) && spin < SPIN_CAP; ++spin) { __builtin_amdgcn_s_sleep(2); v = LDA(h.peer + word); }
  __builtin_amdgcn_fence(__ATOMIC_ACQUIRE, "agent");
}
constexpr unsigned HBOX = 161792;
DI void hs_hook(Hs& h, int k) {
  const unsigned lane = lane_id();
  if (k == 0) hs_post(h.peer, HBOX);
  else if (k == 1) { h.e = hs_take(HBOX); if (lane < 8) STA(h.mine + 32 + lane, h.e); }
  else if (k == 4) hs_post(h.peer + 32 + h.me, HBOX);
  else if (k == 5) { h.ok = __all(hs_take(HBOX) == h.x); hs_post(h.peer, HBOX); }
  else if (k == 6) { const unsigned e = hs_take(HBOX); if (lane < 8 && e != h.e) STA(h.mine + 32 + lane, e); hs_post(h.peer + 1, HBOX); }
  else if (k == 7) {
    unsigned f = hs_take(HBOX);
    if (!h.ok) {
      for (int spin = 0; spin < SPIN_CAP; ++spin) {
        const unsigned e = LDA(h.peer);
        if (lane < 8) STA(h.mine + 32 + lane, e);
        if (__all(LDA(h.peer + 32 + h.me) == h.x)) break;
        __builtin_amdgcn_s_sleep(2);
      }
      WAITV(0);
      const unsigned e = LDA(h.peer);
      if (lane < 8) STA(h.mine + 32 + lane, e);
      f = 0;
    }
    hs_wait_flag(h, f, 1);
  }
}

constexpr unsigned EX = 122880;
template <int NR> DI void q_phase(bf16x8 (&own)[2][2], unsigned wid, unsigned lb) {
  asm volatile("; q phase %0" :: "i"(NR) : "memory");
  const unsigned lane = lane_id(), li = lane & 15, g = lane >> 4, pos = lane & 3, tq = li >> 2;
  unsigned w0 = lb + WRING + 1024 * (wid & 1) + 2048 * g + 64 * tq + 32 * (g & 1) + 8 * pos, w1 = w0 ^ 32u;
  const unsigned xl = XB + 1024 * (li >> 3) + 64 * (li & 7) + 16 * (g ^ (2 * (li >> 3)));
  unsigned xa[3] = {xl + wid * 4096, xl + (wid ^ 1) * 4096, xl + 16384};
  bf16x8 w[4], v[4], xA[NR], xB[NR], l0, l1;
  f32x4 qa[NR][4];
  PIN(w0); PIN(w1); PIN(xa[0]); PIN(xa[1]); if (NR == 5) PIN(xa[2]);
  BAR();
#pragma unroll
  for (int u = 0; u < 4; ++u) { const unsigned a = ((u & 1) ? w1 : w0) + 512 * (u >> 1); w[u] = cat8(ldstr(a), ldstr(a + 256)); }
#pragma unroll
  for (int q = 0; q < NR; ++q) { const unsigned a = xa[q >> 1] + (q & 1) * 2048 + XSLOT; xB[q] = lds128(a); if (q < NR - 1) xA[q] = lds128(a + 512); else l0 = lds128(a + 512); }
  BAR(); q_unit<NR, 1, -1, true>(qa, w, v, xB, xB[NR - 1], xA, l1, w0, w1, xa);
#pragma unroll 1
  for (int n = 0; n < 4; ++n) {
    WAITV(0);
    BAR(); q_unit<NR, 2, 0>(qa, v, w, xA, l0, xB, l1, w0, w1, xa);
    BAR(); q_unit<NR, 3, -1>(qa, w, v, xB, xB[NR - 1], xA, l0, w0, w1, xa);
    BAR(); q_unit<NR, 0, XSLOT>(qa, v, w, xA, l1, xB, l0, w0, w1, xa);
    BAR(); q_unit<NR, 1, -1>(qa, w, v, xB, xB[NR - 1], xA, l1, w0, w1, xa);
  }
  BAR(); q_unit<NR, -1, -1>(qa, v, w, xA, l0, xB, l1, w0, w1, xa);
  asm volatile("s_nop 7" ::: "memory"); SBAR();
  unsigned ea = EX + (lb ? 0 : 20480) + lane * 16; PIN(ea);
#pragma unroll
  for (int s = 0; s < 2; ++s) {
    *(LAS bf16x8*)(ea + wid * 4096 + s * 1024) = q_frag(qa[2][2 * s], qa[2][2 * s + 1]);
    *(LAS bf16x8*)(ea + wid * 4096 + 2048 + s * 1024) = q_frag(qa[3][2 * s], qa[3][2 * s + 1]);
    if constexpr (NR == 5) *(LAS bf16x8*)(ea + 16384 + wid * 2048 + s * 1024) = q_frag(qa[4][2 * s], qa[4][2 * s + 1]);
  }
#pragma unroll
  for (int q = 0; q < 2; ++q)
#pragma unroll
    for (int s = 0; s < 2; ++s) own[q][s] = q_frag(qa[q][2 * s], qa[q][2 * s + 1]);
}

template <int NQ> DI void att_wave(char* ows_pair, const u16* kbase, const u16* vbase, unsigned qt0, unsigned m, unsigned hg0, unsigned lb, Hs& hs, const u16* x) {
  asm volatile("; attention wave NQ=%0" :: "i"(NQ) : "memory");
#pragma unroll 1
  for (int r = 0; r < 2; ++r) {
    AttC<NQ> c;
    if constexpr (NQ == 2) {
      bf16x8 own[2][2];
      if (qt0 < 4) q_phase<5>(own, qt0 >> 1, lb); else q_phase<4>(own, qt0 >> 1, lb);
      unsigned ea = EX + (lb ? 0 : 20480) + (qt0 ^ 2) * 2048 + lane_id() * 16; PIN(ea);
      BAR();
      const bool hi = qt0 & 2;
#pragma unroll
      for (int q = 0; q < 2; ++q)
#pragma unroll
        for (int s = 0; s < 2; ++s) { const bf16x8 rc = lds128(ea + q * 2048 + s * 1024); c.Q[q][s] = hi ? rc : own[q][s]; c.Q[q][2 + s] = hi ? own[q][s] : rc; }
    } else {
      const __amdgpu_buffer_rsrc_t srdX = __builtin_amdgcn_make_buffer_rsrc((void*)x, (short)0, SQ * DM * 2, 0x00020000);
      const unsigned lane = lane_id(), half = lb != 0, vx = ((((lane >> 2) & 7) + 8 * half) * (DM * 2) + (lane >> 5) * 64 + (lane & 3) * 16) ^ (32u * half);
      BAR();
#pragma unroll 1
      for (int n = 1; n < 19; n += 2) {
        BAR();
        if (n < 14) { dma_tile9<16 * DM * 2>(((n & 2) ? XB : XA) + half * 1024, ((n + 3) >> 1) * 128, vx, srdX); WAITV(9); } else WAITV(0);
        BAR();         if (n == 1 && lb == 0 && r == 1 && lane == 0) STA(hs.mine + 1, 1u);
      }
      BAR();
      unsigned ea = EX + (lb ? 0 : 20480) + 16384 + lane_id() * 16; PIN(ea);
#pragma unroll
      for (int s = 0; s < 4; ++s) c.Q[0][s] = lds128(ea + s * 1024);
      __builtin_amdgcn_s_waitcnt(0xC07F);
    }
    { const unsigned lane = lane_id(), li = lane & 15, g = lane >> 4;
      c.kb = lb + 2048 * (li >> 3) + 64 * (li & 7) + 16 * (g ^ (2 * (li >> 3)));
      c.vb0 = lb + 2048 * (g >> 1) + 64 * (4 * (g & 1) + (li >> 2)) + 32 * (g & 1) + 8 * (lane & 3); c.vb1 = c.vb0 ^ 32u;
      PIN(c.kb); PIN(c.vb0); PIN(c.vb1); }
    if (NQ == 1 && lb == 0) hs_hook(hs, 4 * r);
    att_prologue<NQ>(c);
    SBAR();
#pragma unroll 1
    for (int ii = 0; ii < 3; ++ii) { att_step<NQ, 0, true>(c); att_step<NQ, 1, true>(c); att_step<NQ, 2, true>(c); att_step<NQ, 3, true>(c);
      if (NQ == 1 && lb == 0) hs_hook(hs, 4 * r + 1 + ii); }
    att_step<NQ, 0, true>(c); att_step<NQ, 1, true>(c);
    if (NQ == 1) WAITV(0);
    att_step<NQ, 2, true>(c);
#pragma unroll
    for (int qt = 0; qt < NQ; ++qt) asm volatile("" :: "v"(c.negm[qt]));
    att_step<NQ, 3, false>(c);
    asm volatile("s_nop 7\n\ts_nop 1" ::: "memory"); SBAR();
    const unsigned h = 8 * ((hg0 + 2 * r) & 3) + m;
    att_epilogue<NQ>(c, ows_pair + (size_t)h * ITEM_B + qt0 * 4096 + lane_id() * 16, kbase + (size_t)h * SK * DH, vbase + (size_t)h * SK * DH);
  }
}

constexpr unsigned STEP_TICKS = 106;
DI void wait_newer(int n) {
  if (n >= 32) { if (n >= 40) WAITV(40); else WAITV(32); }
  else if (n >= 16) { if (n >= 24) WAITV(24); else WAITV(16); }
  else if (n >= 8) WAITV(8); else WAITV(0);
}
DI unsigned oslot_o(unsigned s) { return s == 0 ? 131072 : GRP - 9216 + 9216 * s; }
DI unsigned oslot_w(unsigned s) { return s == 0 ? 140288 : 18432 * (s - 1); }
constexpr unsigned OPART = 93184;
constexpr unsigned OBOX = 55296;
struct OLd {
  __amdgpu_buffer_rsrc_t srd; unsigned vo[9], h0, isw, rot;
  DI void init(const char* op, const u16* wo, unsigned m, unsigned g0, unsigned isw_, unsigned rot_) {
    const unsigned lane = lane_id(); h0 = 8 * g0 + (m & 4); isw = isw_; rot = rot_;
    srd = isw ? __builtin_amdgcn_make_buffer_rsrc((void*)wo, (short)0, HD * DM * 2, 0x00020000) : __builtin_amdgcn_make_buffer_rsrc((void*)op, (short)0, NH * ITEM_B, 0x00020000);
#pragma unroll
    for (int p = 0; p < 9; ++p) { const unsigned n = 64 * p + lane, row = n / 36, ch = n - 36 * row;
      vo[p] = isw ? row * (DM * 2) + ((ch ^ ((row >> 1) & 2)) * 16) + (m & 1) * 576 : p * 4096 + lane * 16; }
  }
#define OPIECE(k) "buffer_load_dwordx4 %" #k ", %1, %2 offen lds\n\ts_add_u32 m0, m0, 1024\n\ts_nop 0\n\t"
  DI void issue(int j, unsigned part) const {
    const unsigned h = ((h0 + (j & 16)) & 31) + (((j >> 2) + rot) & 3), s = j & 3;
    const unsigned so = isw ? ((h * 4 + s) * 32 + 16 * part) * (DM * 2) : h * ITEM_B + s * 1024, lo = isw ? oslot_w(j & 3) + part * 9216 : oslot_o(j & 3);
    asm volatile("s_mov_b32 m0, %0\n\ts_nop 3\n\t" OPIECE(3) OPIECE(4) OPIECE(5) OPIECE(6) OPIECE(7) OPIECE(8) OPIECE(9) OPIECE(10) OPIECE(11)
                 :: "s"(lo), "s"(srd), "s"(so), "v"(vo[0]), "v"(vo[1]), "v"(vo[2]), "v"(vo[3]), "v"(vo[4]), "v"(vo[5]), "v"(vo[6]), "v"(vo[7]), "v"(vo[8]) : "memory", "scc");
  }
  DI void run(unsigned part) const {
#pragma unroll 1
    for (int j = 0; j < 32; ++j) {
      if (j == 0 || j == 30) WAITV(9); else if (j < 30) WAITV(18); else WAITV(0);
      RBAR();
      if (j == 0) issue(2, part);
      if (j + 3 < 32) issue(j + 3, part);
    }
  }
};

DI void loader(const u16* wq, const u16* x, const u16* kbase, const u16* vbase, unsigned m, unsigned hg0, unsigned half, OLd& ol, const char* op, const u16* wo, unsigned g0, unsigned rot) {
  const unsigned lane = lane_id(), row7 = (lane >> 2) & 7, sh = lane >> 5, pos = lane & 3, lb = half * GRP;
  const __amdgpu_buffer_rsrc_t srdW = __builtin_amdgcn_make_buffer_rsrc((void*)wq, (short)0, DM * HD * 2, 0x00020000);
  const __amdgpu_buffer_rsrc_t srdX = __builtin_amdgcn_make_buffer_rsrc((void*)x, (short)0, SQ * DM * 2, 0x00020000);
  const __amdgpu_buffer_rsrc_t srdK = __builtin_amdgcn_make_buffer_rsrc((void*)kbase, (short)0, NH * SK * DH * 2, 0x00020000);
  const __amdgpu_buffer_rsrc_t srdV = __builtin_amdgcn_make_buffer_rsrc((void*)vbase, (short)0, NH * SK * DH * 2, 0x00020000);
  const unsigned vw0 = row7 * (HD * 2) + sh * 64 + pos * 16, vw1 = vw0 ^ 32u;
  const unsigned vx = (row7 * (DM * 2) + sh * 64 + pos * 16) ^ (32u * half);
  const unsigned vk0 = row7 * 256 + sh * 64 + pos * 16, vk1 = vk0 ^ 32u, vv = row7 * 256 + sh * 64 + ((pos ^ (2 * (row7 >> 2))) * 16);
  int cnt = 0;
  auto issue_w = [&](unsigned h, int s) {
    dma_tile8<false, 8 * HD * 2>(lb + WRING + (s & 3) * 8192, s * 32 * (HD * 2) + h * (DH * 2), vw0, vw1, srdW);
    return cnt += 8;
  };
  auto issue_x = [&](int e) {
    dma_tile9<16 * DM * 2>(((e & 1) ? XB : XA) + half * 1024, e * 128 + half * 8 * (DM * 2), vx, srdX);
    return cnt += 9;
  };
  auto issue_k = [&](unsigned h, int t) {
    const unsigned q = t & 3, lo = q == 0 ? kslot(0) : q == 1 ? kslot(1) : q == 2 ? kslot(2) : kslot(3);
    dma_tile8<true, 2048>(lb + lo, h * (SK * DH * 2) + t * 8192, vk0, vk1, srdK);
    return cnt += 8;
  };
  auto issue_v = [&](unsigned h, int t) {
    const unsigned q = t & 3, lo = q == 0 ? vslot(0) : q == 1 ? vslot(1) : q == 2 ? vslot(2) : vslot(3);
    dma_tile8<true, 2048>(lb + lo, h * (SK * DH * 2) + t * 8192, vv, vv, srdV);
    return cnt += 8;
  };
  unsigned h = 8 * hg0 + m;
  unsigned tnext = 0;
  int kx = 0;
  int n0, n1, n2, n3;
  issue_w(h, 0); n0 = issue_x(0); n1 = issue_w(h, 1); issue_w(h, 2);
  ol.init(op, wo, m, g0, half ^ 1, rot);
#pragma unroll 1
  for (int r = 0; r < 2; ++r) {
    const unsigned hn = 8 * ((hg0 + 2) & 3) + m;
    wait_newer(cnt - n0); RBAR();
    n2 = issue_x(1);
    if (r == 0) issue_w(h, 3);
    n3 = cnt;
#pragma unroll 1
    for (int G = 1; G < 15; ++G) {
      wait_newer(cnt - n1); RBAR();
      n1 = n2; n2 = n3;
      issue_w(h, G + 3);
      n3 = cnt;
      if (r == 0) { if (G == 8) kx = issue_k(h, 0); if (G == 11) issue_k(h, 1); if (G == 14) issue_v(h, 0); }
    }
    int a0 = cnt, a1, a2;
    wait_newer(cnt - n1); RBAR();
    issue_k(h, 2);
    wait_newer(cnt - n2); RBAR();
    a1 = issue_v(h, 1);
    wait_newer(cnt - n3); RBAR();
    issue_k(h, 3);
    wait_newer(cnt - kx); RBAR();
    a2 = issue_v(h, 2);
    RBAR();
#pragma unroll 1
    for (int i = 0; i < 16; ++i) {
      wait_newer(cnt - a0);
      {
        unsigned now = (unsigned)__builtin_amdgcn_s_memrealtime();
        while (i > 0 && i < 14 && (int)(tnext - now) > 0) { __builtin_amdgcn_s_sleep(1); now = (unsigned)__builtin_amdgcn_s_memrealtime(); }
        tnext = now + STEP_TICKS; }
      RBAR();
      a0 = a1; a1 = a2;
      if (i < 12) issue_k(h, i + 4);
      if (i < 13) issue_v(h, i + 3);
      a2 = cnt;
      if (r == 0) {
        if (i == 12) issue_k(hn, 0);
        if (i == 13) { issue_k(hn, 1); issue_v(hn, 0); }
        if (i == 14) { issue_x(0); issue_w(hn, 2); issue_w(hn, 3); }
        if (i == 15) { n0 = issue_w(hn, 0); n1 = issue_w(hn, 1); }
      } else {
        if (i == 13 && ol.isw) { ol.issue(0, 0); ol.issue(0, 1); ol.issue(1, 0); ol.issue(1, 1); cnt += 36; }
        if (i == 14 && !ol.isw) { ol.issue(0, 0); ol.issue(1, 0); cnt += 18; }
      }
    }
    h = hn;
  }
}

template <int NQT, int NNT> struct OSet { bf16x8 w[NNT], ol; };
template <int NQT, int NNT, bool RD, bool Z> DI void o_unit(f32x4 (&acc)[NQT][NNT], bf16x8 (&of)[NQT - 1], const OSet<NQT, NNT>& cs, OSet<NQT, NNT>& ns, unsigned oa, unsigned wa0, unsigned wa1) {
#pragma unroll
  for (int q = 0; q < NQT; ++q) {
#pragma unroll
    for (int n = 0; n < NNT; ++n) {
      if constexpr (Z) MFMA16_Z(acc[q][n], cs.w[n], q < NQT - 1 ? of[q < NQT - 1 ? q : 0] : cs.ol); else MFMA16_V(acc[q][n], cs.w[n], q < NQT - 1 ? of[q < NQT - 1 ? q : 0] : cs.ol);
      SBAR();
      if (RD && q == 0) { const unsigned a = ((n & 1) ? wa1 : wa0) + 64 * (n >> 1); ns.w[n] = cat8(ldstr(a), ldstr(a + 9216)); }
      if (RD && q == 1 && n == 1) ns.ol = lds128(oa + (NQT - 1) * 1024);
      SBAR();
    }
    if (RD && q < NQT - 1) { of[q] = lds128(oa + q * 1024); SBAR(); }
  }
}
template <int NQT, int NNT> DI void oproj_consumer(char* __restrict__ wsb, unsigned nb, unsigned kq, unsigned blk) {
  asm volatile("; oproj consumer %0 x %1" :: "i"(NQT), "i"(NNT) : "memory");
  constexpr unsigned qh = NQT == 4, q0 = 5 * qh;
  f32x4 acc[NQT][NNT];
  bf16x8 of[NQT - 1]; OSet<NQT, NNT> sa, sb;
  unsigned oa, wa0, wa1;
  { const unsigned lane = lane_id(), li = lane & 15, g = lane >> 4, n0 = (9 * nb + 1) >> 1;
    const unsigned rb = oslot_w(0) + 576 * (4 * g + (li >> 2)) + 8 * (lane & 3);
    oa = oslot_o(0) + q0 * 1024 + lane * 16;
    wa0 = rb + 64 * (n0 >> 1) + 32 * ((n0 ^ g) & 1); wa1 = rb + 64 * ((n0 + 1) >> 1) + 32 * ((n0 ^ g ^ 1) & 1); }
#define NEXTO(j) do { const unsigned d_ = oslot_w(((j) + 1) & 3) - oslot_w((j) & 3); oa += oslot_o(((j) + 1) & 3) - oslot_o((j) & 3); wa0 += d_; wa1 += d_; PIN(oa); PIN(wa0); PIN(wa1); } while (0)
  PIN(oa); PIN(wa0); PIN(wa1);
  BAR();
#pragma unroll
  for (int n = 0; n < NNT; ++n) { const unsigned a = ((n & 1) ? wa1 : wa0) + 64 * (n >> 1); sa.w[n] = cat8(ldstr(a), ldstr(a + 9216)); }
#pragma unroll
  for (int q = 0; q < NQT - 1; ++q) of[q] = lds128(oa + q * 1024);
  sa.ol = lds128(oa + (NQT - 1) * 1024);
  NEXTO(0);
  BAR(); o_unit<NQT, NNT, true, true>(acc, of, sa, sb, oa, wa0, wa1); NEXTO(1);
  WAITV(0);
#pragma unroll 1
  for (int j = 2; j < 32; j += 2) {
    BAR(); o_unit<NQT, NNT, true, false>(acc, of, sb, sa, oa, wa0, wa1); NEXTO(j);
    BAR(); o_unit<NQT, NNT, true, false>(acc, of, sa, sb, oa, wa0, wa1); NEXTO(j + 1);
  }
  o_unit<NQT, NNT, false, false>(acc, of, sb, sa, 0, 0, 0);
  asm volatile("s_nop 7\n\ts_nop 1" ::: "memory"); SBAR();
  const unsigned lane = lane_id();
  if (nb == kq) {
    unsigned ra = OPART + q0 * 5120 + lane * 16; PIN(ra);
#pragma unroll
    for (int q = 0; q < NQT; ++q)
#pragma unroll
      for (int n = 0; n < NNT; ++n) *(LAS f32x4*)(ra + (q * 5 + n) * 1024) = acc[q][n];
  } else {
    const __amdgpu_buffer_rsrc_t srdS = __builtin_amdgcn_make_buffer_rsrc((void*)(wsb + WS_X + (size_t)(blk * 4 + nb) * 46080), (short)0, 46080, 0x00020000);
#pragma unroll
    for (int q = 0; q < NQT; ++q)
#pragma unroll
      for (int u = 0; u < (NNT + 1) / 2; ++u) {
        const f32x4 a = acc[q][2 * u], c2 = acc[q][2 * u + 1 < NNT ? 2 * u + 1 : 2 * u];
        __builtin_amdgcn_raw_buffer_store_b128(u32x4{cvtpk(a[0], a[1]), cvtpk(a[2], a[3]), cvtpk(c2[0], c2[1]), cvtpk(c2[2], c2[3])}, srdS, lane * 16, ((q0 + q) * 3 + u) * 1024, 16);
      }
    WAITV(0);
  }
}
DI void oproj_finish(u16* __restrict__ outp, char* __restrict__ wsb, unsigned q, unsigned kq, unsigned blk) {
  const unsigned lane = lane_id(), li = lane & 15, g = lane >> 4, nnt = 5 - (kq & 1);
  BAR();
  unsigned* fl = (unsigned*)(wsb + WS_F);
  if (q == 8 && lane == 0) STA(fl + blk * 64 + 3, 1u);
  { const unsigned* pf = fl + ((blk & ~48u) + 16 * (lane & 3)) * 64 + 3;
    for (int spin = 0; spin < SPIN_CAP && !__all(LDA(pf) == 1u); ++spin) __builtin_amdgcn_s_sleep(1);
    __builtin_amdgcn_fence(__ATOMIC_ACQUIRE, "wavefront"); }
  f32x4 px[3][5];
  u32x4 pw[3][3];
#pragma unroll
  for (int p = 0; p < 3; ++p) {
    const unsigned pb = (blk & ~48u) + 16 * ((kq + 1 + p) & 3);
    const __amdgpu_buffer_rsrc_t srdP = __builtin_amdgcn_make_buffer_rsrc((void*)(wsb + WS_X + (size_t)(pb * 4 + kq) * 46080), (short)0, 46080, 0x00020000);
#pragma unroll
    for (int u = 0; u < 3; ++u) pw[p][u] = __builtin_amdgcn_raw_buffer_load_b128(srdP, lane * 16, (q * 3 + (u < 2 ? u : nnt - 3)) * 1024, 16);
  }
  SBAR();
#pragma unroll
  for (int p = 0; p < 3; ++p)
#pragma unroll
    for (int n = 0; n < 5; ++n) { const unsigned lo = pw[p][n >> 1][2 * (n & 1)], hi = pw[p][n >> 1][2 * (n & 1) + 1];
      px[p][n] = f32x4{__uint_as_float(lo << 16), __uint_as_float(lo & 0xffff0000u), __uint_as_float(hi << 16), __uint_as_float(hi & 0xffff0000u)}; }
  const unsigned tb = OPART + q * 5120;
  f32x4 own[5];
#pragma unroll
  for (int n = 0; n < 5; ++n) own[n] = *(const LAS f32x4*)(tb + (n < 4 ? n : nnt - 1) * 1024 + lane * 16);
#pragma unroll
  for (int n = 0; n < 5; ++n) {
    const f32x4 a = (own[n] + px[0][n]) + (px[1][n] + px[2][n]);
    *(LAS u32x2*)(tb + li * 176 + g * 8 + n * 32) = u32x2{cvtpk(a[0], a[1]), cvtpk(a[2], a[3])};
  }
#pragma unroll
  for (unsigned i = 0; i < 3; ++i) {
    const unsigned ch = i * 64 + lane, row = ch / 10, cc = ch - row * 10;
    if (row < 16 && cc < 2 * nnt) {
      *(u32x4*)(outp + (size_t)(16 * q + row) * (2 * DM) + cc * 8) = *(const LAS u32x4*)(tb + row * 176 + cc * 16);
    }
  }
}

__global__ __launch_bounds__(768, 3) void k_fused(const u16* __restrict__ xA, const u16* __restrict__ kA, const u16* __restrict__ vA, const u16* __restrict__ wqA, const u16* __restrict__ woA,
                                                  const u16* __restrict__ xB, const u16* __restrict__ kB, const u16* __restrict__ vB, const u16* __restrict__ wqB, const u16* __restrict__ woB,
                                                  char* __restrict__ wsb, u16* __restrict__ out) {
  extern __shared__ __attribute__((aligned(16))) char smem[];
  unsigned long long pc0; asm volatile("s_getpc_b64 %0" : "=s"(pc0));
  const unsigned wid12 = rfl(threadIdx.x >> 6), half = wid12 >= 6, wid = wid12 - 6 * half, lb = half * GRP;
  const unsigned blk = blockIdx.x;
  const unsigned idx = blk >> 3, pair = (blk & 7) * 4 + (idx >> 3), m = idx & 7, br = pair >> 4, b = pair & 15;
  const unsigned hg0 = (blk + half) & 3, kq = m >> 1, g0 = ((blk ^ kq) & 1) ? (blk + 1) & 3 : blk & 3;
  char* op = wsb + WS_O + (size_t)pair * NH * ITEM_B;
  const u16* kb = (br ? kB : kA) + (size_t)b * NH * SK * DH; const u16* vb = (br ? vB : vA) + (size_t)b * NH * SK * DH;
  Hs hs;
  if (wid < 4) {
    att_wave<2>(op, kb, vb, 2 * wid, m, hg0, lb, hs, nullptr);
    const unsigned t = half ? 3 - ((wid + 2) & 3) : wid, nb = 2 * half + (t & 1);
    if (t == 0) oproj_consumer<5, 5>(wsb, nb, kq, blk);
    else if (t == 1) oproj_consumer<5, 4>(wsb, nb, kq, blk);
    else if (t == 2) oproj_consumer<4, 5>(wsb, nb, kq, blk);
    else oproj_consumer<4, 4>(wsb, nb, kq, blk);
  } else if (wid == 4) {
    if (!half) hs_entry(hs, wsb, blk);
    else {
      const __amdgpu_buffer_rsrc_t srdT = __builtin_amdgcn_make_buffer_rsrc((char*)((br ? xB : xA) + (size_t)b * SQ * DM) + m * 20736, (short)0, 20736, 0x00020000);
      __builtin_amdgcn_s_sleep(10);
      {
        const unsigned l = lane_id(), n = idx * 17 + (l < 17 ? l : 0);
        asm volatile("s_mov_b32 m0, %0\n\ts_nop 3\n\tglobal_load_lds_dword %1, %2" :: "s"(vslot(3)), "v"(n * 128), "s"(pc0) : "memory"); }
#pragma unroll
      for (int p = 0; p < 3; ++p) { const unsigned n = p * 64 + lane_id(); dma4(vslot(3), (n < 162 ? n : 0) * 128, srdT); }
      {
        constexpr unsigned LN = (576 - 256) * 64 / 128;
        const __amdgpu_buffer_rsrc_t srdQ = __builtin_amdgcn_make_buffer_rsrc((char*)((blk & 1) ? wqB : wqA) + (size_t)256 * 8192 + (size_t)(blk >> 1) * LN * 128, (short)0, LN * 128, 0x00020000);
#pragma unroll
        for (unsigned p = 0; p < (LN + 63) / 64; ++p) { const unsigned n = p * 64 + lane_id(); dma4(vslot(3), (n < LN ? n : 0) * 128, srdQ); } }
    }
    att_wave<1>(op, kb, vb, 8, m, hg0, lb, hs, (br ? xB : xA) + (size_t)b * SQ * DM);
    if (half) {
      OLd ol; ol.init(op, br ? woB : woA, m, g0, 1, pair & 3);
      WAITV(0);
      ol.run(1);
    } else {
      WAITV(0);
      RBAR(); RBAR(); RBAR();
      if (lane_id() == 0) STA(hs.mine + 2, 1u);
#pragma unroll 1
      for (int j = 3; j < 8; ++j) RBAR();
      hs_post(hs.peer + 2, OBOX);
      RBAR(); RBAR();
      hs_wait_flag(hs, hs_take(OBOX), 2);
      RBAR(); RBAR(); RBAR();
      WAITV(0);
    }
    return;
  } else {
    OLd ol;
    loader(br ? wqB : wqA, (br ? xB : xA) + (size_t)b * SQ * DM, kb, vb, m, hg0, half, ol, op, br ? woB : woA, g0, pair & 3);
    ol.run(0);
    if (half) return;
  }
  oproj_finish(out + (size_t)b * SQ * (2 * DM) + br * DM + (m & 1) * 288 + 16 * ((9 * kq + 1) >> 1), wsb, wid < 4 ? wid + 4 * half : 8, kq, blk);
}

extern "C" void kernel_launch(void* const* d_in, const int* in_sizes, int n_in, void* d_out, int out_size, void* d_ws, size_t ws_size, hipStream_t stream) {
  (void)hipFuncSetAttribute((const void*)k_fused, hipFuncAttributeMaxDynamicSharedMemorySize, LDS_BYTES);
  k_fused<<<256, 768, LDS_BYTES, stream>>>((const u16*)d_in[0], (const u16*)d_in[1], (const u16*)d_in[2], (const u16*)d_in[3], (const u16*)d_in[4],
                                           (const u16*)d_in[5], (const u16*)d_in[6], (const u16*)d_in[7], (const u16*)d_in[8], (const u16*)d_in[9], (char*)d_ws, (u16*)d_out);
}
```
